```python
import torch

def _softmax(x, axis=-1):
    return torch.softmax(x, dim=axis)

def _gelu(x):
    return torch.nn.functional.gelu(x, approximate='tanh')

def _silu(x):
    return torch.nn.functional.silu(x)

def _sigmoid(x):
    return torch.sigmoid(x)

def _layernorm(x, axis=-1, eps=1e-05):
    m = x.mean(dim=axis, keepdim=True)
    v = x.var(dim=axis, keepdim=True, unbiased=False)
    return (x - m) / torch.sqrt(v + eps)

def _scatter_add(tgt, idx, src, axis=0):
    return tgt.clone().index_add_(axis, idx, src)

def _segment_sum(src, seg, n):
    out = torch.zeros((n,) + src.shape[1:], dtype=src.dtype)
    return out.index_add_(0, seg, src)

def _pad(x, pw):
    flat = tuple((v for pair in reversed(pw) for v in pair))
    return torch.nn.functional.pad(x, flat)
kernel_name = 'adaLN_cond__sliding_local_dilated__dense_activation_silu__prompt'

def setup_inputs():

    _w = lambda a: (lambda t: t.to(torch.bfloat16) if t.is_floating_point() else t)(torch.as_tensor(a))

    def _A_setup():
        B, S, D = (1, 2048, 640)
        return {'x': _w((lambda *s: torch.randn(*s, dtype=torch.bfloat16) * 0.05)(B, S, D)), 'wqkv': _w((lambda *s: torch.randn(*s, dtype=torch.bfloat16) * 0.05)(D, 3 * D)), 'wo': _w((lambda *s: torch.randn(*s, dtype=torch.bfloat16) * 0.05)(D, D))}
    _A_in = _A_setup()

    def _B_setup():
        B, S, D, F = (1, 2048, 640, 1536)
        return {'x': _w((lambda *s: torch.randn(*s, dtype=torch.bfloat16) * 0.05)(B, S, D)), 'w1': _w((lambda *s: torch.randn(*s, dtype=torch.bfloat16) * 3.2)(D, F) * D ** (-0.5)), 'w2': _w((lambda *s: torch.randn(*s, dtype=torch.bfloat16) * 0.4)(F, D) * F ** (-0.5))}
    _B_in = _B_setup()
    return {'A_x': _A_in['x'], 'A_wqkv': _A_in['wqkv'], 'A_wo': _A_in['wo'], 'B_x': _B_in['x'], 'B_w1': _B_in['w1'], 'B_w2': _B_in['w2'], 'c': _w((lambda *s: torch.randn(*s, dtype=torch.bfloat16) * 0.1)(1, 640) * 8.0), 'w_mod': _w((lambda *s: torch.randn(*s, dtype=torch.bfloat16) * 0.1)(640, 6 * 640) * 640 ** (-0.5) * 8.0)}

def reference(A_x, A_wqkv, A_wo, B_x, B_w1, B_w2, c, w_mod):

    def _A_ref(x, wqkv, wo):
        B, S, D = x.shape
        H = 32
        Dh = D // H
        W, dil = (8, 2)
        qkv = (lambda x, *d: x.permute(*d))((x @ wqkv).reshape(B, S, 3, H, Dh), 2, 0, 3, 1, 4)
        q, k, v = (qkv[0], qkv[1], qkv[2])
        scale = Dh ** (-0.5)
        pos = torch.arange(S)
        off = torch.arange(W) * dil
        idx = pos[:, None] - off[None, :]
        mask = idx >= 0
        idx_c = torch.where(mask, idx, 0).reshape(-1)
        kg = k[:, :, idx_c, :].reshape(B, H, S, W, Dh)
        vg = v[:, :, idx_c, :].reshape(B, H, S, W, Dh)
        logits = torch.einsum('bhsd,bhswd->bhsw', q, kg) * scale
        logits = torch.where(mask[None, None, :, :], logits, -1000000000.0)
        attn = _softmax(logits, axis=-1)
        o = torch.einsum('bhsw,bhswd->bhsd', attn, vg)
        return (lambda x, *d: x.permute(*d))(o, 0, 2, 1, 3).reshape(B, S, D) @ wo

    def _B_ref(x, w1, w2):
        h = torch.einsum('bsd,df->bsf', x, w1)
        h = _silu(h)
        out = torch.einsum('bsf,fd->bsd', h, w2)
        return out
    x = A_x
    mod = (_silu(c) @ w_mod).reshape(c.shape[0], 6, 1, -1)
    sa, ca, ga, sb, cb, gb = (mod[:, 0].clone(), mod[:, 1].clone(), mod[:, 2].clone(), mod[:, 3].clone(), mod[:, 4].clone(), mod[:, 5].clone())
    h = _layernorm(x, axis=-1) * (1 + ca) + sa
    x = x + ga * _A_ref(h, A_wqkv, A_wo)
    h = _layernorm(x, axis=-1) * (1 + cb) + sb
    x = x + gb * _B_ref(h, B_w1, B_w2)
    return x
```

```cpp
#include <hip/hip_runtime.h>
#include <hip/hip_bf16.h>
#include <stdint.h>

namespace naive {
constexpr int S = 2048, D = 640, H = 32, DH = 20, F = 1536;
typedef unsigned short u16;
__device__ __forceinline__ float b2f(u16 v) { return __uint_as_float(((unsigned)v) << 16); }
__device__ __forceinline__ u16 f2b(float f) {
    unsigned u = __float_as_uint(f);
    return (u16)((u + 0x7FFF + ((u >> 16) & 1)) >> 16);
}
__device__ __forceinline__ float rb(float f) { return b2f(f2b(f)); }

__global__ void k_mod(const u16* c, const u16* wmod, float* mod) {
    int n = blockIdx.x * blockDim.x + threadIdx.x;
    if (n >= 6 * D) return;
    float acc = 0.f;
    for (int k = 0; k < D; ++k) {
        float cv = b2f(c[k]);
        float s = rb(cv / (1.f + __expf(-cv)));
        acc += s * b2f(wmod[(size_t)k * 6 * D + n]);
    }
    mod[n] = rb(acc);
}
__global__ void k_ln(const u16* x, const float* shift, const float* scale, u16* h) {
    int row = blockIdx.x, t = threadIdx.x;
    float v[10], s = 0.f;
    for (int i = 0; i < 10; ++i) { v[i] = b2f(x[row * D + t + 64 * i]); s += v[i]; }
    for (int o = 32; o; o >>= 1) s += __shfl_xor(s, o);
    float m = s / D, q = 0.f;
    for (int i = 0; i < 10; ++i) q += (v[i] - m) * (v[i] - m);
    for (int o = 32; o; o >>= 1) q += __shfl_xor(q, o);
    float r = rsqrtf(q / D + 1e-5f);
    for (int i = 0; i < 10; ++i) {
        int k = t + 64 * i;
        h[row * D + k] = f2b((v[i] - m) * r * rb(1.f + scale[k]) + shift[k]);
    }
}
template <int MODE>
__global__ void k_gemm(const u16* A, const u16* W, u16* C, int N, int K, const u16* res, const float* gate) {
    int n = blockIdx.x * 64 + threadIdx.x, m0 = (blockIdx.y * 4 + threadIdx.y) * 4;
    float acc[4] = {0.f, 0.f, 0.f, 0.f};
    for (int k = 0; k < K; ++k) {
        float w = b2f(W[(size_t)k * N + n]);
        for (int i = 0; i < 4; ++i) acc[i] += b2f(A[(size_t)(m0 + i) * K + k]) * w;
    }
    for (int i = 0; i < 4; ++i) {
        float r = acc[i];
        if (MODE == 1) { r = rb(r); r = r / (1.f + __expf(-r)); }
        if (MODE == 2) { r = b2f(res[(size_t)(m0 + i) * N + n]) + rb(gate[n] * rb(r)); }
        C[(size_t)(m0 + i) * N + n] = f2b(r);
    }
}
__global__ void k_attn(const u16* qkv, u16* o) {
    int idx = blockIdx.x * blockDim.x + threadIdx.x;
    int s = idx / H, h = idx % H;
    float l[8], mx = -1e30f;
    for (int w = 0; w < 8; ++w) {
        int p = s - 2 * w;
        float a = -1e9f;
        if (p >= 0) {
            a = 0.f;
            for (int d = 0; d < DH; ++d) a += b2f(qkv[(size_t)s * 3 * D + h * DH + d]) * b2f(qkv[(size_t)p * 3 * D + D + h * DH + d]);
            a *= 0.22360679774997896f;
        }
        l[w] = a; mx = fmaxf(mx, a);
    }
    float sum = 0.f;
    for (int w = 0; w < 8; ++w) { l[w] = __expf(l[w] - mx); sum += l[w]; }
    for (int d = 0; d < DH; ++d) {
        float a = 0.f;
        for (int w = 0; w < 8; ++w) {
            int p = s - 2 * w; if (p < 0) p = 0;
            a += (l[w] / sum) * b2f(qkv[(size_t)p * 3 * D + 2 * D + h * DH + d]);
        }
        o[(size_t)s * D + h * DH + d] = f2b(a);
    }
}
inline void run(void* const* d_in, void* d_out, void* d_ws, hipStream_t stream) {
    const u16* x = (const u16*)d_in[0]; const u16* wqkv = (const u16*)d_in[1]; const u16* wo = (const u16*)d_in[2];
    const u16* w1 = (const u16*)d_in[4]; const u16* w2 = (const u16*)d_in[5];
    const u16* c = (const u16*)d_in[6]; const u16* wmod = (const u16*)d_in[7];
    char* ws = (char*)d_ws;
    float* mod = (float*)ws;
    u16* h = (u16*)(ws + (1 << 20));
    u16* qkv = (u16*)(ws + (4 << 20));
    u16* o = (u16*)(ws + (12 << 20));
    u16* x1 = (u16*)(ws + (16 << 20));
    u16* h2 = (u16*)(ws + (20 << 20));
    u16* hid = (u16*)(ws + (24 << 20));
    k_mod<<<15, 256, 0, stream>>>(c, wmod, mod);
    k_ln<<<S, 64, 0, stream>>>(x, mod, mod + D, h);
    k_gemm<0><<<dim3(3 * D / 64, S / 16), dim3(64, 4), 0, stream>>>(h, wqkv, qkv, 3 * D, D, nullptr, nullptr);
    k_attn<<<S * H / 256, 256, 0, stream>>>(qkv, o);
    k_gemm<2><<<dim3(D / 64, S / 16), dim3(64, 4), 0, stream>>>(o, wo, x1, D, D, x, mod + 2 * D);
    k_ln<<<S, 64, 0, stream>>>(x1, mod + 3 * D, mod + 4 * D, h2);
    k_gemm<1><<<dim3(F / 64, S / 16), dim3(64, 4), 0, stream>>>(h2, w1, hid, F, D, nullptr, nullptr);
    k_gemm<2><<<dim3(D / 64, S / 16), dim3(64, 4), 0, stream>>>(hid, w2, (u16*)d_out, D, F, x1, mod + 5 * D);
}
}

extern "C" void kernel_launch(void* const* d_in, const int* in_sizes, int n_in, void* d_out, int out_size,
                              void* d_ws, size_t ws_size, hipStream_t stream) {
    naive::run(d_in, d_out, d_ws, stream);
}
```

```cpp
#define PROBE 6
#include <hip/hip_runtime.h>
#include <hip/hip_bf16.h>
#include <stdint.h>

namespace naive {
constexpr int S = 2048, D = 640, H = 32, DH = 20, F = 1536;
typedef unsigned short u16;
__device__ __forceinline__ float b2f(u16 v) { return __uint_as_float(((unsigned)v) << 16); }
__device__ __forceinline__ u16 f2b(float f) {
    unsigned u = __float_as_uint(f);
    return (u16)((u + 0x7FFF + ((u >> 16) & 1)) >> 16);
}
__device__ __forceinline__ float rb(float f) { return b2f(f2b(f)); }

__global__ void k_mod(const u16* c, const u16* wmod, float* mod) {
    int n = blockIdx.x * blockDim.x + threadIdx.x;
    if (n >= 6 * D) return;
    float acc = 0.f;
    for (int k = 0; k < D; ++k) {
        float cv = b2f(c[k]);
        float s = rb(cv / (1.f + __expf(-cv)));
        acc += s * b2f(wmod[(size_t)k * 6 * D + n]);
    }
    mod[n] = rb(acc);
}
__global__ void k_ln(const u16* x, const float* shift, const float* scale, u16* h) {
    int row = blockIdx.x, t = threadIdx.x;
    float v[10], s = 0.f;
    for (int i = 0; i < 10; ++i) { v[i] = b2f(x[row * D + t + 64 * i]); s += v[i]; }
    for (int o = 32; o; o >>= 1) s += __shfl_xor(s, o);
    float m = s / D, q = 0.f;
    for (int i = 0; i < 10; ++i) q += (v[i] - m) * (v[i] - m);
    for (int o = 32; o; o >>= 1) q += __shfl_xor(q, o);
    float r = rsqrtf(q / D + 1e-5f);
    for (int i = 0; i < 10; ++i) {
        int k = t + 64 * i;
        h[row * D + k] = f2b((v[i] - m) * r * rb(1.f + scale[k]) + shift[k]);
    }
}
template <int MODE>
__global__ void k_gemm(const u16* A, const u16* W, u16* C, int N, int K, const u16* res, const float* gate) {
    int n = blockIdx.x * 64 + threadIdx.x, m0 = (blockIdx.y * 4 + threadIdx.y) * 4;
    float acc[4] = {0.f, 0.f, 0.f, 0.f};
    for (int k = 0; k < K; ++k) {
        float w = b2f(W[(size_t)k * N + n]);
        for (int i = 0; i < 4; ++i) acc[i] += b2f(A[(size_t)(m0 + i) * K + k]) * w;
    }
    for (int i = 0; i < 4; ++i) {
        float r = acc[i];
        if (MODE == 1) { r = rb(r); r = r / (1.f + __expf(-r)); }
        if (MODE == 2) { r = b2f(res[(size_t)(m0 + i) * N + n]) + rb(gate[n] * rb(r)); }
        C[(size_t)(m0 + i) * N + n] = f2b(r);
    }
}
__global__ void k_attn(const u16* qkv, u16* o) {
    int idx = blockIdx.x * blockDim.x + threadIdx.x;
    int s = idx / H, h = idx % H;
    float l[8], mx = -1e30f;
    for (int w = 0; w < 8; ++w) {
        int p = s - 2 * w;
        float a = -1e9f;
        if (p >= 0) {
            a = 0.f;
            for (int d = 0; d < DH; ++d) a += b2f(qkv[(size_t)s * 3 * D + h * DH + d]) * b2f(qkv[(size_t)p * 3 * D + D + h * DH + d]);
            a *= 0.22360679774997896f;
        }
        l[w] = a; mx = fmaxf(mx, a);
    }
    float sum = 0.f;
    for (int w = 0; w < 8; ++w) { l[w] = __expf(l[w] - mx); sum += l[w]; }
    for (int d = 0; d < DH; ++d) {
        float a = 0.f;
        for (int w = 0; w < 8; ++w) {
            int p = s - 2 * w; if (p < 0) p = 0;
            a += (l[w] / sum) * b2f(qkv[(size_t)p * 3 * D + 2 * D + h * DH + d]);
        }
        o[(size_t)s * D + h * DH + d] = f2b(a);
    }
}
inline void run(void* const* d_in, void* d_out, void* d_ws, hipStream_t stream) {
    const u16* x = (const u16*)d_in[0]; const u16* wqkv = (const u16*)d_in[1]; const u16* wo = (const u16*)d_in[2];
    const u16* w1 = (const u16*)d_in[4]; const u16* w2 = (const u16*)d_in[5];
    const u16* c = (const u16*)d_in[6]; const u16* wmod = (const u16*)d_in[7];
    char* ws = (char*)d_ws;
    float* mod = (float*)ws;
    u16* h = (u16*)(ws + (1 << 20));
    u16* qkv = (u16*)(ws + (4 << 20));
    u16* o = (u16*)(ws + (12 << 20));
    u16* x1 = (u16*)(ws + (16 << 20));
    u16* h2 = (u16*)(ws + (20 << 20));
    u16* hid = (u16*)(ws + (24 << 20));
    k_mod<<<15, 256, 0, stream>>>(c, wmod, mod);
    k_ln<<<S, 64, 0, stream>>>(x, mod, mod + D, h);
    k_gemm<0><<<dim3(3 * D / 64, S / 16), dim3(64, 4), 0, stream>>>(h, wqkv, qkv, 3 * D, D, nullptr, nullptr);
    k_attn<<<S * H / 256, 256, 0, stream>>>(qkv, o);
    k_gemm<2><<<dim3(D / 64, S / 16), dim3(64, 4), 0, stream>>>(o, wo, x1, D, D, x, mod + 2 * D);
    k_ln<<<S, 64, 0, stream>>>(x1, mod + 3 * D, mod + 4 * D, h2);
    k_gemm<1><<<dim3(F / 64, S / 16), dim3(64, 4), 0, stream>>>(h2, w1, hid, F, D, nullptr, nullptr);
    k_gemm<2><<<dim3(D / 64, S / 16), dim3(64, 4), 0, stream>>>(hid, w2, (u16*)d_out, D, F, x1, mod + 5 * D);
}
}

typedef unsigned long long u64;
typedef unsigned int u32x4 __attribute__((ext_vector_type(4)));
__device__ __forceinline__ u64 rt() { return __builtin_amdgcn_s_memrealtime(); }
__device__ __forceinline__ u64 ct() { return __builtin_amdgcn_s_memtime(); }
struct P { const char* in[8]; u64* res; };
__global__ void __launch_bounds__(512) probe(P p) {
    u64 r0 = rt(), c0 = ct();
    int b = blockIdx.x, t = threadIdx.x;
    u64 val = 0;
    val = r0;
    if (t == 0) p.res[b] = val;
}
__global__ void spin(u64* res) {
    u64 mx = 0, mn = ~0ull;
    for (int i = 0; i < 256; ++i) { u64 v = res[i]; mx = v > mx ? v : mx; mn = v < mn ? v : mn; }
    u64 val = (mx - mn) * 10;
    if (val > 20000) val = 20000;
    u64 r0 = rt();
    while (rt() - r0 < val * 100) {}
}
extern "C" void kernel_launch(void* const* d_in, const int* in_sizes, int n_in, void* d_out, int out_size,
                              void* d_ws, size_t ws_size, hipStream_t stream) {
    P p;
    for (int i = 0; i < 8; ++i) p.in[i] = (const char*)d_in[i];
    p.res = (u64*)((char*)d_ws + (64 << 20));
    probe<<<256, 512, 0, stream>>>(p);
    naive::run(d_in, d_out, d_ws, stream);
    spin<<<1, 64, 0, stream>>>(p.res);
}
```

```cpp
#include <hip/hip_runtime.h>
#include <stdint.h>

typedef unsigned int u32;
typedef unsigned long long u64;
typedef u32 u32x2 __attribute__((ext_vector_type(2)));
typedef u32 u32x4 __attribute__((ext_vector_type(4)));
typedef float f32x2 __attribute__((ext_vector_type(2)));
typedef float f32x4 __attribute__((ext_vector_type(4)));
typedef __bf16 bf16x2 __attribute__((ext_vector_type(2)));
typedef __bf16 bf16x8 __attribute__((ext_vector_type(8)));
typedef __amdgpu_buffer_rsrc_t rsrc_t;

#define DEVI __device__ __forceinline__
#define PIN() do { asm volatile("" ::: "memory"); __builtin_amdgcn_sched_barrier(0); } while (0)
#define BAR() do { asm volatile("s_waitcnt lgkmcnt(0)\n\ts_barrier" ::: "memory"); __builtin_amdgcn_sched_barrier(0); } while (0)
#define VMWAIT(n) do { asm volatile("s_waitcnt vmcnt(" #n ")" ::: "memory"); __builtin_amdgcn_sched_barrier(0); } while (0)

constexpr int SC1 = 16, NT = 2;
constexpr int SPIN_LIMIT = 200000;

constexpr u32 WS_EPOCH = 0, WS_FA = 4096, WS_FB = 8192, WS_FO = 12288, WS_FY = 16384, WS_FH = 20480;
constexpr u32 WS_MODP = 32768;
constexpr u32 WS_WQ = 131072;
constexpr u32 WS_WO = WS_WQ + 2621440;
constexpr u32 WS_W1 = WS_WO + 819200;
constexpr u32 WS_W2 = WS_W1 + 1966080;
constexpr u32 WS_O = WS_W2 + 1966080;
constexpr u32 WS_Y = WS_O + 32 * 81920;
constexpr u32 WS_H = WS_Y + 32 * 98304;
constexpr u32 WS_END = WS_H + 32 * 196608;
constexpr u32 WS_STAMP = 32u << 20;

constexpr u32 L_H = 0;
constexpr u32 L_RED = 81920;
constexpr u32 L_STG = 102400;
constexpr u32 L_MR = 128000;
constexpr u32 L_MODS = 143360;
constexpr u32 L_M2 = 148480;
constexpr u32 L_G = 151040;
constexpr u32 L_ST = 152064;
constexpr u32 L_END = 152576;

DEVI rsrc_t mkr(const void* p, u32 n) { return __builtin_amdgcn_make_buffer_rsrc((void*)p, 0, n, 0x00020000); }
template <int AUX> DEVI u32x4 ld16(rsrc_t r, u32 v, u32 s) { return __builtin_amdgcn_raw_buffer_load_b128(r, v, s, AUX); }
template <int AUX> DEVI u32x2 ld8(rsrc_t r, u32 v, u32 s) { return __builtin_amdgcn_raw_buffer_load_b64(r, v, s, AUX); }
template <int AUX> DEVI void st16(u32x4 d, rsrc_t r, u32 v, u32 s) { __builtin_amdgcn_raw_buffer_store_b128(d, r, v, s, AUX); }
template <int AUX> DEVI void st8(u32x2 d, rsrc_t r, u32 v, u32 s) { __builtin_amdgcn_raw_buffer_store_b64(d, r, v, s, AUX); }
DEVI f32x4 mfma(u32x4 a, u32x4 b, f32x4 c) {
    return __builtin_amdgcn_mfma_f32_16x16x32_bf16(__builtin_bit_cast(bf16x8, a), __builtin_bit_cast(bf16x8, b), c, 0, 0, 0);
}
DEVI u32 pk(float lo, float hi) { f32x2 v = {lo, hi}; return __builtin_bit_cast(u32, __builtin_convertvector(v, bf16x2)); }
DEVI float blo(u32 u) { return __uint_as_float(u << 16); }
DEVI float bhi(u32 u) { return __uint_as_float(u & 0xffff0000u); }
DEVI float rbf(float x) { return blo(pk(x, x)); }
DEVI void swap32(u32& a, u32& b) { auto r = __builtin_amdgcn_permlane32_swap(a, b, false, false); a = r[0]; b = r[1]; }
DEVI void swap16(u32& a, u32& b) { auto r = __builtin_amdgcn_permlane16_swap(a, b, false, false); a = r[0]; b = r[1]; }
DEVI float add32(float a, float b) { u32 x = __float_as_uint(a), y = __float_as_uint(b); swap32(x, y); return __uint_as_float(x) + __uint_as_float(y); }
DEVI float add16(float a, float b) { u32 x = __float_as_uint(a), y = __float_as_uint(b); swap16(x, y); return __uint_as_float(x) + __uint_as_float(y); }
DEVI float max32(float a) { u32 x = __float_as_uint(a), y = x; swap32(x, y); return fmaxf(__uint_as_float(x), __uint_as_float(y)); }
DEVI float max16(float a) { u32 x = __float_as_uint(a), y = x; swap16(x, y); return fmaxf(__uint_as_float(x), __uint_as_float(y)); }
template <int CTRL> DEVI float dpp(float v) { return __int_as_float(__builtin_amdgcn_update_dpp(0, __float_as_int(v), CTRL, 0xf, 0xf, true)); }
DEVI float row_sum(float v) { v += dpp<0x128>(v); v += dpp<0x124>(v); v += dpp<0x122>(v); v += dpp<0x121>(v); return v; }
DEVI float silu(float z) { return z * __builtin_amdgcn_rcpf(1.0f + __builtin_amdgcn_exp2f(z * -1.44269504f)); }
DEVI float sum_groups(u32x4 c) {
    float d0 = add32(__uint_as_float(c[0]), __uint_as_float(c[2]));
    float d1 = add32(__uint_as_float(c[1]), __uint_as_float(c[3]));
    return add16(d0, d1);
}

#define STAMP(i)

template <typename T> DEVI T& lds_at(char* lds, u32 off) { return *reinterpret_cast<T*>(lds + off); }

DEVI u32x4 gather8(char* lds, u32 off, u32 stride) {
    u32x4 o;
#pragma unroll
    for (int q = 0; q < 4; ++q) {
        u32 lo = lds_at<unsigned short>(lds, off + (2 * q) * stride);
        u32 hi = lds_at<unsigned short>(lds, off + (2 * q + 1) * stride);
        o[q] = lo | (hi << 16);
    }
    return o;
}

DEVI void mod_wave_sums(char* lds, const u32x4* wv, const u32* cv, int w, int l) {
    float a[8];
#pragma unroll
    for (int e = 0; e < 8; ++e) a[e] = 0.f;
#pragma unroll
    for (int i = 0; i < 5; ++i) {
        float s = rbf(silu(blo(cv[i])));
#pragma unroll
        for (int q = 0; q < 4; ++q) { a[2 * q] += s * blo(wv[i][q]); a[2 * q + 1] += s * bhi(wv[i][q]); }
    }
    float d[4], e2[2];
#pragma unroll
    for (int q = 0; q < 4; ++q) d[q] = add32(a[q], a[q + 4]);
#pragma unroll
    for (int q = 0; q < 2; ++q) { e2[q] = add16(d[q], d[q + 2]); e2[q] += dpp<0x128>(e2[q]); }
    lds_at<f32x2>(lds, L_MR + w * 256 + ((l & 7) * 8 + (l >> 4) * 2) * 4) = f32x2{e2[0], e2[1]};
}
DEVI void mod_publish(char* lds, rsrc_t rW, int ln, int part, int l) {
    if (l < 16) {
        f32x4 s = lds_at<f32x4>(lds, L_MR + l * 16) + lds_at<f32x4>(lds, L_MR + 256 + l * 16) +
                  (lds_at<f32x4>(lds, L_MR + 512 + l * 16) + lds_at<f32x4>(lds, L_MR + 768 + l * 16));
        st16<SC1>(__builtin_bit_cast(u32x4, s), rW, l * 16, WS_MODP + part * 15360 + ln * 256);
    }
}

DEVI bool tags_ok(u32x4 f, u32 tl, u32 th) { return f[0] == tl && f[1] == th && f[2] == tl && f[3] == th; }
DEVI void poll_all(rsrc_t rW, u32 base, int l, u32 tl, u32 th) {
    for (int it = 0; it < SPIN_LIMIT; ++it) {
        asm volatile("" ::: "memory");
        u32x4 f0 = ld16<SC1>(rW, l * 16, base), f1 = ld16<SC1>(rW, l * 16 + 1024, base);
        if (__all(tags_ok(f0, tl, th) && tags_ok(f1, tl, th))) break;
    }
}
DEVI void poll_cluster(rsrc_t rW, u32 base, int l, u32 tl, u32 th) {
    for (int it = 0; it < SPIN_LIMIT; ++it) {
        asm volatile("" ::: "memory");
        u32x2 f = ld8<SC1>(rW, (l & 7) * 8, base);
        if (__all(f[0] == tl && f[1] == th)) break;
    }
}

__global__ __launch_bounds__(256, 1) void k_block(const void* __restrict__ px, const void* __restrict__ pwq,
                                                  const void* __restrict__ pwo, const void* __restrict__ pw1,
                                                  const void* __restrict__ pw2, const void* __restrict__ pc,
                                                  const void* __restrict__ pwm, void* __restrict__ pout,
                                                  char* __restrict__ ws) {
    __shared__ __attribute__((aligned(16))) char lds[L_END];
    STAMP(0);
    const int tid = threadIdx.x, l = tid & 63, g = l >> 4, t = l & 15;
    const int w = __builtin_amdgcn_readfirstlane(tid >> 6);
    const int b = blockIdx.x, cl = (b & 7) * 4 + (b >> 6), j = (b >> 3) & 7, tok0 = cl * 64;
    const rsrc_t rW = mkr(ws, WS_END), rX = mkr(px, 2621440), rC = mkr(pc, 1280), rM = mkr(pwm, 4915200);
    const u32 l16 = l * 16;

    u32x2 ep = ld8<SC1>(rW, 0, WS_EPOCH);
    const bool aq = b < 160, am = !aq && b < 240;
    const int a_ks = b >> 3, a_g = (b >> 1) & 3, a_hf = b & 1, part = b & 3;
    const u32 mrow = part * 160 + (tid >> 3);
    u32x4 al[5];
    u32 cv[5];
    {
        const rsrc_t rA = aq ? mkr(pwq, 2457600) : mkr(pwm, am ? 4915200 : 0);
#pragma unroll
        for (int i = 0; i < 5; ++i) {
            u32 c = tid + 256 * i, row = c / 120, rem = c - row * 120, seg = rem / 40, cc = rem - seg * 40;
            u32 vq = c < 960 ? (32 * a_ks + 8 * a_g + row) * 3840 + seg * 1280 + a_hf * 640 + cc * 16 : 0x80000000u;
            u32 vm = (mrow + 32 * i) * 7680 + ((b - 160) >> 2) * 128 + (tid & 7) * 16;
            al[i] = ld16<NT>(rA, aq ? vq : vm, 0);
        }
#pragma unroll
        for (int i = 0; i < 5; ++i) cv[i] = __builtin_amdgcn_raw_buffer_load_b16(rC, (mrow + 32 * i) * 2, 0, 0);
    }
    u32x4 xr[5][5];
    u32 vx[5];
#pragma unroll
    for (int rg = 0; rg < 5; ++rg) vx[rg] = max(tok0 - 16 + 20 * w + 4 * rg + g, 0) * 1280 + t * 16;
#pragma unroll
    for (int rg = 0; rg < 2; ++rg)
#pragma unroll
        for (int i = 0; i < 5; ++i) xr[rg][i] = ld16<0>(rX, vx[rg] + 256 * i, 0);
    PIN();
    const u64 e64 = ((u64)__builtin_amdgcn_readfirstlane(ep[1]) << 32) | __builtin_amdgcn_readfirstlane(ep[0]);
    const u64 tag = e64 * 6364136223846793005ull + 1442695040888963407ull;
    const u32 tl = (u32)tag, th = (u32)(tag >> 32);
    const u32x2 tagv = {tl, th};

    if (aq) {
#pragma unroll
        for (int i = 0; i < 4; ++i) lds_at<u32x4>(lds, L_STG + (tid + 256 * i) * 16) = al[i];
    } else {
        mod_wave_sums(lds, al, cv, w, l);
    }
    STAMP(1);
    BAR();
    if (aq) {
        const u32 col = g < 3 ? g * 320 + t : min(t & 3, 2) * 320 + 16 + (t >> 2);
#pragma unroll
        for (int i = 0; i < 4; ++i) {
            u32x4 o = gather8(lds, L_STG + (col + 20 * (4 * w + i)) * 2, 1920);
            st16<SC1>(o, rW, g * 1024 + t * 16, WS_WQ + ((16 * a_hf + 4 * w + i) * 20 + a_ks) * 4096 + a_g * 256);
        }
    } else if (am && w == 0) {
        mod_publish(lds, rW, (b - 160) >> 2, part, l);
    }
    PIN();
#pragma unroll
    for (int rg = 2; rg < 5; ++rg)
#pragma unroll
        for (int i = 0; i < 5; ++i) xr[rg][i] = ld16<0>(rX, vx[rg] + 256 * i, 0);
    VMWAIT(15);
    BAR();
    if (tid == 0) st8<SC1>(tagv, rW, b * 8, WS_FA);
    STAMP(2);

    const int bt = b < 96 ? 0 : b < 176 ? 1 : b < 216 ? 2 : 3;
    const bool bm = b >= 96;
    const int b_u = bt == 0 ? b : bt == 1 ? b - 96 : b - 176;
    const int b_ks = bt == 1 ? b_u >> 2 : b_u >> 1, b_cb = bt == 1 ? b_u & 3 : b_u & 1;
    u32x4 bl[6], ml[5];
    {
        const int jj = l >> 3, hi = jj >> 2, lo = jj & 3;
        const int jp = b_ks / 6, u = b_ks - 6 * jp;
        const int r2 = 192 * jp + (u < 4 ? 48 * u + 16 * hi : 48 * (2 * (u - 4) + hi) + 32) + 4 * w + lo;
        const int r1 = b_ks < 16 ? 80 * (b_ks >> 1) + 20 * w + 8 * (b_ks & 1) + jj : 80 * (2 * (b_ks - 16) + hi) + 20 * w + 16 + lo;
        const int ro = b_ks < 16 ? (2 * b_ks + hi) * 20 + 4 * w + lo : (8 * (b_ks - 16) + 2 * w + hi) * 20 + 16 + lo;
        const u32 vb = (bt == 1 ? r1 * 3072 + b_cb * 768 : (bt == 0 ? r2 : ro) * 1280 + b_cb * 640) + (l & 7) * 16;
        const rsrc_t rB = bt == 0 ? mkr(pw2, 1966080) : bt == 1 ? mkr(pw1, 1966080) : mkr(pwo, bt == 2 ? 819200 : 0);
#pragma unroll
        for (int i = 0; i < 5; ++i) bl[i] = ld16<NT>(rB, vb + 128 * i, 0);
        bl[5] = ld16<NT>(rB, bt == 1 ? vb + 640 : 0x80000000u, 0);
        const rsrc_t rB2 = mkr(pwm, bm ? 4915200 : 0);
#pragma unroll
        for (int i = 0; i < 5; ++i) ml[i] = ld16<NT>(rB2, (mrow + 32 * i) * 7680 + (20 + ((b - 96) >> 2)) * 128 + (tid & 7) * 16, 0);
    }
    PIN();

    f32x2 xn[5][20];
#pragma unroll
    for (int rg = 0; rg < 5; ++rg) {
        f32x2 s = {0.f, 0.f};
#pragma unroll
        for (int q = 0; q < 20; ++q) { u32 u = xr[rg][q >> 2][q & 3]; xn[rg][q] = f32x2{blo(u), bhi(u)}; s += xn[rg][q]; }
        const float mean = row_sum(s[0] + s[1]) * (1.0f / 640.0f);
        f32x2 v = {0.f, 0.f};
#pragma unroll
        for (int q = 0; q < 20; ++q) { xn[rg][q] -= mean; v += xn[rg][q] * xn[rg][q]; }
        const float rstd = __builtin_amdgcn_rsqf(row_sum(v[0] + v[1]) * (1.0f / 640.0f) + 1e-5f);
#pragma unroll
        for (int q = 0; q < 20; ++q) xn[rg][q] *= rstd;
    }
    STAMP(3);
    if (w == 0) poll_all(rW, WS_FA, l, tl, th);
    BAR();
    STAMP(4);
    if (b == 0 && tid == 0) st8<SC1>(tagv, rW, 0, WS_EPOCH);

    const int hh = 4 * j + w;
    const u32 sq = WS_WQ + hh * 81920;
    u32x4 wq[20][4];
#define LOADQ(ks) do { _Pragma("unroll") for (int tt = 0; tt < 4; ++tt) wq[ks][tt] = ld16<SC1>(rW, l16 + tt * 1024, sq + (ks) * 4096); } while (0)
    {
        u32x4 mp[5];
#pragma unroll
        for (int i = 0; i < 5; ++i) mp[i] = ld16<SC1>(rW, g * 15360 + t * 16 + i * 256, WS_MODP + w * 1280);
        LOADQ(0);
        LOADQ(1);
        PIN();
#pragma unroll
        for (int i = 0; i < 6; ++i) lds_at<u32x4>(lds, L_STG + (tid >> 3) * 784 + w * 64 + (l & 7) * 16 + 128 * i) = bl[i];
        mod_wave_sums(lds, ml, cv, w, l);
        BAR();
        if (bt < 3) {
            const u32 gb = L_STG + g * (8 * 784 + 64) + (bt == 1 ? (96 * w + t) * 2 : (80 * w + 20 * (t >> 2) + (t & 3)) * 2);
            const u32 gs = bt == 1 ? 32 : 8;
            const int jd = 4 * b_cb + w;
            const u32 db = bt == 0 ? WS_W2 + (jd * 48 + b_ks) * 5120
                         : bt == 1 ? WS_W1 + ((8 * b_cb + 2 * w) * 20 + b_ks) * 3072
                                   : WS_WO + ((jd * 4 + (b_ks < 16 ? b_ks >> 2 : b_ks - 16)) * 5 + (b_ks < 16 ? b_ks & 3 : 4)) * 5120;
#pragma unroll
            for (int i = 0; i < 5; ++i) {
                u32x4 o = gather8(lds, gb + gs * i, 784);
                st16<SC1>(o, rW, l16, db + i * 1024 + (bt == 1 && i >= 3 ? 58368 : 0));
            }
            if (bt == 1) st16<SC1>(gather8(lds, gb + 160, 784), rW, l16, db + 5120 + 58368);
        }
        if (bm && w == 0) mod_publish(lds, rW, 20 + ((b - 96) >> 2), part, l);
        PIN();
#pragma unroll
        for (int i = 0; i < 5; ++i) {
            float m = rbf(sum_groups(mp[i]));
            if (w >= 2) m = rbf(1.0f + m);
            lds_at<float>(lds, L_MODS + (320 * w + 64 * i + 4 * t + g) * 4) = m;
        }
        BAR();
    }
    STAMP(5);
    {
        u32 ha[5];
#pragma unroll
        for (int rg = 0; rg < 5; ++rg) {
            const u32 R = 20 * w + 4 * rg + g;
            ha[rg] = L_H + (R >> 4) * 20480 + t * 256 + (((R & 15) ^ t) * 16);
        }
#pragma unroll
        for (int i = 0; i < 5; ++i) {
            f32x2 sa[4], ca[4];
#pragma unroll
            for (int q = 0; q < 2; ++q) {
                f32x4 s4 = lds_at<f32x4>(lds, L_MODS + 512 * i + t * 32 + q * 16);
                f32x4 c4 = lds_at<f32x4>(lds, L_MODS + 2560 + 512 * i + t * 32 + q * 16);
                sa[2 * q] = f32x2{s4[0], s4[1]}; sa[2 * q + 1] = f32x2{s4[2], s4[3]};
                ca[2 * q] = f32x2{c4[0], c4[1]}; ca[2 * q + 1] = f32x2{c4[2], c4[3]};
            }
#pragma unroll
            for (int rg = 0; rg < 5; ++rg) {
                u32x4 o;
#pragma unroll
                for (int q = 0; q < 4; ++q) { f32x2 h = xn[rg][4 * i + q] * ca[q] + sa[q]; o[q] = pk(h[0], h[1]); }
                lds_at<u32x4>(lds, ha[rg] + i * 4096) = o;
            }
            LOADQ(2 + i);
            PIN();
        }
    }
    VMWAIT(20);
    BAR();
    if (tid == 0) st8<SC1>(tagv, rW, b * 8, WS_FB);
    STAMP(6);
    f32x4 aQ[5], aK[5], aV[5], aR[5];
    u32x4 fb0, fb1;
    {
        const f32x4 z = {0.f, 0.f, 0.f, 0.f};
#pragma unroll
        for (int m = 0; m < 5; ++m) { aQ[m] = z; aK[m] = z; aV[m] = z; aR[m] = z; }
        u32 hr[4];
#pragma unroll
        for (int q = 0; q < 4; ++q) hr[q] = L_H + g * 256 + ((t ^ (4 * q + g)) * 16);
        u32x4 hf[2][5];
#pragma unroll
        for (int m = 0; m < 5; ++m) hf[0][m] = lds_at<u32x4>(lds, hr[0] + m * 20480);
#pragma unroll
        for (int ks = 0; ks < 20; ++ks) {
            if (ks + 1 < 20) {
#pragma unroll
                for (int m = 0; m < 5; ++m) hf[(ks + 1) & 1][m] = lds_at<u32x4>(lds, hr[(ks + 1) & 3] + m * 20480 + (ks + 1) * 1024);
            }
            if (ks + 7 < 20) LOADQ(ks + 7);
            if (ks == 15) { fb0 = ld16<SC1>(rW, l16, WS_FB); fb1 = ld16<SC1>(rW, l16 + 1024, WS_FB); }
#pragma unroll
            for (int m = 0; m < 5; ++m) {
                const u32x4 h = hf[ks & 1][m];
                aK[m] = mfma(wq[ks][1], h, aK[m]);
                aV[m] = mfma(h, wq[ks][2], aV[m]);
                aR[m] = mfma(wq[ks][3], h, aR[m]);
                if (m) aQ[m] = mfma(wq[ks][0], h, aQ[m]);
            }
            PIN();
        }
    }
    STAMP(7);
    if (!__all(tags_ok(fb0, tl, th) && tags_ok(fb1, tl, th))) poll_all(rW, WS_FB, l, tl, th);
    PIN();

    u32x4 wo[5][5], mq[6], xa, xb;
    u32x2 xc;
    const u32 so = WS_WO + (4 * j + w) * 25600;
#define LOADO(kk) do { _Pragma("unroll") for (int tt = 0; tt < 5; ++tt) wo[kk][tt] = ld16<SC1>(rW, l16 + (tt & 3) * 1024, so + (kk) * 5120 + (tt >> 2) * 4096); } while (0)
    const u32 vres = (tok0 + 16 * w + t) * 1280 + (80 * j + 20 * g) * 2;
    {
#pragma unroll
        for (int i = 0; i < 5; ++i) mq[i] = ld16<SC1>(rW, g * 15360 + t * 16 + i * 256, WS_MODP + 7680 + w * 1280);
        mq[5] = ld16<SC1>(rW, g * 15360 + t * 16, WS_MODP + ((w >= 2 ? 3200 : 1280) + 80 * j + 64 * (w & 1)) * 4);
        xa = ld16<0>(rX, vres, 0); xb = ld16<0>(rX, vres + 16, 0); xc = ld8<0>(rX, vres + 32, 0);
        PIN();
        const f32x4 z = {0.f, 0.f, 0.f, 0.f};
        u32x4 qf[5], kf[5];
        u32x2 vp[5], vr[5];
        const u32x4 sel = {(t & 3) == g ? 0x3F80u : 0u, 0u, 0u, 0u};
#pragma unroll
        for (int m = 0; m < 5; ++m) {
            qf[m] = u32x4{pk(aQ[m][0], aQ[m][1]), pk(aQ[m][2], aQ[m][3]), pk(aR[m][0], 0.f), 0u};
            kf[m] = u32x4{pk(aK[m][0], aK[m][1]), pk(aK[m][2], aK[m][3]), pk(aR[m][1], 0.f), 0u};
            vp[m] = u32x2{pk(aV[m][0], aV[m][1]), pk(aV[m][2], aV[m][3])};
            f32x4 r = mfma(u32x4{pk(aR[m][2], 0.f), 0u, 0u, 0u}, sel, z);
            vr[m] = u32x2{pk(r[0], r[1]), pk(r[2], r[3])};
        }
        LOADO(0);
        PIN();
        const bool odd = t & 1, c0 = 4 * g + (t & 1) <= t, c1 = 4 * g + 2 + (t & 1) <= t;
        const float sc = 0.22360679775f * 1.44269504f;
        u32x2 om[5], orr[5];
#pragma unroll
        for (int m = 1; m < 5; ++m) {
            f32x4 sC = mfma(kf[m], qf[m], z), sP = mfma(kf[m - 1], qf[m], z);
            float p0 = (odd ? sP[1] : sP[0]) * sc, p1 = (odd ? sP[3] : sP[2]) * sc;
            if (m == 1 && cl == 0) { p0 = -1.44269504e9f; p1 = -1.44269504e9f; }
            const float s0 = c0 ? (odd ? sC[1] : sC[0]) * sc : p0, s1 = c1 ? (odd ? sC[3] : sC[2]) * sc : p1;
            const float mx = max16(max32(fmaxf(s0, s1)));
            const float e0 = __builtin_amdgcn_exp2f(s0 - mx), e1 = __builtin_amdgcn_exp2f(s1 - mx);
            float sm = e0 + e1;
            sm = add32(sm, sm);
            sm = add16(sm, sm);
            const float ri = __builtin_amdgcn_rcpf(sm);
            const u32 pw = pk(e0 * ri, e1 * ri);
            const u32 w0 = odd ? pw << 16 : pw & 0xffffu, w1 = odd ? pw & 0xffff0000u : pw >> 16;
            const u32x4 pf = {c0 ? w0 : 0u, c1 ? w1 : 0u, c0 ? 0u : w0, c1 ? 0u : w1};
            f32x4 o = mfma(u32x4{vp[m][0], vp[m][1], vp[m - 1][0], vp[m - 1][1]}, pf, z);
            f32x4 r = mfma(u32x4{vr[m][0], vr[m][1], vr[m - 1][0], vr[m - 1][1]}, pf, z);
            om[m] = u32x2{pk(o[0], o[1]), pk(o[2], o[3])};
            orr[m] = u32x2{pk(r[0], r[1]), pk(r[2], r[3])};
            LOADO(m);
        }
        PIN();
        const u32 ob = WS_O + cl * 81920;
        st16<SC1>(u32x4{om[1][0], om[1][1], om[2][0], om[2][1]}, rW, l16, ob + hh * 2048);
        st16<SC1>(u32x4{om[3][0], om[3][1], om[4][0], om[4][1]}, rW, l16 + 1024, ob + hh * 2048);
        if (l < 32) {
            const bool hi = g & 1;
            st16<SC1>(u32x4{hi ? orr[3][0] : orr[1][0], hi ? orr[3][1] : orr[1][1], hi ? orr[4][0] : orr[2][0], hi ? orr[4][1] : orr[2][1]},
                      rW, l16, ob + 65536 + hh * 512);
        }
        PIN();
    }
    STAMP(8);
#pragma unroll
    for (int i = 0; i < 5; ++i) {
        float m = rbf(sum_groups(mq[i]));
        if (w >= 2) m = 1.0f + m;
        lds_at<unsigned short>(lds, L_M2 + (320 * w + 64 * i + 4 * t + g) * 2) = (unsigned short)pk(m, m);
    }
    lds_at<float>(lds, L_G + (64 * w + 4 * t + g) * 4) = rbf(sum_groups(mq[5]));
    VMWAIT(0);
    BAR();
    if (tid == 0) st8<SC1>(tagv, rW, j * 8, WS_FO + cl * 128);
    STAMP(9);
    u32x4 w1[20][3];
    const u32 s1b = WS_W1 + (4 * j + w) * 61440;
#define LOAD1(ks) do { _Pragma("unroll") for (int tp = 0; tp < 3; ++tp) w1[ks][tp] = ld16<SC1>(rW, l16 + tp * 1024, s1b + (ks) * 3072); } while (0)
#pragma unroll
    for (int ks = 0; ks < 5; ++ks) LOAD1(ks);
    PIN();
    if (w == 0) poll_cluster(rW, WS_FO + cl * 128, l, tl, th);
    BAR();
    STAMP(10);

    const bool wodd = w & 1;
    u32 x1p[10];
    {
        u32x4 oa[2][8], ob2[2][2];
        const u32 ob = WS_O + cl * 81920;
#pragma unroll
        for (int pp = 0; pp < 2; ++pp) {
            const int pr = (w >> 1) ^ 1 ^ pp;
#pragma unroll
            for (int q = 0; q < 8; ++q) oa[pp][q] = ld16<SC1>(rW, l16 + (q & 1) * 2048, ob + (8 * w + (q & ~1)) * 2048 + pr * 1024);
#pragma unroll
            for (int e = 0; e < 2; ++e) ob2[pp][e] = ld16<SC1>(rW, g * 1024 + t * 16 + e * 512, ob + 65536 + w * 4096 + pr * 256);
        }
        PIN();
        f32x4 acc[5];
#pragma unroll
        for (int s = 0; s < 4; ++s) {
            const int pp = s >> 1;
            const bool hi = s < 2 ? (s & 1) : (wodd == (bool)(s & 1));
#pragma unroll
            for (int tt = 0; tt < 5; ++tt) acc[tt] = f32x4{0.f, 0.f, 0.f, 0.f};
#pragma unroll
            for (int kk = 0; kk < 5; ++kk) {
                const u32x4 A = kk < 4 ? oa[pp][2 * kk] : ob2[pp][0], B = kk < 4 ? oa[pp][2 * kk + 1] : ob2[pp][1];
                const u32x4 f = {hi ? A[2] : A[0], hi ? A[3] : A[1], hi ? B[2] : B[0], hi ? B[3] : B[1]};
#pragma unroll
                for (int tt = 0; tt < 5; ++tt) acc[tt] = mfma(wo[kk][tt], f, acc[tt]);
            }
            if (s < 3) {
                const int dst = s < 2 ? 2 * ((w >> 1) ^ 1) + s : w ^ 1;
#pragma unroll
                for (int tt = 0; tt < 5; ++tt) lds_at<f32x4>(lds, L_RED + (dst * 3 + (dst ^ w) - 1) * 5120 + tt * 1024 + l16) = acc[tt];
            }
            LOAD1(5 + 2 * s);
            LOAD1(6 + 2 * s);
            PIN();
        }
        BAR();
        STAMP(11);
        const u32 xw[10] = {xa[0], xa[1], xa[2], xa[3], xb[0], xb[1], xb[2], xb[3], xc[0], xc[1]};
        u32 yp[10];
        float su = 0.f, sq2 = 0.f;
#pragma unroll
        for (int tt = 0; tt < 5; ++tt) {
            f32x4 a = acc[tt];
#pragma unroll
            for (int s = 0; s < 3; ++s) a += lds_at<f32x4>(lds, L_RED + (w * 3 + s) * 5120 + tt * 1024 + l16);
            const f32x4 ga = lds_at<f32x4>(lds, L_G + (20 * g + 4 * tt) * 4);
            const u32x2 cb = lds_at<u32x2>(lds, L_M2 + 1280 + (80 * j + 20 * g + 4 * tt) * 2);
#pragma unroll
            for (int q = 0; q < 2; ++q) {
                const u32 xp = pk(blo(xw[2 * tt + q]) + ga[2 * q] * a[2 * q], bhi(xw[2 * tt + q]) + ga[2 * q + 1] * a[2 * q + 1]);
                x1p[2 * tt + q] = xp;
                const float v0 = blo(xp), v1 = bhi(xp);
                su += v0 + v1;
                sq2 += v0 * v0 + v1 * v1;
                yp[2 * tt + q] = pk(v0 * blo(cb[q]), v1 * bhi(cb[q]));
            }
        }
        const u32 yb = WS_Y + cl * 98304;
        st16<SC1>(u32x4{yp[0], yp[1], yp[2], yp[3]}, rW, l16, yb + (w * 16 + 2 * j) * 1024);
        st16<SC1>(u32x4{yp[4], yp[5], yp[6], yp[7]}, rW, l16 + 1024, yb + (w * 16 + 2 * j) * 1024);
        u32 a0 = yp[8], a1 = yp[9], b0 = __float_as_uint(su), b1 = __float_as_uint(sq2);
        swap32(a0, b0);
        swap32(a1, b1);
        st16<SC1>(u32x4{a0, a1, b0, b1}, rW, (l >> 5) * 16384 + (l & 31) * 16, yb + 65536 + (w * 8 + j) * 512);
        PIN();
    }
    STAMP(12);
    VMWAIT(0);
    BAR();
    if (tid == 0) st8<SC1>(tagv, rW, j * 8, WS_FY + cl * 128);
    STAMP(13);
#pragma unroll
    for (int ks = 13; ks < 20; ++ks) LOAD1(ks);
    PIN();
    f32x4 uu[3], vv[3];
    {
        f32x4 uv[3];
#pragma unroll
        for (int tp = 0; tp < 3; ++tp) uv[tp] = f32x4{0.f, 0.f, 0.f, 0.f};
        const u32 mb = L_M2 + ((t & 1) ? 0 : 1280) + 40 * g;
#pragma unroll
        for (int ks = 0; ks < 20; ++ks) {
            const u32 o0 = ks < 16 ? 160 * (ks >> 1) + 16 * (ks & 1) : 320 * (ks - 16) + 32;
            const u32x2 lo = lds_at<u32x2>(lds, mb + o0), hi = lds_at<u32x2>(lds, mb + o0 + (ks < 16 ? 8 : 160));
#pragma unroll
            for (int tp = 0; tp < 3; ++tp) uv[tp] = mfma(w1[ks][tp], u32x4{lo[0], lo[1], hi[0], hi[1]}, uv[tp]);
        }
#pragma unroll
        for (int tp = 0; tp < 3; ++tp)
#pragma unroll
            for (int r = 0; r < 4; ++r) { uu[tp][r] = dpp<0xA0>(uv[tp][r]); vv[tp][r] = dpp<0xF5>(uv[tp][r]); }
    }
    PIN();
    STAMP(14);
    if (w == 0) poll_cluster(rW, WS_FY + cl * 128, l, tl, th);
    BAR();
    STAMP(15);

    u32x4 w2[12][5];
    const u32 s2b = WS_W2 + (j * 48 + 12 * w) * 5120;
#define LOAD2(kk) do { _Pragma("unroll") for (int tt = 0; tt < 5; ++tt) w2[kk][tt] = ld16<SC1>(rW, l16 + (tt & 3) * 1024, s2b + (kk) * 5120 + (tt >> 2) * 4096); } while (0)
    {
        const u32 yb = WS_Y + cl * 98304;
        u32x4 yl[4][5], sl[4];
#pragma unroll
        for (int m = 0; m < 4; ++m) {
#pragma unroll
            for (int i = 0; i < 4; ++i) yl[m][i] = ld16<SC1>(rW, l16 + i * 1024, yb + (m * 16 + 4 * w) * 1024);
            yl[m][4] = ld16<SC1>(rW, l16, yb + 65536 + (m * 8 + 2 * w) * 512);
        }
#pragma unroll
        for (int i = 0; i < 4; ++i) sl[i] = ld16<SC1>(rW, l16 + i * 1024, yb + 81920 + w * 4096);
        PIN();
#define STAGEY(m) do { \
        _Pragma("unroll") for (int i = 0; i < 4; ++i) lds_at<u32x4>(lds, L_H + ((m) * 20 + 4 * w + i) * 1024 + l16) = yl[m][i]; \
        const u32 ra = L_H + ((m) * 20 + 16 + w) * 1024 + (l & 31) * 16 + (l >> 5) * 8; \
        lds_at<u32x2>(lds, ra) = u32x2{yl[m][4][0], yl[m][4][1]}; \
        lds_at<u32x2>(lds, ra + 512) = u32x2{yl[m][4][2], yl[m][4][3]}; } while (0)
        STAGEY(0);
        BAR();
        const u32 hb = WS_H + cl * 196608 + j * 24576;
        f32x4 acc[2][3];
        u32x2 ex[2];
#pragma unroll
        for (int m = 0; m < 5; ++m) {
            f32x2 st;
            u32 hp[6];
            if (m > 0) st = lds_at<f32x2>(lds, L_ST + ((m - 1) * 16 + t) * 8);
            if (m < 4) {
#pragma unroll
                for (int tp = 0; tp < 3; ++tp) acc[m & 1][tp] = f32x4{0.f, 0.f, 0.f, 0.f};
            }
#pragma unroll
            for (int ks = 0; ks < 20; ++ks) {
                if (m < 4) {
                    const u32x4 y = lds_at<u32x4>(lds, L_H + (m * 20 + ks) * 1024 + l16);
#pragma unroll
                    for (int tp = 0; tp < 3; ++tp) acc[m & 1][tp] = mfma(w1[ks][tp], y, acc[m & 1][tp]);
                }
                if (m > 0 && ks >= 2 && ks < 14 && (ks & 1) == 0) {
                    const int q = (ks - 2) >> 1, tp = q >> 1, r = (q & 1) * 2;
                    const f32x4 a = acc[(m - 1) & 1][tp];
                    hp[q] = pk(silu(a[r] * st[0] + (st[1] * uu[tp][r] + vv[tp][r])),
                               silu(a[r + 1] * st[0] + (st[1] * uu[tp][r + 1] + vv[tp][r + 1])));
                }
                if (m == 0 && ks == 6) STAGEY(1);
                if (m == 0 && ks == 11) STAGEY(2);
                if (m == 0 && ks == 16) STAGEY(3);
                if (m >= 1 && m < 4 && ks == 3) LOAD2(2 * m - 2);
                if (m >= 1 && m < 4 && ks == 12) LOAD2(2 * m - 1);
            }
            if (m == 0) {
                float su = 0.f, sq2 = 0.f;
#pragma unroll
                for (int i = 0; i < 4; ++i) {
                    su += __uint_as_float(sl[i][0]) + __uint_as_float(sl[i][2]);
                    sq2 += __uint_as_float(sl[i][1]) + __uint_as_float(sl[i][3]);
                }
                su = add32(su, su); su = add16(su, su);
                sq2 = add32(sq2, sq2); sq2 = add16(sq2, sq2);
                const float mean = su * (1.0f / 640.0f);
                const float rstd = __builtin_amdgcn_rsqf(fmaxf(sq2 * (1.0f / 640.0f) - mean * mean, 0.f) + 1e-5f);
                lds_at<f32x2>(lds, L_ST + (w * 16 + t) * 8) = f32x2{rstd, -mean * rstd};
                BAR();
            } else {
                st16<SC1>(u32x4{hp[0], hp[1], hp[2], hp[3]}, rW, l16, hb + (w * 4 + m - 1) * 1024);
                ex[(m - 1) & 1] = u32x2{hp[4], hp[5]};
                if (((m - 1) & 1) == 1)
                    st16<SC1>(u32x4{ex[0][0], ex[0][1], ex[1][0], ex[1][1]}, rW, l16, hb + 16384 + (w * 2 + ((m - 1) >> 1)) * 1024);
                PIN();
            }
        }
    }
    STAMP(16);
    VMWAIT(0);
    BAR();
    if (tid == 0) st8<SC1>(tagv, rW, j * 8, WS_FH + cl * 128);
    STAMP(17);
#pragma unroll
    for (int kk = 6; kk < 12; ++kk) LOAD2(kk);
    PIN();
    if (w == 0) poll_cluster(rW, WS_FH + cl * 128, l, tl, th);
    BAR();
    STAMP(18);

    {
        const u32 hb = WS_H + cl * 196608 + 2 * w * 24576;
        const int po = (w >> 1) ^ 1;
        u32x4 hm[4][8], he[2][8];
#define LOADHM(s) do { const int mt = (s) < 2 ? 2 * po + (s) : (s) == 2 ? w ^ 1 : w; \
        _Pragma("unroll") for (int q = 0; q < 8; ++q) hm[s][q] = ld16<SC1>(rW, l16, hb + (q >> 2) * 24576 + (q & 3) * 4096 + mt * 1024); } while (0)
#define LOADHE(pp) do { \
        _Pragma("unroll") for (int q = 0; q < 8; ++q) he[pp][q] = ld16<SC1>(rW, l16 + (q & 1) * 2048, hb + (q >> 2) * 24576 + 16384 + (q & 2) * 2048 + (po ^ (pp)) * 1024); } while (0)
        LOADHM(0);
        LOADHE(0);
        LOADHM(1);
        PIN();
        f32x4 acc[5];
#pragma unroll
        for (int s = 0; s < 4; ++s) {
            const int pp = s >> 1;
            const bool hi = s < 2 ? (s & 1) : (wodd == (bool)(s & 1));
#pragma unroll
            for (int tt = 0; tt < 5; ++tt) acc[tt] = f32x4{0.f, 0.f, 0.f, 0.f};
#pragma unroll
            for (int q = 0; q < 8; ++q) {
#pragma unroll
                for (int tt = 0; tt < 5; ++tt) acc[tt] = mfma(w2[6 * (q >> 2) + (q & 3)][tt], hm[s][q], acc[tt]);
            }
            if (s == 0) LOADHM(2);
            if (s == 1) LOADHE(1);
            PIN();
#pragma unroll
            for (int q = 0; q < 4; ++q) {
                const u32x4 A = he[pp][2 * q], B = he[pp][2 * q + 1];
                const u32x4 f = {hi ? A[2] : A[0], hi ? A[3] : A[1], hi ? B[2] : B[0], hi ? B[3] : B[1]};
#pragma unroll
                for (int tt = 0; tt < 5; ++tt) acc[tt] = mfma(w2[6 * (q >> 1) + 4 + (q & 1)][tt], f, acc[tt]);
            }
            if (s == 1) LOADHM(3);
            if (s < 3) {
                const int dst = s < 2 ? 2 * po + s : w ^ 1;
#pragma unroll
                for (int tt = 0; tt < 5; ++tt) lds_at<f32x4>(lds, L_RED + (dst * 3 + (dst ^ w) - 1) * 5120 + tt * 1024 + l16) = acc[tt];
            }
            PIN();
        }
        BAR();
        STAMP(19);
        u32 op[10];
#pragma unroll
        for (int tt = 0; tt < 5; ++tt) {
            f32x4 a = acc[tt];
#pragma unroll
            for (int s = 0; s < 3; ++s) a += lds_at<f32x4>(lds, L_RED + (w * 3 + s) * 5120 + tt * 1024 + l16);
            const f32x4 gb = lds_at<f32x4>(lds, L_G + 512 + (20 * g + 4 * tt) * 4);
#pragma unroll
            for (int q = 0; q < 2; ++q)
                op[2 * tt + q] = pk(blo(x1p[2 * tt + q]) + gb[2 * q] * a[2 * q], bhi(x1p[2 * tt + q]) + gb[2 * q + 1] * a[2 * q + 1]);
        }
        const rsrc_t rO = mkr(pout, 2621440);
        st16<0>(u32x4{op[0], op[1], op[2], op[3]}, rO, vres, 0);
        st16<0>(u32x4{op[4], op[5], op[6], op[7]}, rO, vres + 16, 0);
        st8<0>(u32x2{op[8], op[9]}, rO, vres + 32, 0);
    }
}

extern "C" void kernel_launch(void* const* d_in, const int* in_sizes, int n_in, void* d_out, int out_size, void* d_ws,
                              size_t ws_size, hipStream_t stream) {
    k_block<<<256, 256, 0, stream>>>(d_in[0], d_in[1], d_in[2], d_in[4], d_in[5], d_in[6], d_in[7], d_out, (char*)d_ws);
}
```

```cpp
#include <hip/hip_runtime.h>
#include <stdint.h>

typedef unsigned int u32;
typedef unsigned long long u64;
typedef u32 u32x2 __attribute__((ext_vector_type(2)));
typedef u32 u32x4 __attribute__((ext_vector_type(4)));
typedef float f32x2 __attribute__((ext_vector_type(2)));
typedef float f32x4 __attribute__((ext_vector_type(4)));
typedef __bf16 bf16x2 __attribute__((ext_vector_type(2)));
typedef __bf16 bf16x8 __attribute__((ext_vector_type(8)));
typedef __amdgpu_buffer_rsrc_t rsrc_t;

#define DEVI __device__ __forceinline__
#define PIN() do { asm volatile("" ::: "memory"); __builtin_amdgcn_sched_barrier(0); } while (0)
#define BAR() do { asm volatile("s_waitcnt lgkmcnt(0)\n\ts_barrier" ::: "memory"); __builtin_amdgcn_sched_barrier(0); } while (0)
#define VMWAIT(n) do { asm volatile("s_waitcnt vmcnt(" #n ")" ::: "memory"); __builtin_amdgcn_sched_barrier(0); } while (0)

constexpr int SC1 = 16, NT = 2;
constexpr int SPIN_LIMIT = 200000;

constexpr u32 WS_EPOCH = 0, WS_FA = 4096, WS_FB = 8192, WS_FO = 12288, WS_FY = 16384, WS_FH = 20480;
constexpr u32 WS_MODP = 32768;
constexpr u32 WS_WQ = 131072;
constexpr u32 WS_WO = WS_WQ + 2621440;
constexpr u32 WS_W1 = WS_WO + 819200;
constexpr u32 WS_W2 = WS_W1 + 1966080;
constexpr u32 WS_O = WS_W2 + 1966080;
constexpr u32 WS_Y = WS_O + 32 * 81920;
constexpr u32 WS_H = WS_Y + 32 * 98304;
constexpr u32 WS_END = WS_H + 32 * 196608;
constexpr u32 WS_STAMP = 32u << 20;

constexpr u32 L_H = 0;
constexpr u32 L_RED = 81920;
constexpr u32 L_STG = 102400;
constexpr u32 L_MR = 128000;
constexpr u32 L_MODS = 143360;
constexpr u32 L_M2 = 148480;
constexpr u32 L_G = 151040;
constexpr u32 L_ST = 152064;
constexpr u32 L_END = 152576;

DEVI rsrc_t mkr(const void* p, u32 n) { return __builtin_amdgcn_make_buffer_rsrc((void*)p, 0, n, 0x00020000); }
template <int AUX> DEVI u32x4 ld16(rsrc_t r, u32 v, u32 s) { return __builtin_amdgcn_raw_buffer_load_b128(r, v, s, AUX); }
template <int AUX> DEVI u32x2 ld8(rsrc_t r, u32 v, u32 s) { return __builtin_amdgcn_raw_buffer_load_b64(r, v, s, AUX); }
template <int AUX> DEVI void st16(u32x4 d, rsrc_t r, u32 v, u32 s) { __builtin_amdgcn_raw_buffer_store_b128(d, r, v, s, AUX); }
template <int AUX> DEVI void st8(u32x2 d, rsrc_t r, u32 v, u32 s) { __builtin_amdgcn_raw_buffer_store_b64(d, r, v, s, AUX); }
DEVI f32x4 mfma(u32x4 a, u32x4 b, f32x4 c) {
    return __builtin_amdgcn_mfma_f32_16x16x32_bf16(__builtin_bit_cast(bf16x8, a), __builtin_bit_cast(bf16x8, b), c, 0, 0, 0);
}
DEVI u32 pk(float lo, float hi) { f32x2 v = {lo, hi}; return __builtin_bit_cast(u32, __builtin_convertvector(v, bf16x2)); }
DEVI float blo(u32 u) { return __uint_as_float(u << 16); }
DEVI float bhi(u32 u) { return __uint_as_float(u & 0xffff0000u); }
DEVI float rbf(float x) { return blo(pk(x, x)); }
DEVI void swap32(u32& a, u32& b) { auto r = __builtin_amdgcn_permlane32_swap(a, b, false, false); a = r[0]; b = r[1]; }
DEVI void swap16(u32& a, u32& b) { auto r = __builtin_amdgcn_permlane16_swap(a, b, false, false); a = r[0]; b = r[1]; }
DEVI float add32(float a, float b) { u32 x = __float_as_uint(a), y = __float_as_uint(b); swap32(x, y); return __uint_as_float(x) + __uint_as_float(y); }
DEVI float add16(float a, float b) { u32 x = __float_as_uint(a), y = __float_as_uint(b); swap16(x, y); return __uint_as_float(x) + __uint_as_float(y); }
DEVI float max32(float a) { u32 x = __float_as_uint(a), y = x; swap32(x, y); return fmaxf(__uint_as_float(x), __uint_as_float(y)); }
DEVI float max16(float a) { u32 x = __float_as_uint(a), y = x; swap16(x, y); return fmaxf(__uint_as_float(x), __uint_as_float(y)); }
template <int CTRL> DEVI float dpp(float v) { return __int_as_float(__builtin_amdgcn_update_dpp(0, __float_as_int(v), CTRL, 0xf, 0xf, true)); }
DEVI float row_sum(float v) { v += dpp<0x128>(v); v += dpp<0x124>(v); v += dpp<0x122>(v); v += dpp<0x121>(v); return v; }
DEVI float silu(float z) { return z * __builtin_amdgcn_rcpf(1.0f + __builtin_amdgcn_exp2f(z * -1.44269504f)); }
DEVI float sum_groups(u32x4 c) {
    float d0 = add32(__uint_as_float(c[0]), __uint_as_float(c[2]));
    float d1 = add32(__uint_as_float(c[1]), __uint_as_float(c[3]));
    return add16(d0, d1);
}

#define STAMP(i)

template <typename T> DEVI T& lds_at(char* lds, u32 off) { return *reinterpret_cast<T*>(lds + off); }

DEVI u32x4 gather8(char* lds, u32 off, u32 stride) {
    u32x4 o;
#pragma unroll
    for (int q = 0; q < 4; ++q) {
        u32 lo = lds_at<unsigned short>(lds, off + (2 * q) * stride);
        u32 hi = lds_at<unsigned short>(lds, off + (2 * q + 1) * stride);
        o[q] = lo | (hi << 16);
    }
    return o;
}

DEVI void mod_wave_sums(char* lds, const u32x4* wv, const u32* cv, int w, int l) {
    float a[8];
#pragma unroll
    for (int e = 0; e < 8; ++e) a[e] = 0.f;
#pragma unroll
    for (int i = 0; i < 5; ++i) {
        float s = rbf(silu(blo(cv[i])));
#pragma unroll
        for (int q = 0; q < 4; ++q) { a[2 * q] += s * blo(wv[i][q]); a[2 * q + 1] += s * bhi(wv[i][q]); }
    }
    float d[4], e2[2];
#pragma unroll
    for (int q = 0; q < 4; ++q) d[q] = add32(a[q], a[q + 4]);
#pragma unroll
    for (int q = 0; q < 2; ++q) { e2[q] = add16(d[q], d[q + 2]); e2[q] += dpp<0x128>(e2[q]); }
    lds_at<f32x2>(lds, L_MR + w * 256 + ((l & 7) * 8 + (l >> 4) * 2) * 4) = f32x2{e2[0], e2[1]};
}
DEVI void mod_publish(char* lds, rsrc_t rW, int ln, int part, int l) {
    if (l < 16) {
        f32x4 s = lds_at<f32x4>(lds, L_MR + l * 16) + lds_at<f32x4>(lds, L_MR + 256 + l * 16) +
                  (lds_at<f32x4>(lds, L_MR + 512 + l * 16) + lds_at<f32x4>(lds, L_MR + 768 + l * 16));
        st16<SC1>(__builtin_bit_cast(u32x4, s), rW, l * 16, WS_MODP + part * 15360 + ln * 256);
    }
}

DEVI bool tags_ok(u32x4 f, u32 tl, u32 th) { return f[0] == tl && f[1] == th && f[2] == tl && f[3] == th; }
DEVI void poll_all(rsrc_t rW, u32 base, int l, u32 tl, u32 th) {
    for (int it = 0; it < SPIN_LIMIT; ++it) {
        asm volatile("" ::: "memory");
        u32x4 f0 = ld16<SC1>(rW, l * 16, base), f1 = ld16<SC1>(rW, l * 16 + 1024, base);
        if (__all(tags_ok(f0, tl, th) && tags_ok(f1, tl, th))) break;
    }
}
DEVI void poll_cluster(rsrc_t rW, u32 base, int l, u32 tl, u32 th) {
    for (int it = 0; it < SPIN_LIMIT; ++it) {
        asm volatile("" ::: "memory");
        u32x2 f = ld8<SC1>(rW, (l & 7) * 8, base);
        if (__all(f[0] == tl && f[1] == th)) break;
    }
}

__global__ __launch_bounds__(256, 1) void k_block(const void* __restrict__ px, const void* __restrict__ pwq,
                                                  const void* __restrict__ pwo, const void* __restrict__ pw1,
                                                  const void* __restrict__ pw2, const void* __restrict__ pc,
                                                  const void* __restrict__ pwm, void* __restrict__ pout,
                                                  char* __restrict__ ws) {
    __shared__ __attribute__((aligned(16))) char lds[L_END];
    STAMP(0);
    const int tid = threadIdx.x, l = tid & 63, g = l >> 4, t = l & 15;
    const int w = __builtin_amdgcn_readfirstlane(tid >> 6);
    const int b = blockIdx.x, cl = (b & 7) * 4 + (b >> 6), j = (b >> 3) & 7, tok0 = cl * 64;
    const rsrc_t rW = mkr(ws, WS_END), rX = mkr(px, 2621440), rC = mkr(pc, 1280), rM = mkr(pwm, 4915200);
    const u32 l16 = l * 16;

    u64 pcv;
    asm volatile("s_getpc_b64 %0" : "=s"(pcv));
    u32x4 cpf = ld16<0>(mkr((const void*)(pcv & ~127ull), 45056), l16 + ((b >> 3) * 4 + w) * 1024, 0);
    PIN();
    u32x2 ep = ld8<SC1>(rW, 0, WS_EPOCH);
    const bool aq = b < 160, am = !aq && b < 240;
    const int a_ks = b >> 3, a_g = (b >> 1) & 3, a_hf = b & 1, part = b & 3;
    const u32 mrow = part * 160 + (tid >> 3);
    u32x4 al[5];
    u32 cv[5];
    {
        const rsrc_t rA = aq ? mkr(pwq, 2457600) : mkr(pwm, am ? 4915200 : 0);
#pragma unroll
        for (int i = 0; i < 5; ++i) {
            u32 c = tid + 256 * i, row = c / 120, rem = c - row * 120, seg = rem / 40, cc = rem - seg * 40;
            u32 vq = c < 960 ? (32 * a_ks + 8 * a_g + row) * 3840 + seg * 1280 + a_hf * 640 + cc * 16 : 0x80000000u;
            u32 vm = (mrow + 32 * i) * 7680 + ((b - 160) >> 2) * 128 + (tid & 7) * 16;
            al[i] = ld16<NT>(rA, aq ? vq : vm, 0);
        }
#pragma unroll
        for (int i = 0; i < 5; ++i) cv[i] = __builtin_amdgcn_raw_buffer_load_b16(rC, (mrow + 32 * i) * 2, 0, 0);
    }
    u32x4 xr[5][5];
    u32 vx[5];
#pragma unroll
    for (int rg = 0; rg < 5; ++rg) vx[rg] = max(tok0 - 16 + 20 * w + 4 * rg + g, 0) * 1280 + t * 16;
#pragma unroll
    for (int rg = 0; rg < 2; ++rg)
#pragma unroll
        for (int i = 0; i < 5; ++i) xr[rg][i] = ld16<0>(rX, vx[rg] + 256 * i, 0);
    PIN();
    const u64 e64 = ((u64)__builtin_amdgcn_readfirstlane(ep[1]) << 32) | __builtin_amdgcn_readfirstlane(ep[0]);
    const u64 tag = e64 * 6364136223846793005ull + 1442695040888963407ull;
    const u32 tl = (u32)tag, th = (u32)(tag >> 32);
    const u32x2 tagv = {tl, th};

    if (aq) {
#pragma unroll
        for (int i = 0; i < 4; ++i) lds_at<u32x4>(lds, L_STG + (tid + 256 * i) * 16) = al[i];
    } else {
        mod_wave_sums(lds, al, cv, w, l);
    }
    asm volatile("" :: "v"(cpf));
    STAMP(1);
    BAR();
    if (aq) {
        const u32 col = g < 3 ? g * 320 + t : min(t & 3, 2) * 320 + 16 + (t >> 2);
#pragma unroll
        for (int i = 0; i < 4; ++i) {
            u32x4 o = gather8(lds, L_STG + (col + 20 * (4 * w + i)) * 2, 1920);
            st16<SC1>(o, rW, g * 1024 + t * 16, WS_WQ + ((16 * a_hf + 4 * w + i) * 20 + a_ks) * 4096 + a_g * 256);
        }
    } else if (am && w == 0) {
        mod_publish(lds, rW, (b - 160) >> 2, part, l);
    }
    PIN();
#pragma unroll
    for (int rg = 2; rg < 5; ++rg)
#pragma unroll
        for (int i = 0; i < 5; ++i) xr[rg][i] = ld16<0>(rX, vx[rg] + 256 * i, 0);
    VMWAIT(15);
    BAR();
    if (tid == 0) st8<SC1>(tagv, rW, b * 8, WS_FA);
    STAMP(2);

    const int bt = b < 96 ? 0 : b < 176 ? 1 : b < 216 ? 2 : 3;
    const bool bm = b >= 96;
    const int b_u = bt == 0 ? b : bt == 1 ? b - 96 : b - 176;
    const int b_ks = bt == 1 ? b_u >> 2 : b_u >> 1, b_cb = bt == 1 ? b_u & 3 : b_u & 1;
    u32x4 bl[6], ml[5];
    {
        const int jj = l >> 3, hi = jj >> 2, lo = jj & 3;
        const int jp = b_ks / 6, u = b_ks - 6 * jp;
        const int r2 = 192 * jp + (u < 4 ? 48 * u + 16 * hi : 48 * (2 * (u - 4) + hi) + 32) + 4 * w + lo;
        const int r1 = b_ks < 16 ? 80 * (b_ks >> 1) + 20 * w + 8 * (b_ks & 1) + jj : 80 * (2 * (b_ks - 16) + hi) + 20 * w + 16 + lo;
        const int ro = b_ks < 16 ? (2 * b_ks + hi) * 20 + 4 * w + lo : (8 * (b_ks - 16) + 2 * w + hi) * 20 + 16 + lo;
        const u32 vb = (bt == 1 ? r1 * 3072 + b_cb * 768 : (bt == 0 ? r2 : ro) * 1280 + b_cb * 640) + (l & 7) * 16;
        const rsrc_t rB = bt == 0 ? mkr(pw2, 1966080) : bt == 1 ? mkr(pw1, 1966080) : mkr(pwo, bt == 2 ? 819200 : 0);
#pragma unroll
        for (int i = 0; i < 5; ++i) bl[i] = ld16<NT>(rB, vb + 128 * i, 0);
        bl[5] = ld16<NT>(rB, bt == 1 ? vb + 640 : 0x80000000u, 0);
        const rsrc_t rB2 = mkr(pwm, bm ? 4915200 : 0);
#pragma unroll
        for (int i = 0; i < 5; ++i) ml[i] = ld16<NT>(rB2, (mrow + 32 * i) * 7680 + (20 + ((b - 96) >> 2)) * 128 + (tid & 7) * 16, 0);
    }
    PIN();

    f32x2 xn[5][20];
#pragma unroll
    for (int rg = 0; rg < 5; ++rg) {
        f32x2 s = {0.f, 0.f};
#pragma unroll
        for (int q = 0; q < 20; ++q) { u32 u = xr[rg][q >> 2][q & 3]; xn[rg][q] = f32x2{blo(u), bhi(u)}; s += xn[rg][q]; }
        const float mean = row_sum(s[0] + s[1]) * (1.0f / 640.0f);
        f32x2 v = {0.f, 0.f};
#pragma unroll
        for (int q = 0; q < 20; ++q) { xn[rg][q] -= mean; v += xn[rg][q] * xn[rg][q]; }
        const float rstd = __builtin_amdgcn_rsqf(row_sum(v[0] + v[1]) * (1.0f / 640.0f) + 1e-5f);
#pragma unroll
        for (int q = 0; q < 20; ++q) xn[rg][q] *= rstd;
    }
    STAMP(3);
    if (w == 0) poll_all(rW, WS_FA, l, tl, th);
    BAR();
    STAMP(4);
    if (b == 0 && tid == 0) st8<SC1>(tagv, rW, 0, WS_EPOCH);

    const int hh = 4 * j + w;
    const u32 sq = WS_WQ + hh * 81920;
    u32x4 wq[20][4];
#define LOADQ(ks) do { _Pragma("unroll") for (int tt = 0; tt < 4; ++tt) wq[ks][tt] = ld16<SC1>(rW, l16 + tt * 1024, sq + (ks) * 4096); } while (0)
    {
        u32x4 mp[5];
#pragma unroll
        for (int i = 0; i < 5; ++i) mp[i] = ld16<SC1>(rW, g * 15360 + t * 16 + i * 256, WS_MODP + w * 1280);
        LOADQ(0);
        LOADQ(1);
        PIN();
#pragma unroll
        for (int i = 0; i < 6; ++i) lds_at<u32x4>(lds, L_STG + (tid >> 3) * 784 + w * 64 + (l & 7) * 16 + 128 * i) = bl[i];
        mod_wave_sums(lds, ml, cv, w, l);
        BAR();
        if (bt < 3) {
            const u32 gb = L_STG + g * (8 * 784 + 64) + (bt == 1 ? (96 * w + t) * 2 : (80 * w + 20 * (t >> 2) + (t & 3)) * 2);
            const u32 gs = bt == 1 ? 32 : 8;
            const int jd = 4 * b_cb + w;
            const u32 db = bt == 0 ? WS_W2 + (jd * 48 + b_ks) * 5120
                         : bt == 1 ? WS_W1 + ((8 * b_cb + 2 * w) * 20 + b_ks) * 3072
                                   : WS_WO + ((jd * 4 + (b_ks < 16 ? b_ks >> 2 : b_ks - 16)) * 5 + (b_ks < 16 ? b_ks & 3 : 4)) * 5120;
#pragma unroll
            for (int i = 0; i < 5; ++i) {
                u32x4 o = gather8(lds, gb + gs * i, 784);
                st16<SC1>(o, rW, l16, db + i * 1024 + (bt == 1 && i >= 3 ? 58368 : 0));
            }
            if (bt == 1) st16<SC1>(gather8(lds, gb + 160, 784), rW, l16, db + 5120 + 58368);
        }
        if (bm && w == 0) mod_publish(lds, rW, 20 + ((b - 96) >> 2), part, l);
        PIN();
#pragma unroll
        for (int i = 0; i < 5; ++i) {
            float m = rbf(sum_groups(mp[i]));
            if (w >= 2) m = rbf(1.0f + m);
            lds_at<float>(lds, L_MODS + (320 * w + 64 * i + 4 * t + g) * 4) = m;
        }
        BAR();
    }
    STAMP(5);
    {
        u32 ha[5];
#pragma unroll
        for (int rg = 0; rg < 5; ++rg) {
            const u32 R = 20 * w + 4 * rg + g;
            ha[rg] = L_H + (R >> 4) * 20480 + t * 256 + (((R & 15) ^ t) * 16);
        }
#pragma unroll
        for (int i = 0; i < 5; ++i) {
            f32x2 sa[4], ca[4];
#pragma unroll
            for (int q = 0; q < 2; ++q) {
                f32x4 s4 = lds_at<f32x4>(lds, L_MODS + 512 * i + t * 32 + q * 16);
                f32x4 c4 = lds_at<f32x4>(lds, L_MODS + 2560 + 512 * i + t * 32 + q * 16);
                sa[2 * q] = f32x2{s4[0], s4[1]}; sa[2 * q + 1] = f32x2{s4[2], s4[3]};
                ca[2 * q] = f32x2{c4[0], c4[1]}; ca[2 * q + 1] = f32x2{c4[2], c4[3]};
            }
#pragma unroll
            for (int rg = 0; rg < 5; ++rg) {
                u32x4 o;
#pragma unroll
                for (int q = 0; q < 4; ++q) { f32x2 h = xn[rg][4 * i + q] * ca[q] + sa[q]; o[q] = pk(h[0], h[1]); }
                lds_at<u32x4>(lds, ha[rg] + i * 4096) = o;
            }
            LOADQ(2 + i);
            PIN();
        }
    }
    VMWAIT(20);
    BAR();
    if (tid == 0) st8<SC1>(tagv, rW, b * 8, WS_FB);
    STAMP(6);
    f32x4 aQ[5], aK[5], aV[5], aR[5];
    u32x4 fb0, fb1;
    {
        const f32x4 z = {0.f, 0.f, 0.f, 0.f};
#pragma unroll
        for (int m = 0; m < 5; ++m) { aQ[m] = z; aK[m] = z; aV[m] = z; aR[m] = z; }
        u32 hr[4];
#pragma unroll
        for (int q = 0; q < 4; ++q) hr[q] = L_H + g * 256 + ((t ^ (4 * q + g)) * 16);
        u32x4 hf[2][5];
#pragma unroll
        for (int m = 0; m < 5; ++m) hf[0][m] = lds_at<u32x4>(lds, hr[0] + m * 20480);
#pragma unroll
        for (int ks = 0; ks < 20; ++ks) {
            if (ks + 1 < 20) {
#pragma unroll
                for (int m = 0; m < 5; ++m) hf[(ks + 1) & 1][m] = lds_at<u32x4>(lds, hr[(ks + 1) & 3] + m * 20480 + (ks + 1) * 1024);
            }
            if (ks + 7 < 20) LOADQ(ks + 7);
            if (ks == 15) { fb0 = ld16<SC1>(rW, l16, WS_FB); fb1 = ld16<SC1>(rW, l16 + 1024, WS_FB); }
#pragma unroll
            for (int m = 0; m < 5; ++m) {
                const u32x4 h = hf[ks & 1][m];
                aK[m] = mfma(wq[ks][1], h, aK[m]);
                aV[m] = mfma(h, wq[ks][2], aV[m]);
                aR[m] = mfma(wq[ks][3], h, aR[m]);
                if (m) aQ[m] = mfma(wq[ks][0], h, aQ[m]);
            }
            PIN();
        }
    }
    STAMP(7);
    if (!__all(tags_ok(fb0, tl, th) && tags_ok(fb1, tl, th))) poll_all(rW, WS_FB, l, tl, th);
    PIN();

    u32x4 wo[5][5], mq[6], xa, xb;
    u32x2 xc;
    const u32 so = WS_WO + (4 * j + w) * 25600;
#define LOADO(kk) do { _Pragma("unroll") for (int tt = 0; tt < 5; ++tt) wo[kk][tt] = ld16<SC1>(rW, l16 + (tt & 3) * 1024, so + (kk) * 5120 + (tt >> 2) * 4096); } while (0)
    const u32 vres = (tok0 + 16 * w + t) * 1280 + (80 * j + 20 * g) * 2;
    {
#pragma unroll
        for (int i = 0; i < 5; ++i) mq[i] = ld16<SC1>(rW, g * 15360 + t * 16 + i * 256, WS_MODP + 7680 + w * 1280);
        mq[5] = ld16<SC1>(rW, g * 15360 + t * 16, WS_MODP + ((w >= 2 ? 3200 : 1280) + 80 * j + 64 * (w & 1)) * 4);
        xa = ld16<0>(rX, vres, 0); xb = ld16<0>(rX, vres + 16, 0); xc = ld8<0>(rX, vres + 32, 0);
        PIN();
        const f32x4 z = {0.f, 0.f, 0.f, 0.f};
        u32x4 qf[5], kf[5];
        u32x2 vp[5], vr[5];
        const u32x4 sel = {(t & 3) == g ? 0x3F80u : 0u, 0u, 0u, 0u};
#pragma unroll
        for (int m = 0; m < 5; ++m) {
            qf[m] = u32x4{pk(aQ[m][0], aQ[m][1]), pk(aQ[m][2], aQ[m][3]), pk(aR[m][0], 0.f), 0u};
            kf[m] = u32x4{pk(aK[m][0], aK[m][1]), pk(aK[m][2], aK[m][3]), pk(aR[m][1], 0.f), 0u};
            vp[m] = u32x2{pk(aV[m][0], aV[m][1]), pk(aV[m][2], aV[m][3])};
            f32x4 r = mfma(u32x4{pk(aR[m][2], 0.f), 0u, 0u, 0u}, sel, z);
            vr[m] = u32x2{pk(r[0], r[1]), pk(r[2], r[3])};
        }
        LOADO(0);
        PIN();
        const bool odd = t & 1, c0 = 4 * g + (t & 1) <= t, c1 = 4 * g + 2 + (t & 1) <= t;
        const float sc = 0.22360679775f * 1.44269504f;
        u32x2 om[5], orr[5];
#pragma unroll
        for (int m = 1; m < 5; ++m) {
            f32x4 sC = mfma(kf[m], qf[m], z), sP = mfma(kf[m - 1], qf[m], z);
            float p0 = (odd ? sP[1] : sP[0]) * sc, p1 = (odd ? sP[3] : sP[2]) * sc;
            if (m == 1 && cl == 0) { p0 = -1.44269504e9f; p1 = -1.44269504e9f; }
            const float s0 = c0 ? (odd ? sC[1] : sC[0]) * sc : p0, s1 = c1 ? (odd ? sC[3] : sC[2]) * sc : p1;
            const float mx = max16(max32(fmaxf(s0, s1)));
            const float e0 = __builtin_amdgcn_exp2f(s0 - mx), e1 = __builtin_amdgcn_exp2f(s1 - mx);
            float sm = e0 + e1;
            sm = add32(sm, sm);
            sm = add16(sm, sm);
            const float ri = __builtin_amdgcn_rcpf(sm);
            const u32 pw = pk(e0 * ri, e1 * ri);
            const u32 w0 = odd ? pw << 16 : pw & 0xffffu, w1 = odd ? pw & 0xffff0000u : pw >> 16;
            const u32x4 pf = {c0 ? w0 : 0u, c1 ? w1 : 0u, c0 ? 0u : w0, c1 ? 0u : w1};
            f32x4 o = mfma(u32x4{vp[m][0], vp[m][1], vp[m - 1][0], vp[m - 1][1]}, pf, z);
            f32x4 r = mfma(u32x4{vr[m][0], vr[m][1], vr[m - 1][0], vr[m - 1][1]}, pf, z);
            om[m] = u32x2{pk(o[0], o[1]), pk(o[2], o[3])};
            orr[m] = u32x2{pk(r[0], r[1]), pk(r[2], r[3])};
            LOADO(m);
        }
        PIN();
        const u32 ob = WS_O + cl * 81920;
        st16<SC1>(u32x4{om[1][0], om[1][1], om[2][0], om[2][1]}, rW, l16, ob + hh * 2048);
        st16<SC1>(u32x4{om[3][0], om[3][1], om[4][0], om[4][1]}, rW, l16 + 1024, ob + hh * 2048);
        if (l < 32) {
            const bool hi = g & 1;
            st16<SC1>(u32x4{hi ? orr[3][0] : orr[1][0], hi ? orr[3][1] : orr[1][1], hi ? orr[4][0] : orr[2][0], hi ? orr[4][1] : orr[2][1]},
                      rW, l16, ob + 65536 + hh * 512);
        }
        PIN();
    }
    STAMP(8);
#pragma unroll
    for (int i = 0; i < 5; ++i) {
        float m = rbf(sum_groups(mq[i]));
        if (w >= 2) m = 1.0f + m;
        lds_at<unsigned short>(lds, L_M2 + (320 * w + 64 * i + 4 * t + g) * 2) = (unsigned short)pk(m, m);
    }
    lds_at<float>(lds, L_G + (64 * w + 4 * t + g) * 4) = rbf(sum_groups(mq[5]));
    VMWAIT(0);
    BAR();
    if (tid == 0) st8<SC1>(tagv, rW, j * 8, WS_FO + cl * 128);
    STAMP(9);
    u32x4 w1[20][3];
    const u32 s1b = WS_W1 + (4 * j + w) * 61440;
#define LOAD1(ks) do { _Pragma("unroll") for (int tp = 0; tp < 3; ++tp) w1[ks][tp] = ld16<SC1>(rW, l16 + tp * 1024, s1b + (ks) * 3072); } while (0)
#pragma unroll
    for (int ks = 0; ks < 5; ++ks) LOAD1(ks);
    PIN();
    if (w == 0) poll_cluster(rW, WS_FO + cl * 128, l, tl, th);
    BAR();
    STAMP(10);

    const bool wodd = w & 1;
    u32 x1p[10];
    {
        u32x4 oa[2][8], ob2[2][2];
        const u32 ob = WS_O + cl * 81920;
#pragma unroll
        for (int pp = 0; pp < 2; ++pp) {
            const int pr = (w >> 1) ^ 1 ^ pp;
#pragma unroll
            for (int q = 0; q < 8; ++q) oa[pp][q] = ld16<SC1>(rW, l16 + (q & 1) * 2048, ob + (8 * w + (q & ~1)) * 2048 + pr * 1024);
#pragma unroll
            for (int e = 0; e < 2; ++e) ob2[pp][e] = ld16<SC1>(rW, g * 1024 + t * 16 + e * 512, ob + 65536 + w * 4096 + pr * 256);
        }
        PIN();
        f32x4 acc[5];
#pragma unroll
        for (int s = 0; s < 4; ++s) {
            const int pp = s >> 1;
            const bool hi = s < 2 ? (s & 1) : (wodd == (bool)(s & 1));
#pragma unroll
            for (int tt = 0; tt < 5; ++tt) acc[tt] = f32x4{0.f, 0.f, 0.f, 0.f};
#pragma unroll
            for (int kk = 0; kk < 5; ++kk) {
                const u32x4 A = kk < 4 ? oa[pp][2 * kk] : ob2[pp][0], B = kk < 4 ? oa[pp][2 * kk + 1] : ob2[pp][1];
                const u32x4 f = {hi ? A[2] : A[0], hi ? A[3] : A[1], hi ? B[2] : B[0], hi ? B[3] : B[1]};
#pragma unroll
                for (int tt = 0; tt < 5; ++tt) acc[tt] = mfma(wo[kk][tt], f, acc[tt]);
            }
            if (s < 3) {
                const int dst = s < 2 ? 2 * ((w >> 1) ^ 1) + s : w ^ 1;
#pragma unroll
                for (int tt = 0; tt < 5; ++tt) lds_at<f32x4>(lds, L_RED + (dst * 3 + (dst ^ w) - 1) * 5120 + tt * 1024 + l16) = acc[tt];
            }
            LOAD1(5 + 2 * s);
            LOAD1(6 + 2 * s);
            PIN();
        }
        BAR();
        STAMP(11);
        const u32 xw[10] = {xa[0], xa[1], xa[2], xa[3], xb[0], xb[1], xb[2], xb[3], xc[0], xc[1]};
        u32 yp[10];
        float su = 0.f, sq2 = 0.f;
#pragma unroll
        for (int tt = 0; tt < 5; ++tt) {
            f32x4 a = acc[tt];
#pragma unroll
            for (int s = 0; s < 3; ++s) a += lds_at<f32x4>(lds, L_RED + (w * 3 + s) * 5120 + tt * 1024 + l16);
            const f32x4 ga = lds_at<f32x4>(lds, L_G + (20 * g + 4 * tt) * 4);
            const u32x2 cb = lds_at<u32x2>(lds, L_M2 + 1280 + (80 * j + 20 * g + 4 * tt) * 2);
#pragma unroll
            for (int q = 0; q < 2; ++q) {
                const u32 xp = pk(blo(xw[2 * tt + q]) + ga[2 * q] * a[2 * q], bhi(xw[2 * tt + q]) + ga[2 * q + 1] * a[2 * q + 1]);
                x1p[2 * tt + q] = xp;
                const float v0 = blo(xp), v1 = bhi(xp);
                su += v0 + v1;
                sq2 += v0 * v0 + v1 * v1;
                yp[2 * tt + q] = pk(v0 * blo(cb[q]), v1 * bhi(cb[q]));
            }
        }
        const u32 yb = WS_Y + cl * 98304;
        st16<SC1>(u32x4{yp[0], yp[1], yp[2], yp[3]}, rW, l16, yb + (w * 16 + 2 * j) * 1024);
        st16<SC1>(u32x4{yp[4], yp[5], yp[6], yp[7]}, rW, l16 + 1024, yb + (w * 16 + 2 * j) * 1024);
        u32 a0 = yp[8], a1 = yp[9], b0 = __float_as_uint(su), b1 = __float_as_uint(sq2);
        swap32(a0, b0);
        swap32(a1, b1);
        st16<SC1>(u32x4{a0, a1, b0, b1}, rW, (l >> 5) * 16384 + (l & 31) * 16, yb + 65536 + (w * 8 + j) * 512);
        PIN();
    }
    STAMP(12);
    VMWAIT(0);
    BAR();
    if (tid == 0) st8<SC1>(tagv, rW, j * 8, WS_FY + cl * 128);
    STAMP(13);
#pragma unroll
    for (int ks = 13; ks < 20; ++ks) LOAD1(ks);
    PIN();
    f32x4 uu[3], vv[3];
    {
        f32x4 uv[3];
#pragma unroll
        for (int tp = 0; tp < 3; ++tp) uv[tp] = f32x4{0.f, 0.f, 0.f, 0.f};
        const u32 mb = L_M2 + ((t & 1) ? 0 : 1280) + 40 * g;
#pragma unroll
        for (int ks = 0; ks < 20; ++ks) {
            const u32 o0 = ks < 16 ? 160 * (ks >> 1) + 16 * (ks & 1) : 320 * (ks - 16) + 32;
            const u32x2 lo = lds_at<u32x2>(lds, mb + o0), hi = lds_at<u32x2>(lds, mb + o0 + (ks < 16 ? 8 : 160));
#pragma unroll
            for (int tp = 0; tp < 3; ++tp) uv[tp] = mfma(w1[ks][tp], u32x4{lo[0], lo[1], hi[0], hi[1]}, uv[tp]);
        }
#pragma unroll
        for (int tp = 0; tp < 3; ++tp)
#pragma unroll
            for (int r = 0; r < 4; ++r) { uu[tp][r] = dpp<0xA0>(uv[tp][r]); vv[tp][r] = dpp<0xF5>(uv[tp][r]); }
    }
    PIN();
    STAMP(14);
    if (w == 0) poll_cluster(rW, WS_FY + cl * 128, l, tl, th);
    BAR();
    STAMP(15);

    u32x4 w2[12][5];
    const u32 s2b = WS_W2 + (j * 48 + 12 * w) * 5120;
#define LOAD2(kk) do { _Pragma("unroll") for (int tt = 0; tt < 5; ++tt) w2[kk][tt] = ld16<SC1>(rW, l16 + (tt & 3) * 1024, s2b + (kk) * 5120 + (tt >> 2) * 4096); } while (0)
    {
        const u32 yb = WS_Y + cl * 98304;
        u32x4 yl[4][5], sl[4];
#pragma unroll
        for (int m = 0; m < 4; ++m) {
#pragma unroll
            for (int i = 0; i < 4; ++i) yl[m][i] = ld16<SC1>(rW, l16 + i * 1024, yb + (m * 16 + 4 * w) * 1024);
            yl[m][4] = ld16<SC1>(rW, l16, yb + 65536 + (m * 8 + 2 * w) * 512);
        }
#pragma unroll
        for (int i = 0; i < 4; ++i) sl[i] = ld16<SC1>(rW, l16 + i * 1024, yb + 81920 + w * 4096);
        PIN();
#define STAGEY(m) do { \
        _Pragma("unroll") for (int i = 0; i < 4; ++i) lds_at<u32x4>(lds, L_H + ((m) * 20 + 4 * w + i) * 1024 + l16) = yl[m][i]; \
        const u32 ra = L_H + ((m) * 20 + 16 + w) * 1024 + (l & 31) * 16 + (l >> 5) * 8; \
        lds_at<u32x2>(lds, ra) = u32x2{yl[m][4][0], yl[m][4][1]}; \
        lds_at<u32x2>(lds, ra + 512) = u32x2{yl[m][4][2], yl[m][4][3]}; } while (0)
        STAGEY(0);
        BAR();
        const u32 hb = WS_H + cl * 196608 + j * 24576;
        f32x4 acc[2][3];
        u32x2 ex[2];
#pragma unroll
        for (int m = 0; m < 5; ++m) {
            f32x2 st;
            u32 hp[6];
            if (m > 0) st = lds_at<f32x2>(lds, L_ST + ((m - 1) * 16 + t) * 8);
            if (m < 4) {
#pragma unroll
                for (int tp = 0; tp < 3; ++tp) acc[m & 1][tp] = f32x4{0.f, 0.f, 0.f, 0.f};
            }
#pragma unroll
            for (int ks = 0; ks < 20; ++ks) {
                if (m < 4) {
                    const u32x4 y = lds_at<u32x4>(lds, L_H + (m * 20 + ks) * 1024 + l16);
#pragma unroll
                    for (int tp = 0; tp < 3; ++tp) acc[m & 1][tp] = mfma(w1[ks][tp], y, acc[m & 1][tp]);
                }
                if (m > 0 && ks >= 2 && ks < 14 && (ks & 1) == 0) {
                    const int q = (ks - 2) >> 1, tp = q >> 1, r = (q & 1) * 2;
                    const f32x4 a = acc[(m - 1) & 1][tp];
                    hp[q] = pk(silu(a[r] * st[0] + (st[1] * uu[tp][r] + vv[tp][r])),
                               silu(a[r + 1] * st[0] + (st[1] * uu[tp][r + 1] + vv[tp][r + 1])));
                }
                if (m == 0 && ks == 6) STAGEY(1);
                if (m == 0 && ks == 11) STAGEY(2);
                if (m == 0 && ks == 16) STAGEY(3);
                if (m >= 1 && m < 4 && ks == 3) LOAD2(2 * m - 2);
                if (m >= 1 && m < 4 && ks == 12) LOAD2(2 * m - 1);
            }
            if (m == 0) {
                float su = 0.f, sq2 = 0.f;
#pragma unroll
                for (int i = 0; i < 4; ++i) {
                    su += __uint_as_float(sl[i][0]) + __uint_as_float(sl[i][2]);
                    sq2 += __uint_as_float(sl[i][1]) + __uint_as_float(sl[i][3]);
                }
                su = add32(su, su); su = add16(su, su);
                sq2 = add32(sq2, sq2); sq2 = add16(sq2, sq2);
                const float mean = su * (1.0f / 640.0f);
                const float rstd = __builtin_amdgcn_rsqf(fmaxf(sq2 * (1.0f / 640.0f) - mean * mean, 0.f) + 1e-5f);
                lds_at<f32x2>(lds, L_ST + (w * 16 + t) * 8) = f32x2{rstd, -mean * rstd};
                BAR();
            } else {
                st16<SC1>(u32x4{hp[0], hp[1], hp[2], hp[3]}, rW, l16, hb + (w * 4 + m - 1) * 1024);
                ex[(m - 1) & 1] = u32x2{hp[4], hp[5]};
                if (((m - 1) & 1) == 1)
                    st16<SC1>(u32x4{ex[0][0], ex[0][1], ex[1][0], ex[1][1]}, rW, l16, hb + 16384 + (w * 2 + ((m - 1) >> 1)) * 1024);
                PIN();
            }
        }
    }
    STAMP(16);
    VMWAIT(0);
    BAR();
    if (tid == 0) st8<SC1>(tagv, rW, j * 8, WS_FH + cl * 128);
    STAMP(17);
#pragma unroll
    for (int kk = 6; kk < 12; ++kk) LOAD2(kk);
    PIN();
    if (w == 0) poll_cluster(rW, WS_FH + cl * 128, l, tl, th);
    BAR();
    STAMP(18);

    {
        const u32 hb = WS_H + cl * 196608 + 2 * w * 24576;
        const int po = (w >> 1) ^ 1;
        u32x4 hm[4][8], he[2][8];
#define LOADHM(s) do { const int mt = (s) < 2 ? 2 * po + (s) : (s) == 2 ? w ^ 1 : w; \
        _Pragma("unroll") for (int q = 0; q < 8; ++q) hm[s][q] = ld16<SC1>(rW, l16, hb + (q >> 2) * 24576 + (q & 3) * 4096 + mt * 1024); } while (0)
#define LOADHE(pp) do { \
        _Pragma("unroll") for (int q = 0; q < 8; ++q) he[pp][q] = ld16<SC1>(rW, l16 + (q & 1) * 2048, hb + (q >> 2) * 24576 + 16384 + (q & 2) * 2048 + (po ^ (pp)) * 1024); } while (0)
        LOADHM(0);
        LOADHE(0);
        LOADHM(1);
        PIN();
        f32x4 acc[5];
#pragma unroll
        for (int s = 0; s < 4; ++s) {
            const int pp = s >> 1;
            const bool hi = s < 2 ? (s & 1) : (wodd == (bool)(s & 1));
#pragma unroll
            for (int tt = 0; tt < 5; ++tt) acc[tt] = f32x4{0.f, 0.f, 0.f, 0.f};
#pragma unroll
            for (int q = 0; q < 8; ++q) {
#pragma unroll
                for (int tt = 0; tt < 5; ++tt) acc[tt] = mfma(w2[6 * (q >> 2) + (q & 3)][tt], hm[s][q], acc[tt]);
            }
            if (s == 0) LOADHM(2);
            if (s == 1) LOADHE(1);
            PIN();
#pragma unroll
            for (int q = 0; q < 4; ++q) {
                const u32x4 A = he[pp][2 * q], B = he[pp][2 * q + 1];
                const u32x4 f = {hi ? A[2] : A[0], hi ? A[3] : A[1], hi ? B[2] : B[0], hi ? B[3] : B[1]};
#pragma unroll
                for (int tt = 0; tt < 5; ++tt) acc[tt] = mfma(w2[6 * (q >> 1) + 4 + (q & 1)][tt], f, acc[tt]);
            }
            if (s == 1) LOADHM(3);
            if (s < 3) {
                const int dst = s < 2 ? 2 * po + s : w ^ 1;
#pragma unroll
                for (int tt = 0; tt < 5; ++tt) lds_at<f32x4>(lds, L_RED + (dst * 3 + (dst ^ w) - 1) * 5120 + tt * 1024 + l16) = acc[tt];
            }
            PIN();
        }
        BAR();
        STAMP(19);
        u32 op[10];
#pragma unroll
        for (int tt = 0; tt < 5; ++tt) {
            f32x4 a = acc[tt];
#pragma unroll
            for (int s = 0; s < 3; ++s) a += lds_at<f32x4>(lds, L_RED + (w * 3 + s) * 5120 + tt * 1024 + l16);
            const f32x4 gb = lds_at<f32x4>(lds, L_G + 512 + (20 * g + 4 * tt) * 4);
#pragma unroll
            for (int q = 0; q < 2; ++q)
                op[2 * tt + q] = pk(blo(x1p[2 * tt + q]) + gb[2 * q] * a[2 * q], bhi(x1p[2 * tt + q]) + gb[2 * q + 1] * a[2 * q + 1]);
        }
        const rsrc_t rO = mkr(pout, 2621440);
        st16<0>(u32x4{op[0], op[1], op[2], op[3]}, rO, vres, 0);
        st16<0>(u32x4{op[4], op[5], op[6], op[7]}, rO, vres + 16, 0);
        st8<0>(u32x2{op[8], op[9]}, rO, vres + 32, 0);
    }
}

extern "C" void kernel_launch(void* const* d_in, const int* in_sizes, int n_in, void* d_out, int out_size, void* d_ws,
                              size_t ws_size, hipStream_t stream) {
    k_block<<<256, 256, 0, stream>>>(d_in[0], d_in[1], d_in[2], d_in[4], d_in[5], d_in[6], d_in[7], d_out, (char*)d_ws);
}
```

```cpp
#include <hip/hip_runtime.h>
#include <stdint.h>

#define NSTAMP 19

__global__ __launch_bounds__(256, 1) void k_block(const void* px, const void* pwq, const void* pwo, const void* pw1,
                                                  const void* pw2, const void* pc, const void* pwm, void* pout, void* ws) {
    __shared__ __attribute__((aligned(16))) char lds[152576];
    asm volatile(
        ".Lbeg:\n"
        "s_load_dwordx16 s[4:19], s[0:1], 0x0\n"
        "s_load_dwordx2 s[20:21], s[0:1], 0x40\n"
        "s_getpc_b64 s[48:49]\n"
        "v_readfirstlane_b32 s52, v0\n"
        "v_and_b32 v2, 63, v0\n"
        "v_lshlrev_b32 v2, 4, v2\n"
        "s_lshr_b32 s52, s52, 6\n"
        "s_and_b32 s49, s49, 0xffff\n"
        "s_mov_b32 s50, .Lend-.Lbeg-32\n"
        "s_mov_b32 s51, 0x20000\n"
        "s_lshr_b32 s54, s2, 3\n"
        "s_lshl_b32 s66, s52, 5\n"
        "s_add_u32 s66, s66, s54\n"
        "s_lshl_b32 s66, s66, 10\n"
        "buffer_load_dwordx4 a[252:255], v2, s[48:51], s66 offen\n"
        "s_cmp_lt_u32 s2, 160\n"
        "s_cbranch_scc0 .Lt1\n"
        "v_lshrrev_b32 v18, 5, v0\n"
        "v_and_b32 v19, 0x1f0, v2\n"
        "v_bfe_u32 v20, v2, 7, 2\n"
        "v_and_b32 v21, 0x70, v2\n"
        "v_mul_u32_u24 v22, 3840, v18\n"
        "v_add_u32 v23, v22, v19\n"
        "s_lshr_b32 s67, s2, 1\n"
        "s_mul_i32 s67, s67, 30720\n"
        "s_and_b32 s71, s2, 1\n"
        "s_mul_i32 s68, s71, 640\n"
        "s_add_u32 s67, s67, s68\n"
        "v_mul_u32_u24 v24, 1280, v20\n"
        "v_add3_u32 v24, v24, v22, v21\n"
        "s_waitcnt lgkmcnt(0)\n"
        "s_mov_b32 s96, s6\n"
        "s_and_b32 s97, s7, 0xffff\n"
        "s_mov_b32 s98, 2457600\n"
        "s_mov_b32 s99, 0x20000\n"
        "buffer_load_dwordx4 a[0:3], v23, s[96:99], s67 offen nt\n"
        "buffer_load_dwordx4 a[4:7], v23, s[96:99], s67 offen offset:1280 nt\n"
        "buffer_load_dwordx4 a[8:11], v23, s[96:99], s67 offen offset:2560 nt\n"
        "buffer_load_dwordx4 a[12:15], v24, s[96:99], s67 offen offset:512 nt\n"
        "s_branch .Le1\n"
        ".Lt1:\n"
        "s_sub_u32 s71, s2, 160\n"
        "s_lshr_b32 s71, s71, 2\n"
        "s_cmp_lt_u32 s2, 240\n"
        "s_cselect_b32 s98, 4915200, 0\n"
        "s_and_b32 s67, s2, 3\n"
        "s_mul_i32 s67, s67, 1228800\n"
        "s_lshl_b32 s68, s71, 7\n"
        "s_add_u32 s69, s67, s68\n"
        "v_lshrrev_b32 v33, 3, v0\n"
        "v_mul_u32_u24 v33, 7680, v33\n"
        "v_and_b32 v28, 0x70, v2\n"
        "v_add_u32 v33, v33, v28\n"
        "s_waitcnt lgkmcnt(0)\n"
        "s_mov_b32 s96, s16\n"
        "s_and_b32 s97, s17, 0xffff\n"
        "s_mov_b32 s99, 0x20000\n"
        "buffer_load_dwordx4 v[36:39], v33, s[96:99], s69 offen nt\n"
        "s_add_u32 s69, s69, 245760\n"
        "buffer_load_dwordx4 v[40:43], v33, s[96:99], s69 offen nt\n"
        "s_add_u32 s69, s69, 245760\n"
        "buffer_load_dwordx4 v[44:47], v33, s[96:99], s69 offen nt\n"
        "s_add_u32 s69, s69, 245760\n"
        "buffer_load_dwordx4 v[48:51], v33, s[96:99], s69 offen nt\n"
        "s_add_u32 s69, s69, 245760\n"
        "buffer_load_dwordx4 v[52:55], v33, s[96:99], s69 offen nt\n"
        ".Le1:\n"
        "s_and_b32 s53, s2, 7\n"
        "s_lshl_b32 s53, s53, 2\n"
        "s_lshr_b32 s69, s2, 6\n"
        "s_add_u32 s53, s53, s69\n"
        "s_and_b32 s54, s54, 7\n"
        "s_mul_i32 s69, s53, 81920\n"
        "s_sub_u32 s69, s69, 20480\n"
        "s_lshl_b32 s66, s54, 2\n"
        "s_add_u32 s66, s66, s52\n"
        "s_mul_i32 s70, s66, 3200\n"
        "s_add_u32 s70, s70, s69\n"
        "v_min_u32 v26, 384, v2\n"
        "v_lshl_add_u32 v26, v26, 3, s70\n"
        "s_mov_b32 s28, s4\n"
        "s_and_b32 s29, s5, 0xffff\n"
        "s_mov_b32 s30, 2621440\n"
        "s_mov_b32 s31, 0x20000\n"
        "s_mov_b32 s44, s14\n"
        "s_and_b32 s45, s15, 0xffff\n"
        "s_mov_b32 s46, 1280\n"
        "s_mov_b32 s47, 0x20000\n"
        "s_and_b32 s67, s2, 3\n"
        "s_mul_i32 s68, s67, 320\n"
        "v_lshrrev_b32 v27, 3, v0\n"
        "v_lshlrev_b32 v17, 1, v27\n"
        "buffer_load_ushort v12, v17, s[44:47], s68 offen\n"
        "buffer_load_ushort v13, v17, s[44:47], s68 offen offset:64\n"
        "buffer_load_ushort v14, v17, s[44:47], s68 offen offset:128\n"
        "buffer_load_ushort v15, v17, s[44:47], s68 offen offset:192\n"
        "buffer_load_ushort v16, v17, s[44:47], s68 offen offset:256\n"
        "s_mov_b32 s24, s20\n"
        "s_and_b32 s25, s21, 0xffff\n"
        "s_mov_b32 s26, 20086784\n"
        "s_mov_b32 s27, 0x20000\n"
        "buffer_load_dwordx2 v[6:7], off, s[24:27], 0 sc1\n"
        "s_mul_i32 s70, s52, 25600\n"
        "s_add_u32 s69, s69, s70\n"
        "v_lshrrev_b32 v10, 8, v2\n"
        "v_and_b32 v11, 0xf0, v2\n"
        "v_mul_u32_u24 v17, 1280, v10\n"
        "v_add3_u32 v56, v17, v11, s69\n"
        "v_add_u32 v64, 5120, v56\n"
        "v_add_u32 v72, 10240, v56\n"
        "v_add_u32 v80, 15360, v56\n"
        "v_add_u32 v88, 20480, v56\n"
        "s_mul_i32 s58, s66, 81920\n"
        "s_add_u32 s58, s58, 131072\n"
        "s_mul_i32 s59, s66, 25600\n"
        "s_add_u32 s59, s59, 2752512\n"
        "s_mul_i32 s60, s66, 61440\n"
        "s_add_u32 s60, s60, 3571712\n"
        "s_mul_i32 s61, s66, 61440\n"
        "s_add_u32 s61, s61, 5537792\n"
        "s_mul_i32 s62, s53, 393216\n"
        "s_add_u32 s62, s62, 7503872\n"
        "s_add_u32 s63, s62, 81920\n"
        "s_add_u32 s64, s62, 180224\n"
        "s_mul_i32 s45, s66, 2560\n"
        "s_add_u32 s45, s45, s62\n"
        "s_mul_i32 s3, s66, 3072\n"
        "s_add_u32 s46, s3, s63\n"
        "s_lshl1_add_u32 s3, s3, s64\n"
        "s_lshl_b32 s65, s53, 7\n"
        "s_mov_b32 s47, 0xffbfffbf\n"
        "s_lshr_b32 s89, s52, 1\n"
        "s_cmp_lt_u32 s2, 96\n"
        "s_cbranch_scc0 .Lt2\n"
        "s_lshr_b32 s66, s2, 1\n"
        "s_and_b32 s67, s2, 1\n"
        "s_mul_i32 s68, s66, 43\n"
        "s_lshr_b32 s68, s68, 8\n"
        "s_mul_i32 s69, s68, 6\n"
        "s_sub_u32 s69, s66, s69\n"
        "s_mul_i32 s70, s69, 48\n"
        "s_mul_i32 s71, s69, 96\n"
        "s_sub_u32 s71, s71, 352\n"
        "s_mul_i32 s72, s89, 40\n"
        "s_add_u32 s71, s71, s72\n"
        "s_cmp_lt_u32 s69, 4\n"
        "s_cselect_b32 s70, s70, s71\n"
        "s_cselect_b32 s72, 16, 8\n"
        "s_mul_i32 s68, s68, 192\n"
        "s_add_u32 s70, s70, s68\n"
        "s_lshl_b32 s71, s52, 2\n"
        "s_add_u32 s70, s70, s71\n"
        "s_lshl_b32 s71, s67, 2\n"
        "s_add_u32 s71, s71, s52\n"
        "s_mul_i32 s71, s71, 48\n"
        "s_add_u32 s71, s71, s66\n"
        "s_mul_i32 s71, s71, 5120\n"
        "s_add_u32 s74, s71, 5537792\n"
        "s_mul_i32 s67, s67, 640\n"
        "s_movk_i32 s73, 1280\n"
        "s_mov_b32 s75, 0\n"
        "s_mov_b32 s36, s12\n"
        "s_and_b32 s37, s13, 0xffff\n"
        "s_mov_b32 s38, 1966080\n"
        "s_mov_b32 s39, 0x20000\n"
        "s_branch .Le2\n"
        ".Lt2:\n"
        "s_cmp_lt_u32 s2, 176\n"
        "s_cbranch_scc0 .Lt3\n"
        "s_sub_u32 s66, s2, 96\n"
        "s_and_b32 s67, s66, 3\n"
        "s_lshr_b32 s66, s66, 2\n"
        "s_lshr_b32 s68, s66, 1\n"
        "s_mul_i32 s68, s68, 80\n"
        "s_and_b32 s69, s66, 1\n"
        "s_lshl_b32 s69, s69, 3\n"
        "s_add_u32 s70, s68, s69\n"
        "s_mul_i32 s71, s66, 160\n"
        "s_sub_u32 s71, s71, 2544\n"
        "s_cmp_lt_u32 s66, 16\n"
        "s_cselect_b32 s70, s70, s71\n"
        "s_cselect_b32 s72, 4, 80\n"
        "s_mul_i32 s71, s52, 20\n"
        "s_add_u32 s70, s70, s71\n"
        "s_lshl_b32 s71, s67, 2\n"
        "s_add_u32 s71, s71, s52\n"
        "s_mul_i32 s71, s71, 40\n"
        "s_add_u32 s71, s71, s66\n"
        "s_mul_i32 s71, s71, 3072\n"
        "s_add_u32 s74, s71, 3571712\n"
        "s_mul_i32 s67, s67, 768\n"
        "s_movk_i32 s73, 3072\n"
        "s_mov_b32 s75, 1\n"
        "s_mov_b32 s36, s10\n"
        "s_and_b32 s37, s11, 0xffff\n"
        "s_mov_b32 s38, 1966080\n"
        "s_mov_b32 s39, 0x20000\n"
        "s_branch .Le3\n"
        ".Lt3:\n"
        "s_sub_u32 s66, s2, 176\n"
        "s_and_b32 s67, s66, 1\n"
        "s_lshr_b32 s66, s66, 1\n"
        "s_mul_i32 s70, s66, 40\n"
        "s_mul_i32 s71, s66, 160\n"
        "s_sub_u32 s71, s71, 2544\n"
        "s_mul_i32 s72, s89, 12\n"
        "s_add_u32 s70, s70, s72\n"
        "s_lshr_b32 s68, s66, 2\n"
        "s_add_u32 s68, s68, s66\n"
        "s_mul_i32 s69, s66, 5\n"
        "s_sub_u32 s69, s69, 76\n"
        "s_cmp_lt_u32 s66, 16\n"
        "s_cselect_b32 s70, s70, s71\n"
        "s_cselect_b32 s68, s68, s69\n"
        "s_cselect_b32 s72, 8, 20\n"
        "s_cselect_b32 s71, 4, 40\n"
        "s_mul_i32 s71, s71, s52\n"
        "s_add_u32 s70, s70, s71\n"
        "s_lshl_b32 s71, s67, 2\n"
        "s_add_u32 s71, s71, s52\n"
        "s_mul_i32 s71, s71, 20\n"
        "s_add_u32 s71, s71, s68\n"
        "s_mul_i32 s71, s71, 5120\n"
        "s_add_u32 s74, s71, 2752512\n"
        "s_mul_i32 s67, s67, 640\n"
        "s_movk_i32 s73, 1280\n"
        "s_cmp_lt_u32 s2, 216\n"
        "s_cselect_b32 s38, 819200, 0\n"
        "s_cselect_b32 s75, 0, 2\n"
        "s_mov_b32 s36, s8\n"
        "s_and_b32 s37, s9, 0xffff\n"
        "s_mov_b32 s39, 0x20000\n"
        ".Le3:\n"
        ".Le2:\n"
        "s_mul_i32 s70, s70, s73\n"
        "s_add_u32 s77, s70, s67\n"
        "v_lshrrev_b32 v31, 9, v2\n"
        "v_bfe_u32 v30, v2, 7, 2\n"
        "v_mad_u32_u24 v31, v31, s72, v30\n"
        "s_cmp_lt_u32 s2, 96\n"
        "s_cselect_b32 s42, 0, 4915200\n"
        "s_mov_b32 s40, s16\n"
        "s_and_b32 s41, s17, 0xffff\n"
        "s_mov_b32 s43, 0x20000\n"
        "s_sub_u32 s88, s2, 16\n"
        "s_lshr_b32 s88, s88, 2\n"
        "s_and_b32 s67, s2, 3\n"
        "s_mul_i32 s67, s67, 1228800\n"
        "s_lshl_b32 s68, s88, 7\n"
        "s_add_u32 s44, s67, s68\n"
        "v_lshrrev_b32 v33, 3, v0\n"
        "v_mul_u32_u24 v33, 7680, v33\n"
        "v_and_b32 v28, 0x70, v2\n"
        "v_add_u32 v33, v33, v28\n"
        "v_mad_u32_u24 v31, v31, s73, v28\n"
        "s_mov_b32 s56, 0xffff0000\n"
        "s_mov_b32 s57, 0x3f803f80\n"
        "s_mov_b32 s55, 0x40000\n"
        "v_add_u32 v3, 4096, v2\n"
        "v_add_u32 v4, 8192, v2\n"
        "v_add_u32 v5, 12288, v2\n"
        "v_lshrrev_b32 v9, 9, v2\n"
        "v_mul_u32_u24 v9, 15872, v9\n"
        "v_add_u32 v9, v9, v2\n"
        "v_mul_u32_u24 v34, 15360, v10\n"
        "v_add_u32 v34, v34, v11\n"
        "s_mul_i32 s87, s52, 1280\n"
        "s_add_u32 s87, s87, 32768\n"
        "v_mul_u32_u24 v32, 800, v27\n"
        "s_lshl_b32 s67, s52, 7\n"
        "s_add_u32 s67, s67, 102400\n"
        "v_add3_u32 v32, v32, v28, s67\n"
        "v_bfe_u32 v35, v2, 4, 2\n"
        "v_bfe_u32 v29, v2, 6, 2\n"
        "v_mul_u32_u24 v29, 800, v29\n"
        "v_mul_u32_u24 v30, 6528, v10\n"
        "s_cmp_eq_u32 s75, 1\n"
        "s_cselect_b32 s68, 8, 40\n"
        "s_cselect_b32 s67, 6, 5\n"
        "s_lshl_b32 s67, s67, 5\n"
        "s_mul_i32 s67, s67, s52\n"
        "s_add_u32 s67, s67, 102400\n"
        "v_mad_u32_u24 v35, v35, s68, v29\n"
        "v_add3_u32 v35, v35, v30, s67\n"
        "s_lshl_b32 s76, s2, 3\n"
        "buffer_load_dword a251, v26, s[28:31], 0 offen nt\n"
        "s_cmp_lt_u32 s2, 160\n"
        "s_cbranch_scc0 .Lt4\n"
        "s_and_b32 s71, s2, 1\n"
        "v_mul_u32_u24 v22, 2592, v18\n"
        "v_add_u32 v22, 102400, v22\n"
        "v_add_u32 v23, v22, v19\n"
        "v_mul_u32_u24 v24, 640, v20\n"
        "v_add3_u32 v24, v24, v22, v21\n"
        "v_bfe_u32 v18, v2, 4, 2\n"
        "v_bfe_u32 v19, v2, 6, 2\n"
        "v_cmp_eq_u32 vcc, 3, v10\n"
        "v_min_u32 v20, 2, v18\n"
        "v_mul_u32_u24 v20, 640, v20\n"
        "v_add_u32 v20, 32, v20\n"
        "v_mul_u32_u24 v21, 640, v10\n"
        "v_lshl_add_u32 v21, v18, 3, v21\n"
        "v_cndmask_b32 v20, v21, v20, vcc\n"
        "s_mul_i32 s67, s52, 160\n"
        "s_add_u32 s67, s67, 102400\n"
        "v_mul_u32_u24 v21, 2592, v19\n"
        "v_add3_u32 v20, v20, v21, s67\n"
        "v_lshlrev_b32 v21, 4, v19\n"
        "v_lshl_add_u32 v21, v18, 6, v21\n"
        "v_cndmask_b32 v21, v11, v21, vcc\n"
        "v_lshl_add_u32 v21, v10, 10, v21\n"
        "s_lshl_b32 s71, s71, 2\n"
        "s_add_u32 s71, s71, s52\n"
        "s_mul_i32 s71, s71, 327680\n"
        "s_lshr_b32 s67, s2, 3\n"
        "s_lshl_b32 s67, s67, 12\n"
        "s_add_u32 s71, s71, s67\n"
        "s_bfe_u32 s67, s2, 0x20001\n"
        "s_lshl_b32 s67, s67, 8\n"
        "s_add_u32 s71, s71, s67\n"
        "s_add_u32 s71, s71, 131072\n"
        "s_waitcnt vmcnt(10)\n"
        "ds_write_b128 v23, a[0:3]\n"
        "s_waitcnt vmcnt(9)\n"
        "ds_write_b128 v23, a[4:7] offset:640\n"
        "s_waitcnt vmcnt(8)\n"
        "ds_write_b128 v23, a[8:11] offset:1280\n"
        "s_waitcnt vmcnt(7)\n"
        "ds_write_b128 v24, a[12:15] offset:512\n"
        "buffer_load_dwordx4 v[60:63], v56, s[28:31], 0 offen\n"
        "buffer_load_dwordx4 v[100:103], v56, s[28:31], 0 offen offset:256\n"
        "buffer_load_dwordx4 v[140:143], v56, s[28:31], 0 offen offset:512\n"
        "buffer_load_dwordx4 v[180:183], v56, s[28:31], 0 offen offset:768\n"
        "buffer_load_dwordx4 v[220:223], v56, s[28:31], 0 offen offset:1024\n"
        "buffer_load_dwordx4 v[68:71], v64, s[28:31], 0 offen\n"
        "buffer_load_dwordx4 v[108:111], v64, s[28:31], 0 offen offset:256\n"
        "buffer_load_dwordx4 v[148:151], v64, s[28:31], 0 offen offset:512\n"
        "buffer_load_dwordx4 v[188:191], v64, s[28:31], 0 offen offset:768\n"
        "buffer_load_dwordx4 v[228:231], v64, s[28:31], 0 offen offset:1024\n"
        "s_waitcnt lgkmcnt(0)\n"
        "s_barrier\n"
        "ds_read_b64_tr_b16 a[16:17], v20\n"
        "ds_read_b64_tr_b16 a[18:19], v20 offset:10368\n"
        "ds_read_b64_tr_b16 a[20:21], v20 offset:40\n"
        "ds_read_b64_tr_b16 a[22:23], v20 offset:10408\n"
        "ds_read_b64_tr_b16 a[24:25], v20 offset:80\n"
        "ds_read_b64_tr_b16 a[26:27], v20 offset:10448\n"
        "ds_read_b64_tr_b16 a[28:29], v20 offset:120\n"
        "ds_read_b64_tr_b16 a[30:31], v20 offset:10488\n"
        "s_waitcnt lgkmcnt(6)\n"
        "buffer_store_dwordx4 a[16:19], v21, s[24:27], s71 offen sc1\n"
        "s_add_u32 s71, s71, 81920\n"
        "s_waitcnt lgkmcnt(4)\n"
        "buffer_store_dwordx4 a[20:23], v21, s[24:27], s71 offen sc1\n"
        "s_add_u32 s71, s71, 81920\n"
        "s_waitcnt lgkmcnt(2)\n"
        "buffer_store_dwordx4 a[24:27], v21, s[24:27], s71 offen sc1\n"
        "s_add_u32 s71, s71, 81920\n"
        "s_waitcnt lgkmcnt(0)\n"
        "buffer_store_dwordx4 a[28:31], v21, s[24:27], s71 offen sc1\n"
        "s_waitcnt vmcnt(20)\n"
        "v_lshlrev_b32 v12, 16, v12\n"
        "s_waitcnt vmcnt(19)\n"
        "v_lshlrev_b32 v13, 16, v13\n"
        "s_waitcnt vmcnt(18)\n"
        "v_lshlrev_b32 v14, 16, v14\n"
        "s_waitcnt vmcnt(17)\n"
        "v_lshlrev_b32 v15, 16, v15\n"
        "s_waitcnt vmcnt(16)\n"
        "v_lshlrev_b32 v16, 16, v16\n"
        "v_mul_f32 v26, 0xbfb8aa3b, v12\n"
        "v_mul_f32 v27, 0xbfb8aa3b, v13\n"
        "v_mul_f32 v28, 0xbfb8aa3b, v14\n"
        "v_mul_f32 v29, 0xbfb8aa3b, v15\n"
        "v_mul_f32 v30, 0xbfb8aa3b, v16\n"
        "v_exp_f32 v26, v26\n"
        "v_exp_f32 v27, v27\n"
        "v_exp_f32 v28, v28\n"
        "v_exp_f32 v29, v29\n"
        "v_exp_f32 v30, v30\n"
        "v_add_f32 v26, 1.0, v26\n"
        "v_add_f32 v27, 1.0, v27\n"
        "v_add_f32 v28, 1.0, v28\n"
        "v_add_f32 v29, 1.0, v29\n"
        "v_add_f32 v30, 1.0, v30\n"
        "v_rcp_f32 v26, v26\n"
        "v_rcp_f32 v27, v27\n"
        "v_rcp_f32 v28, v28\n"
        "v_rcp_f32 v29, v29\n"
        "v_rcp_f32 v30, v30\n"
        "v_mul_f32 v12, v12, v26\n"
        "v_mul_f32 v13, v13, v27\n"
        "v_mul_f32 v14, v14, v28\n"
        "v_mul_f32 v15, v15, v29\n"
        "v_mul_f32 v16, v16, v30\n"
        "v_cvt_pk_bf16_f32 v12, v12, 0\n"
        "v_cvt_pk_bf16_f32 v13, v13, 0\n"
        "v_cvt_pk_bf16_f32 v14, v14, 0\n"
        "v_cvt_pk_bf16_f32 v15, v15, 0\n"
        "v_cvt_pk_bf16_f32 v16, v16, 0\n"
        "s_branch .Le4\n"
        ".Lt4:\n"
        "s_sub_u32 s71, s2, 160\n"
        "s_lshr_b32 s71, s71, 2\n"
        "s_waitcnt vmcnt(6)\n"
        "v_lshlrev_b32 v12, 16, v12\n"
        "s_waitcnt vmcnt(5)\n"
        "v_lshlrev_b32 v13, 16, v13\n"
        "s_waitcnt vmcnt(4)\n"
        "v_lshlrev_b32 v14, 16, v14\n"
        "s_waitcnt vmcnt(3)\n"
        "v_lshlrev_b32 v15, 16, v15\n"
        "s_waitcnt vmcnt(2)\n"
        "v_lshlrev_b32 v16, 16, v16\n"
        "v_mul_f32 v26, 0xbfb8aa3b, v12\n"
        "v_mul_f32 v27, 0xbfb8aa3b, v13\n"
        "v_mul_f32 v28, 0xbfb8aa3b, v14\n"
        "v_mul_f32 v29, 0xbfb8aa3b, v15\n"
        "v_mul_f32 v30, 0xbfb8aa3b, v16\n"
        "v_exp_f32 v26, v26\n"
        "v_exp_f32 v27, v27\n"
        "v_exp_f32 v28, v28\n"
        "v_exp_f32 v29, v29\n"
        "v_exp_f32 v30, v30\n"
        "v_add_f32 v26, 1.0, v26\n"
        "v_add_f32 v27, 1.0, v27\n"
        "v_add_f32 v28, 1.0, v28\n"
        "v_add_f32 v29, 1.0, v29\n"
        "v_add_f32 v30, 1.0, v30\n"
        "v_rcp_f32 v26, v26\n"
        "v_rcp_f32 v27, v27\n"
        "v_rcp_f32 v28, v28\n"
        "v_rcp_f32 v29, v29\n"
        "v_rcp_f32 v30, v30\n"
        "v_mul_f32 v12, v12, v26\n"
        "v_mul_f32 v13, v13, v27\n"
        "v_mul_f32 v14, v14, v28\n"
        "v_mul_f32 v15, v15, v29\n"
        "v_mul_f32 v16, v16, v30\n"
        "v_cvt_pk_bf16_f32 v12, v12, 0\n"
        "v_cvt_pk_bf16_f32 v13, v13, 0\n"
        "v_cvt_pk_bf16_f32 v14, v14, 0\n"
        "v_cvt_pk_bf16_f32 v15, v15, 0\n"
        "v_cvt_pk_bf16_f32 v16, v16, 0\n"
        "buffer_load_dwordx4 v[60:63], v56, s[28:31], 0 offen\n"
        "buffer_load_dwordx4 v[100:103], v56, s[28:31], 0 offen offset:256\n"
        "v_mov_b32 v18, 0\n"
        "v_mov_b32 v19, 0\n"
        "v_mov_b32 v20, 0\n"
        "v_mov_b32 v21, 0\n"
        "v_mov_b32 v22, 0\n"
        "buffer_load_dwordx4 v[140:143], v56, s[28:31], 0 offen offset:512\n"
        "v_mov_b32 v23, 0\n"
        "v_mov_b32 v24, 0\n"
        "v_mov_b32 v25, 0\n"
        "v_lshlrev_b32 v17, 16, v12\n"
        "v_dot2c_f32_bf16 v18, v36, v12\n"
        "v_dot2c_f32_bf16 v19, v36, v17\n"
        "v_dot2c_f32_bf16 v20, v37, v12\n"
        "v_dot2c_f32_bf16 v21, v37, v17\n"
        "buffer_load_dwordx4 v[180:183], v56, s[28:31], 0 offen offset:768\n"
        "v_dot2c_f32_bf16 v22, v38, v12\n"
        "v_dot2c_f32_bf16 v23, v38, v17\n"
        "v_dot2c_f32_bf16 v24, v39, v12\n"
        "v_dot2c_f32_bf16 v25, v39, v17\n"
        "v_lshlrev_b32 v17, 16, v13\n"
        "v_dot2c_f32_bf16 v18, v40, v13\n"
        "v_dot2c_f32_bf16 v19, v40, v17\n"
        "buffer_load_dwordx4 v[220:223], v56, s[28:31], 0 offen offset:1024\n"
        "v_dot2c_f32_bf16 v20, v41, v13\n"
        "v_dot2c_f32_bf16 v21, v41, v17\n"
        "v_dot2c_f32_bf16 v22, v42, v13\n"
        "v_dot2c_f32_bf16 v23, v42, v17\n"
        "v_dot2c_f32_bf16 v24, v43, v13\n"
        "v_dot2c_f32_bf16 v25, v43, v17\n"
        "v_lshlrev_b32 v17, 16, v14\n"
        "v_dot2c_f32_bf16 v18, v44, v14\n"
        "buffer_load_dwordx4 v[68:71], v64, s[28:31], 0 offen\n"
        "v_dot2c_f32_bf16 v19, v44, v17\n"
        "v_dot2c_f32_bf16 v20, v45, v14\n"
        "v_dot2c_f32_bf16 v21, v45, v17\n"
        "v_dot2c_f32_bf16 v22, v46, v14\n"
        "v_dot2c_f32_bf16 v23, v46, v17\n"
        "v_dot2c_f32_bf16 v24, v47, v14\n"
        "v_dot2c_f32_bf16 v25, v47, v17\n"
        "buffer_load_dwordx4 v[108:111], v64, s[28:31], 0 offen offset:256\n"
        "v_lshlrev_b32 v17, 16, v15\n"
        "v_dot2c_f32_bf16 v18, v48, v15\n"
        "v_dot2c_f32_bf16 v19, v48, v17\n"
        "v_dot2c_f32_bf16 v20, v49, v15\n"
        "v_dot2c_f32_bf16 v21, v49, v17\n"
        "v_dot2c_f32_bf16 v22, v50, v15\n"
        "v_dot2c_f32_bf16 v23, v50, v17\n"
        "v_dot2c_f32_bf16 v24, v51, v15\n"
        "buffer_load_dwordx4 v[148:151], v64, s[28:31], 0 offen offset:512\n"
        "v_dot2c_f32_bf16 v25, v51, v17\n"
        "v_lshlrev_b32 v17, 16, v16\n"
        "v_dot2c_f32_bf16 v18, v52, v16\n"
        "v_dot2c_f32_bf16 v19, v52, v17\n"
        "v_dot2c_f32_bf16 v20, v53, v16\n"
        "v_dot2c_f32_bf16 v21, v53, v17\n"
        "v_dot2c_f32_bf16 v22, v54, v16\n"
        "buffer_load_dwordx4 v[188:191], v64, s[28:31], 0 offen offset:768\n"
        "v_dot2c_f32_bf16 v23, v54, v17\n"
        "v_dot2c_f32_bf16 v24, v55, v16\n"
        "v_dot2c_f32_bf16 v25, v55, v17\n"
        "v_and_b32 v26, 0x70, v2\n"
        "v_lshlrev_b32 v26, 1, v26\n"
        "v_lshl_add_u32 v26, v10, 3, v26\n"
        "s_lshl_b32 s67, s52, 8\n"
        "s_add_u32 s67, s67, 129024\n"
        "buffer_load_dwordx4 v[228:231], v64, s[28:31], 0 offen offset:1024\n"
        "v_add_u32 v26, s67, v26\n"
        "v_permlane32_swap_b32 v18, v22\n"
        "v_permlane32_swap_b32 v19, v23\n"
        "v_permlane32_swap_b32 v20, v24\n"
        "v_permlane32_swap_b32 v21, v25\n"
        "v_add_f32 v18, v18, v22\n"
        "v_add_f32 v19, v19, v23\n"
        "v_add_f32 v20, v20, v24\n"
        "v_add_f32 v21, v21, v25\n"
        "s_nop 0\n"
        "v_permlane16_swap_b32 v18, v20\n"
        "v_permlane16_swap_b32 v19, v21\n"
        "v_add_f32 v18, v18, v20\n"
        "v_add_f32 v19, v19, v21\n"
        "s_nop 0\n"
        "v_add_f32_dpp v18, v18, v18 row_ror:8 row_mask:0xf bank_mask:0xf\n"
        "v_add_f32_dpp v19, v19, v19 row_ror:8 row_mask:0xf bank_mask:0xf\n"
        "ds_write_b64 v26, v[18:19]\n"
        "s_waitcnt lgkmcnt(0)\n"
        "s_barrier\n"
        "s_cmp_lt_u32 s2, 240\n"
        "s_cselect_b32 s70, s52, 1\n"
        "s_cmp_eq_u32 s70, 0\n"
        "s_cbranch_scc0 .Lt5\n"
        "v_add_u32 v17, 129024, v11\n"
        "ds_read_b128 v[36:39], v17\n"
        "ds_read_b128 v[40:43], v17 offset:256\n"
        "ds_read_b128 v[44:47], v17 offset:512\n"
        "ds_read_b128 v[48:51], v17 offset:768\n"
        "s_and_b32 s67, s2, 3\n"
        "s_mul_i32 s67, s67, 15360\n"
        "s_lshl_b32 s68, s71, 8\n"
        "s_add_u32 s67, s67, s68\n"
        "s_add_u32 s67, s67, 32768\n"
        "s_waitcnt lgkmcnt(2)\n"
        "v_pk_add_f32 v[36:37], v[36:37], v[40:41]\n"
        "v_pk_add_f32 v[38:39], v[38:39], v[42:43]\n"
        "s_waitcnt lgkmcnt(0)\n"
        "v_pk_add_f32 v[44:45], v[44:45], v[48:49]\n"
        "v_pk_add_f32 v[46:47], v[46:47], v[50:51]\n"
        "v_pk_add_f32 v[36:37], v[36:37], v[44:45]\n"
        "v_pk_add_f32 v[38:39], v[38:39], v[46:47]\n"
        "s_mov_b64 exec, 0xffff\n"
        "buffer_store_dwordx4 v[36:39], v11, s[24:27], s67 offen sc1\n"
        "s_mov_b64 exec, -1\n"
        ".Lt5:\n"
        ".Le4:\n"
        "s_waitcnt vmcnt(11)\n"
        "v_add_u32 v6, 0x7f4a7c15, v6\n"
        "v_add_u32 v7, 0x9e3779b9, v7\n"
        "buffer_load_dwordx4 v[76:79], v72, s[28:31], 0 offen\n"
        "v_mov_b32 v18, 0\n"
        "v_mov_b32 v19, 0\n"
        "v_mov_b32 v20, 0\n"
        "v_mov_b32 v21, 0\n"
        "s_waitcnt vmcnt(10)\n"
        "v_dot2c_f32_bf16 v18, s57, v60\n"
        "v_dot2c_f32_bf16 v19, v60, v60\n"
        "v_dot2c_f32_bf16 v20, s57, v61\n"
        "v_dot2c_f32_bf16 v21, v61, v61\n"
        "v_dot2c_f32_bf16 v18, s57, v62\n"
        "v_dot2c_f32_bf16 v19, v62, v62\n"
        "v_dot2c_f32_bf16 v20, s57, v63\n"
        "v_dot2c_f32_bf16 v21, v63, v63\n"
        "s_waitcnt vmcnt(9)\n"
        "v_dot2c_f32_bf16 v18, s57, v100\n"
        "v_dot2c_f32_bf16 v19, v100, v100\n"
        "v_dot2c_f32_bf16 v20, s57, v101\n"
        "buffer_load_dwordx4 v[116:119], v72, s[28:31], 0 offen offset:256\n"
        "v_dot2c_f32_bf16 v21, v101, v101\n"
        "v_dot2c_f32_bf16 v18, s57, v102\n"
        "v_dot2c_f32_bf16 v19, v102, v102\n"
        "v_dot2c_f32_bf16 v20, s57, v103\n"
        "v_dot2c_f32_bf16 v21, v103, v103\n"
        "s_waitcnt vmcnt(9)\n"
        "v_dot2c_f32_bf16 v18, s57, v140\n"
        "v_dot2c_f32_bf16 v19, v140, v140\n"
        "v_dot2c_f32_bf16 v20, s57, v141\n"
        "v_dot2c_f32_bf16 v21, v141, v141\n"
        "v_dot2c_f32_bf16 v18, s57, v142\n"
        "v_dot2c_f32_bf16 v19, v142, v142\n"
        "v_dot2c_f32_bf16 v20, s57, v143\n"
        "v_dot2c_f32_bf16 v21, v143, v143\n"
        "s_waitcnt vmcnt(8)\n"
        "v_dot2c_f32_bf16 v18, s57, v180\n"
        "v_dot2c_f32_bf16 v19, v180, v180\n"
        "buffer_load_dwordx4 v[156:159], v72, s[28:31], 0 offen offset:512\n"
        "v_dot2c_f32_bf16 v20, s57, v181\n"
        "v_dot2c_f32_bf16 v21, v181, v181\n"
        "v_dot2c_f32_bf16 v18, s57, v182\n"
        "v_dot2c_f32_bf16 v19, v182, v182\n"
        "v_dot2c_f32_bf16 v20, s57, v183\n"
        "v_dot2c_f32_bf16 v21, v183, v183\n"
        "s_waitcnt vmcnt(8)\n"
        "v_dot2c_f32_bf16 v18, s57, v220\n"
        "v_dot2c_f32_bf16 v19, v220, v220\n"
        "v_dot2c_f32_bf16 v20, s57, v221\n"
        "v_dot2c_f32_bf16 v21, v221, v221\n"
        "v_dot2c_f32_bf16 v18, s57, v222\n"
        "v_dot2c_f32_bf16 v19, v222, v222\n"
        "v_dot2c_f32_bf16 v20, s57, v223\n"
        "v_dot2c_f32_bf16 v21, v223, v223\n"
        "v_lshlrev_b32 v56, 16, v60\n"
        "buffer_load_dwordx4 v[196:199], v72, s[28:31], 0 offen offset:768\n"
        "v_and_b32 v57, s56, v60\n"
        "v_lshlrev_b32 v58, 16, v61\n"
        "v_and_b32 v59, s56, v61\n"
        "v_lshlrev_b32 v60, 16, v62\n"
        "v_pk_add_f32 v[18:19], v[18:19], v[20:21]\n"
        "v_and_b32 v61, s56, v62\n"
        "v_lshlrev_b32 v62, 16, v63\n"
        "v_add_f32_dpp v18, v18, v18 row_ror:8 row_mask:0xf bank_mask:0xf\n"
        "v_and_b32 v63, s56, v63\n"
        "v_lshlrev_b32 v96, 16, v100\n"
        "v_add_f32_dpp v19, v19, v19 row_ror:8 row_mask:0xf bank_mask:0xf\n"
        "v_and_b32 v97, s56, v100\n"
        "v_lshlrev_b32 v98, 16, v101\n"
        "v_add_f32_dpp v18, v18, v18 row_ror:4 row_mask:0xf bank_mask:0xf\n"
        "v_and_b32 v99, s56, v101\n"
        "buffer_load_dwordx4 v[236:239], v72, s[28:31], 0 offen offset:1024\n"
        "v_lshlrev_b32 v100, 16, v102\n"
        "v_add_f32_dpp v19, v19, v19 row_ror:4 row_mask:0xf bank_mask:0xf\n"
        "v_and_b32 v101, s56, v102\n"
        "v_lshlrev_b32 v102, 16, v103\n"
        "v_add_f32_dpp v18, v18, v18 row_ror:2 row_mask:0xf bank_mask:0xf\n"
        "v_and_b32 v103, s56, v103\n"
        "v_lshlrev_b32 v136, 16, v140\n"
        "v_add_f32_dpp v19, v19, v19 row_ror:2 row_mask:0xf bank_mask:0xf\n"
        "v_and_b32 v137, s56, v140\n"
        "v_lshlrev_b32 v138, 16, v141\n"
        "v_add_f32_dpp v18, v18, v18 row_ror:1 row_mask:0xf bank_mask:0xf\n"
        "v_and_b32 v139, s56, v141\n"
        "v_lshlrev_b32 v140, 16, v142\n"
        "v_add_f32_dpp v19, v19, v19 row_ror:1 row_mask:0xf bank_mask:0xf\n"
        "v_and_b32 v141, s56, v142\n"
        "buffer_load_dwordx4 v[84:87], v80, s[28:31], 0 offen\n"
        "v_lshlrev_b32 v142, 16, v143\n"
        "v_mul_f32 v20, 0x3acccccd, v18\n"
        "v_and_b32 v143, s56, v143\n"
        "v_lshlrev_b32 v176, 16, v180\n"
        "v_mul_f32 v19, 0x3acccccd, v19\n"
        "v_and_b32 v177, s56, v180\n"
        "v_lshlrev_b32 v178, 16, v181\n"
        "v_fma_f32 v19, -v20, v20, v19\n"
        "v_and_b32 v179, s56, v181\n"
        "v_lshlrev_b32 v180, 16, v182\n"
        "v_max_f32 v19, 0, v19\n"
        "v_and_b32 v181, s56, v182\n"
        "v_lshlrev_b32 v182, 16, v183\n"
        "v_add_f32 v19, 0x3727c5ac, v19\n"
        "v_and_b32 v183, s56, v183\n"
        "buffer_load_dwordx4 v[124:127], v80, s[28:31], 0 offen offset:256\n"
        "v_lshlrev_b32 v216, 16, v220\n"
        "v_rsq_f32 v18, v19\n"
        "v_and_b32 v217, s56, v220\n"
        "v_lshlrev_b32 v218, 16, v221\n"
        "v_mul_f32_e64 v19, -v20, v18\n"
        "v_and_b32 v219, s56, v221\n"
        "v_lshlrev_b32 v220, 16, v222\n"
        "v_and_b32 v221, s56, v222\n"
        "v_lshlrev_b32 v222, 16, v223\n"
        "v_and_b32 v223, s56, v223\n"
        "v_pk_fma_f32 v[56:57], v[56:57], v[18:19], v[18:19] op_sel:[0,0,1] op_sel_hi:[1,0,1]\n"
        "v_pk_fma_f32 v[58:59], v[58:59], v[18:19], v[18:19] op_sel:[0,0,1] op_sel_hi:[1,0,1]\n"
        "v_pk_fma_f32 v[60:61], v[60:61], v[18:19], v[18:19] op_sel:[0,0,1] op_sel_hi:[1,0,1]\n"
        "v_pk_fma_f32 v[62:63], v[62:63], v[18:19], v[18:19] op_sel:[0,0,1] op_sel_hi:[1,0,1]\n"
        "buffer_load_dwordx4 v[164:167], v80, s[28:31], 0 offen offset:512\n"
        "v_pk_fma_f32 v[96:97], v[96:97], v[18:19], v[18:19] op_sel:[0,0,1] op_sel_hi:[1,0,1]\n"
        "v_pk_fma_f32 v[98:99], v[98:99], v[18:19], v[18:19] op_sel:[0,0,1] op_sel_hi:[1,0,1]\n"
        "v_pk_fma_f32 v[100:101], v[100:101], v[18:19], v[18:19] op_sel:[0,0,1] op_sel_hi:[1,0,1]\n"
        "v_pk_fma_f32 v[102:103], v[102:103], v[18:19], v[18:19] op_sel:[0,0,1] op_sel_hi:[1,0,1]\n"
        "v_pk_fma_f32 v[136:137], v[136:137], v[18:19], v[18:19] op_sel:[0,0,1] op_sel_hi:[1,0,1]\n"
        "v_pk_fma_f32 v[138:139], v[138:139], v[18:19], v[18:19] op_sel:[0,0,1] op_sel_hi:[1,0,1]\n"
        "v_pk_fma_f32 v[140:141], v[140:141], v[18:19], v[18:19] op_sel:[0,0,1] op_sel_hi:[1,0,1]\n"
        "v_pk_fma_f32 v[142:143], v[142:143], v[18:19], v[18:19] op_sel:[0,0,1] op_sel_hi:[1,0,1]\n"
        "v_pk_fma_f32 v[176:177], v[176:177], v[18:19], v[18:19] op_sel:[0,0,1] op_sel_hi:[1,0,1]\n"
        "v_pk_fma_f32 v[178:179], v[178:179], v[18:19], v[18:19] op_sel:[0,0,1] op_sel_hi:[1,0,1]\n"
        "v_pk_fma_f32 v[180:181], v[180:181], v[18:19], v[18:19] op_sel:[0,0,1] op_sel_hi:[1,0,1]\n"
        "v_pk_fma_f32 v[182:183], v[182:183], v[18:19], v[18:19] op_sel:[0,0,1] op_sel_hi:[1,0,1]\n"
        "v_pk_fma_f32 v[216:217], v[216:217], v[18:19], v[18:19] op_sel:[0,0,1] op_sel_hi:[1,0,1]\n"
        "v_pk_fma_f32 v[218:219], v[218:219], v[18:19], v[18:19] op_sel:[0,0,1] op_sel_hi:[1,0,1]\n"
        "v_pk_fma_f32 v[220:221], v[220:221], v[18:19], v[18:19] op_sel:[0,0,1] op_sel_hi:[1,0,1]\n"
        "buffer_load_dwordx4 v[204:207], v80, s[28:31], 0 offen offset:768\n"
        "v_pk_fma_f32 v[222:223], v[222:223], v[18:19], v[18:19] op_sel:[0,0,1] op_sel_hi:[1,0,1]\n"
        "buffer_load_dwordx4 v[244:247], v80, s[28:31], 0 offen offset:1024\n"
        "buffer_load_dwordx4 v[92:95], v88, s[28:31], 0 offen\n"
        "buffer_load_dwordx4 v[132:135], v88, s[28:31], 0 offen offset:256\n"
        "buffer_load_dwordx4 v[172:175], v88, s[28:31], 0 offen offset:512\n"
        "buffer_load_dwordx4 v[212:215], v88, s[28:31], 0 offen offset:768\n"
        "buffer_load_dwordx4 v[252:255], v88, s[28:31], 0 offen offset:1024\n"
        "s_waitcnt vmcnt(15)\n"
        "s_barrier\n"
        "s_add_u32 s89, s76, 4096\n"
        "s_cmp_eq_u32 s52, 1\n"
        "s_cbranch_scc0 .Lt6\n"
        "s_mov_b64 exec, 1\n"
        "buffer_store_dwordx2 v[6:7], off, s[24:27], s89 sc1\n"
        "s_mov_b64 exec, -1\n"
        ".Lt6:\n"
        "s_cmp_eq_u32 s52, 1\n"
        "s_cbranch_scc0 .Lt7\n"
        "v_min_u32 v26, 176, v2\n"
        "v_mul_u32_u24 v26, 2048, v26\n"
        "buffer_load_dword a252, v26, s[24:27], s62 offen sc1\n"
        "v_min_u32 v26, 65536, v26\n"
        "s_mul_i32 s89, s53, 81920\n"
        "s_mov_b32 s48, s18\n"
        "s_and_b32 s49, s19, 0xffff\n"
        "s_mov_b32 s50, 2621440\n"
        "s_mov_b32 s51, 0x20000\n"
        "buffer_load_dword a253, v26, s[48:51], s89 offen sc1\n"
        ".Lt7:\n"
        "buffer_load_dwordx4 a[216:219], v31, s[36:39], s77 offen nt\n"
        "v_mov_b32 v18, 0\n"
        "v_mov_b32 v19, 0\n"
        "v_mov_b32 v20, 0\n"
        "v_mov_b32 v21, 0\n"
        "v_dot2c_f32_bf16 v18, s57, v68\n"
        "v_dot2c_f32_bf16 v19, v68, v68\n"
        "v_dot2c_f32_bf16 v20, s57, v69\n"
        "v_dot2c_f32_bf16 v21, v69, v69\n"
        "v_dot2c_f32_bf16 v18, s57, v70\n"
        "v_dot2c_f32_bf16 v19, v70, v70\n"
        "buffer_load_dwordx4 a[220:223], v31, s[36:39], s77 offen offset:128 nt\n"
        "v_dot2c_f32_bf16 v20, s57, v71\n"
        "v_dot2c_f32_bf16 v21, v71, v71\n"
        "v_dot2c_f32_bf16 v18, s57, v108\n"
        "v_dot2c_f32_bf16 v19, v108, v108\n"
        "v_dot2c_f32_bf16 v20, s57, v109\n"
        "v_dot2c_f32_bf16 v21, v109, v109\n"
        "v_dot2c_f32_bf16 v18, s57, v110\n"
        "v_dot2c_f32_bf16 v19, v110, v110\n"
        "v_dot2c_f32_bf16 v20, s57, v111\n"
        "v_dot2c_f32_bf16 v21, v111, v111\n"
        "buffer_load_dwordx4 a[224:227], v31, s[36:39], s77 offen offset:256 nt\n"
        "v_dot2c_f32_bf16 v18, s57, v148\n"
        "v_dot2c_f32_bf16 v19, v148, v148\n"
        "v_dot2c_f32_bf16 v20, s57, v149\n"
        "v_dot2c_f32_bf16 v21, v149, v149\n"
        "v_dot2c_f32_bf16 v18, s57, v150\n"
        "v_dot2c_f32_bf16 v19, v150, v150\n"
        "v_dot2c_f32_bf16 v20, s57, v151\n"
        "v_dot2c_f32_bf16 v21, v151, v151\n"
        "v_dot2c_f32_bf16 v18, s57, v188\n"
        "v_dot2c_f32_bf16 v19, v188, v188\n"
        "buffer_load_dwordx4 a[228:231], v31, s[36:39], s77 offen offset:384 nt\n"
        "v_dot2c_f32_bf16 v20, s57, v189\n"
        "v_dot2c_f32_bf16 v21, v189, v189\n"
        "v_dot2c_f32_bf16 v18, s57, v190\n"
        "v_dot2c_f32_bf16 v19, v190, v190\n"
        "v_dot2c_f32_bf16 v20, s57, v191\n"
        "v_dot2c_f32_bf16 v21, v191, v191\n"
        "v_dot2c_f32_bf16 v18, s57, v228\n"
        "v_dot2c_f32_bf16 v19, v228, v228\n"
        "v_dot2c_f32_bf16 v20, s57, v229\n"
        "v_dot2c_f32_bf16 v21, v229, v229\n"
        "buffer_load_dwordx4 a[232:235], v31, s[36:39], s77 offen offset:512 nt\n"
        "v_dot2c_f32_bf16 v18, s57, v230\n"
        "v_dot2c_f32_bf16 v19, v230, v230\n"
        "v_dot2c_f32_bf16 v20, s57, v231\n"
        "v_dot2c_f32_bf16 v21, v231, v231\n"
        "v_mov_b32 v22, 0\n"
        "v_mov_b32 v23, 0\n"
        "v_mov_b32 v24, 0\n"
        "v_mov_b32 v25, 0\n"
        "s_waitcnt vmcnt(19)\n"
        "v_dot2c_f32_bf16 v22, s57, v76\n"
        "v_dot2c_f32_bf16 v23, v76, v76\n"
        "s_cmp_eq_u32 s75, 1\n"
        "s_cselect_b32 s38, s38, 0\n"
        "buffer_load_dwordx4 a[236:239], v31, s[36:39], s77 offen offset:640 nt\n"
        "v_dot2c_f32_bf16 v24, s57, v77\n"
        "v_dot2c_f32_bf16 v25, v77, v77\n"
        "v_dot2c_f32_bf16 v22, s57, v78\n"
        "v_dot2c_f32_bf16 v23, v78, v78\n"
        "v_dot2c_f32_bf16 v24, s57, v79\n"
        "v_dot2c_f32_bf16 v25, v79, v79\n"
        "s_waitcnt vmcnt(19)\n"
        "v_dot2c_f32_bf16 v22, s57, v116\n"
        "v_dot2c_f32_bf16 v23, v116, v116\n"
        "v_dot2c_f32_bf16 v24, s57, v117\n"
        "v_dot2c_f32_bf16 v25, v117, v117\n"
        "buffer_load_dwordx4 a[196:199], v33, s[40:43], s44 offen nt\n"
        "v_dot2c_f32_bf16 v22, s57, v118\n"
        "v_dot2c_f32_bf16 v23, v118, v118\n"
        "v_dot2c_f32_bf16 v24, s57, v119\n"
        "v_dot2c_f32_bf16 v25, v119, v119\n"
        "s_waitcnt vmcnt(19)\n"
        "v_dot2c_f32_bf16 v22, s57, v156\n"
        "v_dot2c_f32_bf16 v23, v156, v156\n"
        "v_dot2c_f32_bf16 v24, s57, v157\n"
        "v_dot2c_f32_bf16 v25, v157, v157\n"
        "v_dot2c_f32_bf16 v22, s57, v158\n"
        "v_dot2c_f32_bf16 v23, v158, v158\n"
        "s_add_u32 s44, s44, 245760\n"
        "buffer_load_dwordx4 a[200:203], v33, s[40:43], s44 offen nt\n"
        "v_dot2c_f32_bf16 v24, s57, v159\n"
        "v_dot2c_f32_bf16 v25, v159, v159\n"
        "s_waitcnt vmcnt(19)\n"
        "v_dot2c_f32_bf16 v22, s57, v196\n"
        "v_dot2c_f32_bf16 v23, v196, v196\n"
        "v_dot2c_f32_bf16 v24, s57, v197\n"
        "v_dot2c_f32_bf16 v25, v197, v197\n"
        "v_dot2c_f32_bf16 v22, s57, v198\n"
        "v_dot2c_f32_bf16 v23, v198, v198\n"
        "v_dot2c_f32_bf16 v24, s57, v199\n"
        "v_dot2c_f32_bf16 v25, v199, v199\n"
        "s_add_u32 s44, s44, 245760\n"
        "buffer_load_dwordx4 a[204:207], v33, s[40:43], s44 offen nt\n"
        "s_waitcnt vmcnt(19)\n"
        "v_dot2c_f32_bf16 v22, s57, v236\n"
        "v_dot2c_f32_bf16 v23, v236, v236\n"
        "v_dot2c_f32_bf16 v24, s57, v237\n"
        "v_dot2c_f32_bf16 v25, v237, v237\n"
        "v_dot2c_f32_bf16 v22, s57, v238\n"
        "v_dot2c_f32_bf16 v23, v238, v238\n"
        "v_dot2c_f32_bf16 v24, s57, v239\n"
        "v_dot2c_f32_bf16 v25, v239, v239\n"
        "v_lshlrev_b32 v64, 16, v68\n"
        "v_and_b32 v65, s56, v68\n"
        "s_add_u32 s44, s44, 245760\n"
        "buffer_load_dwordx4 a[208:211], v33, s[40:43], s44 offen nt\n"
        "v_lshlrev_b32 v66, 16, v69\n"
        "v_and_b32 v67, s56, v69\n"
        "v_lshlrev_b32 v68, 16, v70\n"
        "v_pk_add_f32 v[18:19], v[18:19], v[20:21]\n"
        "v_and_b32 v69, s56, v70\n"
        "v_lshlrev_b32 v70, 16, v71\n"
        "v_pk_add_f32 v[22:23], v[22:23], v[24:25]\n"
        "v_and_b32 v71, s56, v71\n"
        "v_lshlrev_b32 v104, 16, v108\n"
        "v_add_f32_dpp v18, v18, v18 row_ror:8 row_mask:0xf bank_mask:0xf\n"
        "s_add_u32 s44, s44, 245760\n"
        "buffer_load_dwordx4 a[212:215], v33, s[40:43], s44 offen nt\n"
        "v_and_b32 v105, s56, v108\n"
        "v_lshlrev_b32 v106, 16, v109\n"
        "v_add_f32_dpp v19, v19, v19 row_ror:8 row_mask:0xf bank_mask:0xf\n"
        "v_and_b32 v107, s56, v109\n"
        "v_lshlrev_b32 v108, 16, v110\n"
        "v_add_f32_dpp v22, v22, v22 row_ror:8 row_mask:0xf bank_mask:0xf\n"
        "v_and_b32 v109, s56, v110\n"
        "v_lshlrev_b32 v110, 16, v111\n"
        "v_add_f32_dpp v23, v23, v23 row_ror:8 row_mask:0xf bank_mask:0xf\n"
        "v_and_b32 v111, s56, v111\n"
        "v_lshlrev_b32 v144, 16, v148\n"
        "v_add_f32_dpp v18, v18, v18 row_ror:4 row_mask:0xf bank_mask:0xf\n"
        "v_and_b32 v145, s56, v148\n"
        "v_lshlrev_b32 v146, 16, v149\n"
        "v_add_f32_dpp v19, v19, v19 row_ror:4 row_mask:0xf bank_mask:0xf\n"
        "v_and_b32 v147, s56, v149\n"
        "v_lshlrev_b32 v148, 16, v150\n"
        "v_add_f32_dpp v22, v22, v22 row_ror:4 row_mask:0xf bank_mask:0xf\n"
        "v_and_b32 v149, s56, v150\n"
        "v_lshlrev_b32 v150, 16, v151\n"
        "v_add_f32_dpp v23, v23, v23 row_ror:4 row_mask:0xf bank_mask:0xf\n"
        "v_and_b32 v151, s56, v151\n"
        "v_lshlrev_b32 v184, 16, v188\n"
        "v_add_f32_dpp v18, v18, v18 row_ror:2 row_mask:0xf bank_mask:0xf\n"
        "v_and_b32 v185, s56, v188\n"
        "v_lshlrev_b32 v186, 16, v189\n"
        "v_add_f32_dpp v19, v19, v19 row_ror:2 row_mask:0xf bank_mask:0xf\n"
        "v_and_b32 v187, s56, v189\n"
        "v_lshlrev_b32 v188, 16, v190\n"
        "v_add_f32_dpp v22, v22, v22 row_ror:2 row_mask:0xf bank_mask:0xf\n"
        "v_and_b32 v189, s56, v190\n"
        "v_lshlrev_b32 v190, 16, v191\n"
        "v_add_f32_dpp v23, v23, v23 row_ror:2 row_mask:0xf bank_mask:0xf\n"
        "v_and_b32 v191, s56, v191\n"
        "v_lshlrev_b32 v224, 16, v228\n"
        "v_add_f32_dpp v18, v18, v18 row_ror:1 row_mask:0xf bank_mask:0xf\n"
        "v_and_b32 v225, s56, v228\n"
        "v_lshlrev_b32 v226, 16, v229\n"
        "v_add_f32_dpp v19, v19, v19 row_ror:1 row_mask:0xf bank_mask:0xf\n"
        "v_and_b32 v227, s56, v229\n"
        "v_lshlrev_b32 v228, 16, v230\n"
        "v_add_f32_dpp v22, v22, v22 row_ror:1 row_mask:0xf bank_mask:0xf\n"
        "v_and_b32 v229, s56, v230\n"
        "v_lshlrev_b32 v230, 16, v231\n"
        "v_add_f32_dpp v23, v23, v23 row_ror:1 row_mask:0xf bank_mask:0xf\n"
        "v_and_b32 v231, s56, v231\n"
        "v_lshlrev_b32 v72, 16, v76\n"
        "v_mul_f32 v20, 0x3acccccd, v18\n"
        "v_and_b32 v73, s56, v76\n"
        "v_lshlrev_b32 v74, 16, v77\n"
        "v_mul_f32 v19, 0x3acccccd, v19\n"
        "v_and_b32 v75, s56, v77\n"
        "v_lshlrev_b32 v76, 16, v78\n"
        "v_mul_f32 v24, 0x3acccccd, v22\n"
        "v_and_b32 v77, s56, v78\n"
        "v_lshlrev_b32 v78, 16, v79\n"
        "v_mul_f32 v23, 0x3acccccd, v23\n"
        "v_and_b32 v79, s56, v79\n"
        "v_lshlrev_b32 v112, 16, v116\n"
        "v_fma_f32 v19, -v20, v20, v19\n"
        "v_and_b32 v113, s56, v116\n"
        "v_lshlrev_b32 v114, 16, v117\n"
        "v_fma_f32 v23, -v24, v24, v23\n"
        "v_and_b32 v115, s56, v117\n"
        "v_lshlrev_b32 v116, 16, v118\n"
        "v_max_f32 v19, 0, v19\n"
        "v_and_b32 v117, s56, v118\n"
        "v_lshlrev_b32 v118, 16, v119\n"
        "v_max_f32 v23, 0, v23\n"
        "v_and_b32 v119, s56, v119\n"
        "v_lshlrev_b32 v152, 16, v156\n"
        "v_add_f32 v19, 0x3727c5ac, v19\n"
        "v_and_b32 v153, s56, v156\n"
        "v_lshlrev_b32 v154, 16, v157\n"
        "v_add_f32 v23, 0x3727c5ac, v23\n"
        "v_and_b32 v155, s56, v157\n"
        "v_lshlrev_b32 v156, 16, v158\n"
        "v_rsq_f32 v18, v19\n"
        "v_and_b32 v157, s56, v158\n"
        "v_lshlrev_b32 v158, 16, v159\n"
        "v_rsq_f32 v22, v23\n"
        "v_and_b32 v159, s56, v159\n"
        "v_lshlrev_b32 v192, 16, v196\n"
        "v_mul_f32_e64 v19, -v20, v18\n"
        "v_and_b32 v193, s56, v196\n"
        "v_lshlrev_b32 v194, 16, v197\n"
        "v_mul_f32_e64 v23, -v24, v22\n"
        "v_and_b32 v195, s56, v197\n"
        "v_lshlrev_b32 v196, 16, v198\n"
        "v_and_b32 v197, s56, v198\n"
        "v_lshlrev_b32 v198, 16, v199\n"
        "v_and_b32 v199, s56, v199\n"
        "v_lshlrev_b32 v232, 16, v236\n"
        "v_and_b32 v233, s56, v236\n"
        "v_lshlrev_b32 v234, 16, v237\n"
        "v_and_b32 v235, s56, v237\n"
        "v_lshlrev_b32 v236, 16, v238\n"
        "v_and_b32 v237, s56, v238\n"
        "v_lshlrev_b32 v238, 16, v239\n"
        "v_and_b32 v239, s56, v239\n"
        "v_pk_fma_f32 v[64:65], v[64:65], v[18:19], v[18:19] op_sel:[0,0,1] op_sel_hi:[1,0,1]\n"
        "v_pk_fma_f32 v[66:67], v[66:67], v[18:19], v[18:19] op_sel:[0,0,1] op_sel_hi:[1,0,1]\n"
        "v_pk_fma_f32 v[68:69], v[68:69], v[18:19], v[18:19] op_sel:[0,0,1] op_sel_hi:[1,0,1]\n"
        "v_pk_fma_f32 v[70:71], v[70:71], v[18:19], v[18:19] op_sel:[0,0,1] op_sel_hi:[1,0,1]\n"
        "v_pk_fma_f32 v[104:105], v[104:105], v[18:19], v[18:19] op_sel:[0,0,1] op_sel_hi:[1,0,1]\n"
        "v_pk_fma_f32 v[106:107], v[106:107], v[18:19], v[18:19] op_sel:[0,0,1] op_sel_hi:[1,0,1]\n"
        "v_pk_fma_f32 v[108:109], v[108:109], v[18:19], v[18:19] op_sel:[0,0,1] op_sel_hi:[1,0,1]\n"
        "v_pk_fma_f32 v[110:111], v[110:111], v[18:19], v[18:19] op_sel:[0,0,1] op_sel_hi:[1,0,1]\n"
        "v_pk_fma_f32 v[144:145], v[144:145], v[18:19], v[18:19] op_sel:[0,0,1] op_sel_hi:[1,0,1]\n"
        "v_pk_fma_f32 v[146:147], v[146:147], v[18:19], v[18:19] op_sel:[0,0,1] op_sel_hi:[1,0,1]\n"
        "v_pk_fma_f32 v[148:149], v[148:149], v[18:19], v[18:19] op_sel:[0,0,1] op_sel_hi:[1,0,1]\n"
        "v_pk_fma_f32 v[150:151], v[150:151], v[18:19], v[18:19] op_sel:[0,0,1] op_sel_hi:[1,0,1]\n"
        "v_pk_fma_f32 v[184:185], v[184:185], v[18:19], v[18:19] op_sel:[0,0,1] op_sel_hi:[1,0,1]\n"
        "v_pk_fma_f32 v[186:187], v[186:187], v[18:19], v[18:19] op_sel:[0,0,1] op_sel_hi:[1,0,1]\n"
        "v_pk_fma_f32 v[188:189], v[188:189], v[18:19], v[18:19] op_sel:[0,0,1] op_sel_hi:[1,0,1]\n"
        "v_pk_fma_f32 v[190:191], v[190:191], v[18:19], v[18:19] op_sel:[0,0,1] op_sel_hi:[1,0,1]\n"
        "v_pk_fma_f32 v[224:225], v[224:225], v[18:19], v[18:19] op_sel:[0,0,1] op_sel_hi:[1,0,1]\n"
        "v_pk_fma_f32 v[226:227], v[226:227], v[18:19], v[18:19] op_sel:[0,0,1] op_sel_hi:[1,0,1]\n"
        "v_pk_fma_f32 v[228:229], v[228:229], v[18:19], v[18:19] op_sel:[0,0,1] op_sel_hi:[1,0,1]\n"
        "v_pk_fma_f32 v[230:231], v[230:231], v[18:19], v[18:19] op_sel:[0,0,1] op_sel_hi:[1,0,1]\n"
        "v_pk_fma_f32 v[72:73], v[72:73], v[22:23], v[22:23] op_sel:[0,0,1] op_sel_hi:[1,0,1]\n"
        "v_pk_fma_f32 v[74:75], v[74:75], v[22:23], v[22:23] op_sel:[0,0,1] op_sel_hi:[1,0,1]\n"
        "v_pk_fma_f32 v[76:77], v[76:77], v[22:23], v[22:23] op_sel:[0,0,1] op_sel_hi:[1,0,1]\n"
        "v_pk_fma_f32 v[78:79], v[78:79], v[22:23], v[22:23] op_sel:[0,0,1] op_sel_hi:[1,0,1]\n"
        "v_pk_fma_f32 v[112:113], v[112:113], v[22:23], v[22:23] op_sel:[0,0,1] op_sel_hi:[1,0,1]\n"
        "v_pk_fma_f32 v[114:115], v[114:115], v[22:23], v[22:23] op_sel:[0,0,1] op_sel_hi:[1,0,1]\n"
        "v_pk_fma_f32 v[116:117], v[116:117], v[22:23], v[22:23] op_sel:[0,0,1] op_sel_hi:[1,0,1]\n"
        "v_pk_fma_f32 v[118:119], v[118:119], v[22:23], v[22:23] op_sel:[0,0,1] op_sel_hi:[1,0,1]\n"
        "v_pk_fma_f32 v[152:153], v[152:153], v[22:23], v[22:23] op_sel:[0,0,1] op_sel_hi:[1,0,1]\n"
        "v_pk_fma_f32 v[154:155], v[154:155], v[22:23], v[22:23] op_sel:[0,0,1] op_sel_hi:[1,0,1]\n"
        "v_pk_fma_f32 v[156:157], v[156:157], v[22:23], v[22:23] op_sel:[0,0,1] op_sel_hi:[1,0,1]\n"
        "v_pk_fma_f32 v[158:159], v[158:159], v[22:23], v[22:23] op_sel:[0,0,1] op_sel_hi:[1,0,1]\n"
        "v_pk_fma_f32 v[192:193], v[192:193], v[22:23], v[22:23] op_sel:[0,0,1] op_sel_hi:[1,0,1]\n"
        "v_pk_fma_f32 v[194:195], v[194:195], v[22:23], v[22:23] op_sel:[0,0,1] op_sel_hi:[1,0,1]\n"
        "v_pk_fma_f32 v[196:197], v[196:197], v[22:23], v[22:23] op_sel:[0,0,1] op_sel_hi:[1,0,1]\n"
        "v_pk_fma_f32 v[198:199], v[198:199], v[22:23], v[22:23] op_sel:[0,0,1] op_sel_hi:[1,0,1]\n"
        "v_pk_fma_f32 v[232:233], v[232:233], v[22:23], v[22:23] op_sel:[0,0,1] op_sel_hi:[1,0,1]\n"
        "v_pk_fma_f32 v[234:235], v[234:235], v[22:23], v[22:23] op_sel:[0,0,1] op_sel_hi:[1,0,1]\n"
        "v_pk_fma_f32 v[236:237], v[236:237], v[22:23], v[22:23] op_sel:[0,0,1] op_sel_hi:[1,0,1]\n"
        "v_pk_fma_f32 v[238:239], v[238:239], v[22:23], v[22:23] op_sel:[0,0,1] op_sel_hi:[1,0,1]\n"
        "v_mov_b32 v18, 0\n"
        "v_mov_b32 v19, 0\n"
        "v_mov_b32 v20, 0\n"
        "v_mov_b32 v21, 0\n"
        "s_waitcnt vmcnt(20)\n"
        "v_dot2c_f32_bf16 v18, s57, v84\n"
        "v_dot2c_f32_bf16 v19, v84, v84\n"
        "v_dot2c_f32_bf16 v20, s57, v85\n"
        "v_dot2c_f32_bf16 v21, v85, v85\n"
        "v_dot2c_f32_bf16 v18, s57, v86\n"
        "v_dot2c_f32_bf16 v19, v86, v86\n"
        "v_dot2c_f32_bf16 v20, s57, v87\n"
        "v_dot2c_f32_bf16 v21, v87, v87\n"
        "s_waitcnt vmcnt(19)\n"
        "v_dot2c_f32_bf16 v18, s57, v124\n"
        "v_dot2c_f32_bf16 v19, v124, v124\n"
        "v_dot2c_f32_bf16 v20, s57, v125\n"
        "v_dot2c_f32_bf16 v21, v125, v125\n"
        "v_dot2c_f32_bf16 v18, s57, v126\n"
        "v_dot2c_f32_bf16 v19, v126, v126\n"
        "v_dot2c_f32_bf16 v20, s57, v127\n"
        "v_dot2c_f32_bf16 v21, v127, v127\n"
        "s_waitcnt vmcnt(18)\n"
        "v_dot2c_f32_bf16 v18, s57, v164\n"
        "v_dot2c_f32_bf16 v19, v164, v164\n"
        "v_dot2c_f32_bf16 v20, s57, v165\n"
        "v_dot2c_f32_bf16 v21, v165, v165\n"
        "v_dot2c_f32_bf16 v18, s57, v166\n"
        "v_dot2c_f32_bf16 v19, v166, v166\n"
        "v_dot2c_f32_bf16 v20, s57, v167\n"
        "v_dot2c_f32_bf16 v21, v167, v167\n"
        "s_waitcnt vmcnt(17)\n"
        "v_dot2c_f32_bf16 v18, s57, v204\n"
        "v_dot2c_f32_bf16 v19, v204, v204\n"
        "v_dot2c_f32_bf16 v20, s57, v205\n"
        "v_dot2c_f32_bf16 v21, v205, v205\n"
        "v_dot2c_f32_bf16 v18, s57, v206\n"
        "v_dot2c_f32_bf16 v19, v206, v206\n"
        "v_dot2c_f32_bf16 v20, s57, v207\n"
        "v_dot2c_f32_bf16 v21, v207, v207\n"
        "s_waitcnt vmcnt(16)\n"
        "v_dot2c_f32_bf16 v18, s57, v244\n"
        "v_dot2c_f32_bf16 v19, v244, v244\n"
        "v_dot2c_f32_bf16 v20, s57, v245\n"
        "v_dot2c_f32_bf16 v21, v245, v245\n"
        "v_dot2c_f32_bf16 v18, s57, v246\n"
        "v_dot2c_f32_bf16 v19, v246, v246\n"
        "v_dot2c_f32_bf16 v20, s57, v247\n"
        "v_dot2c_f32_bf16 v21, v247, v247\n"
        "v_mov_b32 v22, 0\n"
        "v_mov_b32 v23, 0\n"
        "v_mov_b32 v24, 0\n"
        "v_mov_b32 v25, 0\n"
        "s_waitcnt vmcnt(15)\n"
        "v_dot2c_f32_bf16 v22, s57, v92\n"
        "v_dot2c_f32_bf16 v23, v92, v92\n"
        "v_dot2c_f32_bf16 v24, s57, v93\n"
        "v_dot2c_f32_bf16 v25, v93, v93\n"
        "v_dot2c_f32_bf16 v22, s57, v94\n"
        "v_dot2c_f32_bf16 v23, v94, v94\n"
        "v_dot2c_f32_bf16 v24, s57, v95\n"
        "v_dot2c_f32_bf16 v25, v95, v95\n"
        "s_waitcnt vmcnt(14)\n"
        "v_dot2c_f32_bf16 v22, s57, v132\n"
        "v_dot2c_f32_bf16 v23, v132, v132\n"
        "v_dot2c_f32_bf16 v24, s57, v133\n"
        "v_dot2c_f32_bf16 v25, v133, v133\n"
        "v_dot2c_f32_bf16 v22, s57, v134\n"
        "v_dot2c_f32_bf16 v23, v134, v134\n"
        "v_dot2c_f32_bf16 v24, s57, v135\n"
        "v_dot2c_f32_bf16 v25, v135, v135\n"
        "s_waitcnt vmcnt(13)\n"
        "v_dot2c_f32_bf16 v22, s57, v172\n"
        "v_dot2c_f32_bf16 v23, v172, v172\n"
        "v_dot2c_f32_bf16 v24, s57, v173\n"
        "v_dot2c_f32_bf16 v25, v173, v173\n"
        "v_dot2c_f32_bf16 v22, s57, v174\n"
        "v_dot2c_f32_bf16 v23, v174, v174\n"
        "v_dot2c_f32_bf16 v24, s57, v175\n"
        "v_dot2c_f32_bf16 v25, v175, v175\n"
        "s_waitcnt vmcnt(12)\n"
        "v_dot2c_f32_bf16 v22, s57, v212\n"
        "v_dot2c_f32_bf16 v23, v212, v212\n"
        "v_dot2c_f32_bf16 v24, s57, v213\n"
        "v_dot2c_f32_bf16 v25, v213, v213\n"
        "v_dot2c_f32_bf16 v22, s57, v214\n"
        "v_dot2c_f32_bf16 v23, v214, v214\n"
        "v_dot2c_f32_bf16 v24, s57, v215\n"
        "v_dot2c_f32_bf16 v25, v215, v215\n"
        "s_waitcnt vmcnt(11)\n"
        "v_dot2c_f32_bf16 v22, s57, v252\n"
        "v_dot2c_f32_bf16 v23, v252, v252\n"
        "v_dot2c_f32_bf16 v24, s57, v253\n"
        "v_dot2c_f32_bf16 v25, v253, v253\n"
        "v_dot2c_f32_bf16 v22, s57, v254\n"
        "v_dot2c_f32_bf16 v23, v254, v254\n"
        "v_dot2c_f32_bf16 v24, s57, v255\n"
        "v_dot2c_f32_bf16 v25, v255, v255\n"
        "v_lshlrev_b32 v80, 16, v84\n"
        "v_and_b32 v81, s56, v84\n"
        "v_lshlrev_b32 v82, 16, v85\n"
        "v_and_b32 v83, s56, v85\n"
        "v_lshlrev_b32 v84, 16, v86\n"
        "v_pk_add_f32 v[18:19], v[18:19], v[20:21]\n"
        "v_and_b32 v85, s56, v86\n"
        "v_lshlrev_b32 v86, 16, v87\n"
        "v_pk_add_f32 v[22:23], v[22:23], v[24:25]\n"
        "v_and_b32 v87, s56, v87\n"
        "v_lshlrev_b32 v120, 16, v124\n"
        "v_add_f32_dpp v18, v18, v18 row_ror:8 row_mask:0xf bank_mask:0xf\n"
        "v_and_b32 v121, s56, v124\n"
        "v_lshlrev_b32 v122, 16, v125\n"
        "v_add_f32_dpp v19, v19, v19 row_ror:8 row_mask:0xf bank_mask:0xf\n"
        "v_and_b32 v123, s56, v125\n"
        "v_lshlrev_b32 v124, 16, v126\n"
        "v_add_f32_dpp v22, v22, v22 row_ror:8 row_mask:0xf bank_mask:0xf\n"
        "v_and_b32 v125, s56, v126\n"
        "v_lshlrev_b32 v126, 16, v127\n"
        "v_add_f32_dpp v23, v23, v23 row_ror:8 row_mask:0xf bank_mask:0xf\n"
        "v_and_b32 v127, s56, v127\n"
        "v_lshlrev_b32 v160, 16, v164\n"
        "v_add_f32_dpp v18, v18, v18 row_ror:4 row_mask:0xf bank_mask:0xf\n"
        "v_and_b32 v161, s56, v164\n"
        "v_lshlrev_b32 v162, 16, v165\n"
        "v_add_f32_dpp v19, v19, v19 row_ror:4 row_mask:0xf bank_mask:0xf\n"
        "v_and_b32 v163, s56, v165\n"
        "v_lshlrev_b32 v164, 16, v166\n"
        "v_add_f32_dpp v22, v22, v22 row_ror:4 row_mask:0xf bank_mask:0xf\n"
        "v_and_b32 v165, s56, v166\n"
        "v_lshlrev_b32 v166, 16, v167\n"
        "v_add_f32_dpp v23, v23, v23 row_ror:4 row_mask:0xf bank_mask:0xf\n"
        "v_and_b32 v167, s56, v167\n"
        "v_lshlrev_b32 v200, 16, v204\n"
        "v_add_f32_dpp v18, v18, v18 row_ror:2 row_mask:0xf bank_mask:0xf\n"
        "v_and_b32 v201, s56, v204\n"
        "v_lshlrev_b32 v202, 16, v205\n"
        "v_add_f32_dpp v19, v19, v19 row_ror:2 row_mask:0xf bank_mask:0xf\n"
        "v_and_b32 v203, s56, v205\n"
        "v_lshlrev_b32 v204, 16, v206\n"
        "v_add_f32_dpp v22, v22, v22 row_ror:2 row_mask:0xf bank_mask:0xf\n"
        "v_and_b32 v205, s56, v206\n"
        "v_lshlrev_b32 v206, 16, v207\n"
        "v_add_f32_dpp v23, v23, v23 row_ror:2 row_mask:0xf bank_mask:0xf\n"
        "v_and_b32 v207, s56, v207\n"
        "v_lshlrev_b32 v240, 16, v244\n"
        "v_add_f32_dpp v18, v18, v18 row_ror:1 row_mask:0xf bank_mask:0xf\n"
        "v_and_b32 v241, s56, v244\n"
        "v_lshlrev_b32 v242, 16, v245\n"
        "v_add_f32_dpp v19, v19, v19 row_ror:1 row_mask:0xf bank_mask:0xf\n"
        "v_and_b32 v243, s56, v245\n"
        "v_lshlrev_b32 v244, 16, v246\n"
        "v_add_f32_dpp v22, v22, v22 row_ror:1 row_mask:0xf bank_mask:0xf\n"
        "v_and_b32 v245, s56, v246\n"
        "v_lshlrev_b32 v246, 16, v247\n"
        "v_add_f32_dpp v23, v23, v23 row_ror:1 row_mask:0xf bank_mask:0xf\n"
        "v_and_b32 v247, s56, v247\n"
        "v_lshlrev_b32 v88, 16, v92\n"
        "v_mul_f32 v20, 0x3acccccd, v18\n"
        "v_and_b32 v89, s56, v92\n"
        "v_lshlrev_b32 v90, 16, v93\n"
        "v_mul_f32 v19, 0x3acccccd, v19\n"
        "v_and_b32 v91, s56, v93\n"
        "v_lshlrev_b32 v92, 16, v94\n"
        "v_mul_f32 v24, 0x3acccccd, v22\n"
        "v_and_b32 v93, s56, v94\n"
        "v_lshlrev_b32 v94, 16, v95\n"
        "v_mul_f32 v23, 0x3acccccd, v23\n"
        "v_and_b32 v95, s56, v95\n"
        "v_lshlrev_b32 v128, 16, v132\n"
        "v_fma_f32 v19, -v20, v20, v19\n"
        "v_and_b32 v129, s56, v132\n"
        "v_lshlrev_b32 v130, 16, v133\n"
        "v_fma_f32 v23, -v24, v24, v23\n"
        "v_and_b32 v131, s56, v133\n"
        "v_lshlrev_b32 v132, 16, v134\n"
        "v_max_f32 v19, 0, v19\n"
        "v_and_b32 v133, s56, v134\n"
        "v_lshlrev_b32 v134, 16, v135\n"
        "v_max_f32 v23, 0, v23\n"
        "v_and_b32 v135, s56, v135\n"
        "v_lshlrev_b32 v168, 16, v172\n"
        "v_add_f32 v19, 0x3727c5ac, v19\n"
        "v_and_b32 v169, s56, v172\n"
        "v_lshlrev_b32 v170, 16, v173\n"
        "v_add_f32 v23, 0x3727c5ac, v23\n"
        "v_and_b32 v171, s56, v173\n"
        "v_lshlrev_b32 v172, 16, v174\n"
        "v_rsq_f32 v18, v19\n"
        "v_and_b32 v173, s56, v174\n"
        "v_lshlrev_b32 v174, 16, v175\n"
        "v_rsq_f32 v22, v23\n"
        "v_and_b32 v175, s56, v175\n"
        "v_lshlrev_b32 v208, 16, v212\n"
        "v_mul_f32_e64 v19, -v20, v18\n"
        "v_and_b32 v209, s56, v212\n"
        "v_lshlrev_b32 v210, 16, v213\n"
        "v_mul_f32_e64 v23, -v24, v22\n"
        "v_and_b32 v211, s56, v213\n"
        "v_lshlrev_b32 v212, 16, v214\n"
        "v_and_b32 v213, s56, v214\n"
        "v_lshlrev_b32 v214, 16, v215\n"
        "v_and_b32 v215, s56, v215\n"
        "v_lshlrev_b32 v248, 16, v252\n"
        "v_and_b32 v249, s56, v252\n"
        "v_lshlrev_b32 v250, 16, v253\n"
        "v_and_b32 v251, s56, v253\n"
        "v_lshlrev_b32 v252, 16, v254\n"
        "v_and_b32 v253, s56, v254\n"
        "v_lshlrev_b32 v254, 16, v255\n"
        "v_and_b32 v255, s56, v255\n"
        "v_pk_fma_f32 v[80:81], v[80:81], v[18:19], v[18:19] op_sel:[0,0,1] op_sel_hi:[1,0,1]\n"
        "v_pk_fma_f32 v[82:83], v[82:83], v[18:19], v[18:19] op_sel:[0,0,1] op_sel_hi:[1,0,1]\n"
        "v_pk_fma_f32 v[84:85], v[84:85], v[18:19], v[18:19] op_sel:[0,0,1] op_sel_hi:[1,0,1]\n"
        "v_pk_fma_f32 v[86:87], v[86:87], v[18:19], v[18:19] op_sel:[0,0,1] op_sel_hi:[1,0,1]\n"
        "v_pk_fma_f32 v[120:121], v[120:121], v[18:19], v[18:19] op_sel:[0,0,1] op_sel_hi:[1,0,1]\n"
        "v_pk_fma_f32 v[122:123], v[122:123], v[18:19], v[18:19] op_sel:[0,0,1] op_sel_hi:[1,0,1]\n"
        "v_pk_fma_f32 v[124:125], v[124:125], v[18:19], v[18:19] op_sel:[0,0,1] op_sel_hi:[1,0,1]\n"
        "v_pk_fma_f32 v[126:127], v[126:127], v[18:19], v[18:19] op_sel:[0,0,1] op_sel_hi:[1,0,1]\n"
        "v_pk_fma_f32 v[160:161], v[160:161], v[18:19], v[18:19] op_sel:[0,0,1] op_sel_hi:[1,0,1]\n"
        "v_pk_fma_f32 v[162:163], v[162:163], v[18:19], v[18:19] op_sel:[0,0,1] op_sel_hi:[1,0,1]\n"
        "v_pk_fma_f32 v[164:165], v[164:165], v[18:19], v[18:19] op_sel:[0,0,1] op_sel_hi:[1,0,1]\n"
        "v_pk_fma_f32 v[166:167], v[166:167], v[18:19], v[18:19] op_sel:[0,0,1] op_sel_hi:[1,0,1]\n"
        "v_pk_fma_f32 v[200:201], v[200:201], v[18:19], v[18:19] op_sel:[0,0,1] op_sel_hi:[1,0,1]\n"
        "v_pk_fma_f32 v[202:203], v[202:203], v[18:19], v[18:19] op_sel:[0,0,1] op_sel_hi:[1,0,1]\n"
        "v_pk_fma_f32 v[204:205], v[204:205], v[18:19], v[18:19] op_sel:[0,0,1] op_sel_hi:[1,0,1]\n"
        "v_pk_fma_f32 v[206:207], v[206:207], v[18:19], v[18:19] op_sel:[0,0,1] op_sel_hi:[1,0,1]\n"
        "v_pk_fma_f32 v[240:241], v[240:241], v[18:19], v[18:19] op_sel:[0,0,1] op_sel_hi:[1,0,1]\n"
        "v_pk_fma_f32 v[242:243], v[242:243], v[18:19], v[18:19] op_sel:[0,0,1] op_sel_hi:[1,0,1]\n"
        "v_pk_fma_f32 v[244:245], v[244:245], v[18:19], v[18:19] op_sel:[0,0,1] op_sel_hi:[1,0,1]\n"
        "v_pk_fma_f32 v[246:247], v[246:247], v[18:19], v[18:19] op_sel:[0,0,1] op_sel_hi:[1,0,1]\n"
        "v_pk_fma_f32 v[88:89], v[88:89], v[22:23], v[22:23] op_sel:[0,0,1] op_sel_hi:[1,0,1]\n"
        "v_pk_fma_f32 v[90:91], v[90:91], v[22:23], v[22:23] op_sel:[0,0,1] op_sel_hi:[1,0,1]\n"
        "v_pk_fma_f32 v[92:93], v[92:93], v[22:23], v[22:23] op_sel:[0,0,1] op_sel_hi:[1,0,1]\n"
        "v_pk_fma_f32 v[94:95], v[94:95], v[22:23], v[22:23] op_sel:[0,0,1] op_sel_hi:[1,0,1]\n"
        "v_pk_fma_f32 v[128:129], v[128:129], v[22:23], v[22:23] op_sel:[0,0,1] op_sel_hi:[1,0,1]\n"
        "v_pk_fma_f32 v[130:131], v[130:131], v[22:23], v[22:23] op_sel:[0,0,1] op_sel_hi:[1,0,1]\n"
        "v_pk_fma_f32 v[132:133], v[132:133], v[22:23], v[22:23] op_sel:[0,0,1] op_sel_hi:[1,0,1]\n"
        "v_pk_fma_f32 v[134:135], v[134:135], v[22:23], v[22:23] op_sel:[0,0,1] op_sel_hi:[1,0,1]\n"
        "v_pk_fma_f32 v[168:169], v[168:169], v[22:23], v[22:23] op_sel:[0,0,1] op_sel_hi:[1,0,1]\n"
        "v_pk_fma_f32 v[170:171], v[170:171], v[22:23], v[22:23] op_sel:[0,0,1] op_sel_hi:[1,0,1]\n"
        "v_pk_fma_f32 v[172:173], v[172:173], v[22:23], v[22:23] op_sel:[0,0,1] op_sel_hi:[1,0,1]\n"
        "v_pk_fma_f32 v[174:175], v[174:175], v[22:23], v[22:23] op_sel:[0,0,1] op_sel_hi:[1,0,1]\n"
        "v_pk_fma_f32 v[208:209], v[208:209], v[22:23], v[22:23] op_sel:[0,0,1] op_sel_hi:[1,0,1]\n"
        "v_pk_fma_f32 v[210:211], v[210:211], v[22:23], v[22:23] op_sel:[0,0,1] op_sel_hi:[1,0,1]\n"
        "v_pk_fma_f32 v[212:213], v[212:213], v[22:23], v[22:23] op_sel:[0,0,1] op_sel_hi:[1,0,1]\n"
        "v_pk_fma_f32 v[214:215], v[214:215], v[22:23], v[22:23] op_sel:[0,0,1] op_sel_hi:[1,0,1]\n"
        "v_pk_fma_f32 v[248:249], v[248:249], v[22:23], v[22:23] op_sel:[0,0,1] op_sel_hi:[1,0,1]\n"
        "v_pk_fma_f32 v[250:251], v[250:251], v[22:23], v[22:23] op_sel:[0,0,1] op_sel_hi:[1,0,1]\n"
        "v_pk_fma_f32 v[252:253], v[252:253], v[22:23], v[22:23] op_sel:[0,0,1] op_sel_hi:[1,0,1]\n"
        "v_pk_fma_f32 v[254:255], v[254:255], v[22:23], v[22:23] op_sel:[0,0,1] op_sel_hi:[1,0,1]\n"
        "s_cmp_eq_u32 s52, 0\n"
        "s_cbranch_scc0 .Lt8\n"
        ".Lp1:\n"
        "s_mov_b32 s89, 4096\n"
        "buffer_load_dwordx4 v[18:21], v2, s[24:27], s89 offen sc1\n"
        "buffer_load_dwordx4 v[22:25], v2, s[24:27], s89 offen offset:1024 sc1\n"
        "s_waitcnt vmcnt(0)\n"
        "v_cmp_ne_u64_e64 s[66:67], v[18:19], v[6:7]\n"
        "v_cmp_ne_u64_e64 s[68:69], v[20:21], v[6:7]\n"
        "v_cmp_ne_u64_e64 s[70:71], v[22:23], v[6:7]\n"
        "v_cmp_ne_u64_e64 s[72:73], v[24:25], v[6:7]\n"
        "s_or_b64 s[66:67], s[66:67], s[68:69]\n"
        "s_or_b64 s[66:67], s[66:67], s[70:71]\n"
        "s_or_b64 s[66:67], s[66:67], s[72:73]\n"
        "s_cmp_eq_u64 s[66:67], 0\n"
        "s_cbranch_scc1 .Lq1\n"
        "s_sub_u32 s55, s55, 1\n"
        "s_cbranch_scc0 .Lp1\n"
        "s_mov_b32 s55, 0\n"
        ".Lq1:\n"
        ".Lt8:\n"
        "s_barrier\n"
        "buffer_load_dwordx4 v[36:39], v34, s[24:27], s87 offen sc1\n"
        "buffer_load_dwordx4 v[40:43], v34, s[24:27], s87 offen offset:256 sc1\n"
        "buffer_load_dwordx4 v[44:47], v34, s[24:27], s87 offen offset:512 sc1\n"
        "buffer_load_dwordx4 v[48:51], v34, s[24:27], s87 offen offset:768 sc1\n"
        "buffer_load_dwordx4 v[52:55], v34, s[24:27], s87 offen offset:1024 sc1\n"
        "buffer_load_dwordx4 a[0:3], v2, s[24:27], s58 offen sc1\n"
        "buffer_load_dwordx4 a[4:7], v2, s[24:27], s58 offen offset:1024 sc1\n"
        "buffer_load_dwordx4 a[8:11], v2, s[24:27], s58 offen offset:2048 sc1\n"
        "buffer_load_dwordx4 a[12:15], v2, s[24:27], s58 offen offset:3072 sc1\n"
        "buffer_load_dwordx4 a[16:19], v3, s[24:27], s58 offen sc1\n"
        "buffer_load_dwordx4 a[20:23], v3, s[24:27], s58 offen offset:1024 sc1\n"
        "buffer_load_dwordx4 a[24:27], v3, s[24:27], s58 offen offset:2048 sc1\n"
        "buffer_load_dwordx4 a[28:31], v3, s[24:27], s58 offen offset:3072 sc1\n"
        "s_cmp_eq_u32 s2, 0\n"
        "s_cbranch_scc0 .Lt9\n"
        "s_mov_b32 s89, 0\n"
        "s_cmp_eq_u32 s52, 2\n"
        "s_cbranch_scc0 .Lt10\n"
        "s_mov_b64 exec, 1\n"
        "buffer_store_dwordx2 v[6:7], off, s[24:27], s89 sc1\n"
        "s_mov_b64 exec, -1\n"
        ".Lt10:\n"
        ".Lt9:\n"
        "s_waitcnt vmcnt(23)\n"
        "ds_write_b128 v32, a[216:219]\n"
        "s_waitcnt vmcnt(22)\n"
        "ds_write_b128 v32, a[220:223] offset:128\n"
        "s_waitcnt vmcnt(21)\n"
        "ds_write_b128 v32, a[224:227] offset:256\n"
        "s_waitcnt vmcnt(20)\n"
        "ds_write_b128 v32, a[228:231] offset:384\n"
        "s_waitcnt vmcnt(19)\n"
        "ds_write_b128 v32, a[232:235] offset:512\n"
        "s_waitcnt vmcnt(18)\n"
        "ds_write_b128 v32, a[236:239] offset:640\n"
        "v_mov_b32 v18, 0\n"
        "v_mov_b32 v19, 0\n"
        "v_mov_b32 v20, 0\n"
        "v_mov_b32 v21, 0\n"
        "v_mov_b32 v22, 0\n"
        "v_mov_b32 v23, 0\n"
        "v_mov_b32 v24, 0\n"
        "v_mov_b32 v25, 0\n"
        "s_waitcnt vmcnt(17)\n"
        "v_accvgpr_read_b32 v28, a196\n"
        "v_accvgpr_read_b32 v29, a197\n"
        "v_accvgpr_read_b32 v30, a198\n"
        "v_accvgpr_read_b32 v31, a199\n"
        "v_lshlrev_b32 v17, 16, v12\n"
        "v_dot2c_f32_bf16 v18, v28, v12\n"
        "v_dot2c_f32_bf16 v19, v28, v17\n"
        "v_dot2c_f32_bf16 v20, v29, v12\n"
        "v_dot2c_f32_bf16 v21, v29, v17\n"
        "v_dot2c_f32_bf16 v22, v30, v12\n"
        "v_dot2c_f32_bf16 v23, v30, v17\n"
        "v_dot2c_f32_bf16 v24, v31, v12\n"
        "v_dot2c_f32_bf16 v25, v31, v17\n"
        "s_waitcnt vmcnt(16)\n"
        "v_accvgpr_read_b32 v28, a200\n"
        "v_accvgpr_read_b32 v29, a201\n"
        "v_accvgpr_read_b32 v30, a202\n"
        "v_accvgpr_read_b32 v31, a203\n"
        "v_lshlrev_b32 v17, 16, v13\n"
        "v_dot2c_f32_bf16 v18, v28, v13\n"
        "v_dot2c_f32_bf16 v19, v28, v17\n"
        "v_dot2c_f32_bf16 v20, v29, v13\n"
        "v_dot2c_f32_bf16 v21, v29, v17\n"
        "v_dot2c_f32_bf16 v22, v30, v13\n"
        "v_dot2c_f32_bf16 v23, v30, v17\n"
        "v_dot2c_f32_bf16 v24, v31, v13\n"
        "v_dot2c_f32_bf16 v25, v31, v17\n"
        "s_waitcnt vmcnt(15)\n"
        "v_accvgpr_read_b32 v28, a204\n"
        "v_accvgpr_read_b32 v29, a205\n"
        "v_accvgpr_read_b32 v30, a206\n"
        "v_accvgpr_read_b32 v31, a207\n"
        "v_lshlrev_b32 v17, 16, v14\n"
        "v_dot2c_f32_bf16 v18, v28, v14\n"
        "v_dot2c_f32_bf16 v19, v28, v17\n"
        "v_dot2c_f32_bf16 v20, v29, v14\n"
        "v_dot2c_f32_bf16 v21, v29, v17\n"
        "v_dot2c_f32_bf16 v22, v30, v14\n"
        "v_dot2c_f32_bf16 v23, v30, v17\n"
        "v_dot2c_f32_bf16 v24, v31, v14\n"
        "v_dot2c_f32_bf16 v25, v31, v17\n"
        "s_waitcnt vmcnt(14)\n"
        "v_accvgpr_read_b32 v28, a208\n"
        "v_accvgpr_read_b32 v29, a209\n"
        "v_accvgpr_read_b32 v30, a210\n"
        "v_accvgpr_read_b32 v31, a211\n"
        "v_lshlrev_b32 v17, 16, v15\n"
        "v_dot2c_f32_bf16 v18, v28, v15\n"
        "v_dot2c_f32_bf16 v19, v28, v17\n"
        "v_dot2c_f32_bf16 v20, v29, v15\n"
        "v_dot2c_f32_bf16 v21, v29, v17\n"
        "v_dot2c_f32_bf16 v22, v30, v15\n"
        "v_dot2c_f32_bf16 v23, v30, v17\n"
        "v_dot2c_f32_bf16 v24, v31, v15\n"
        "v_dot2c_f32_bf16 v25, v31, v17\n"
        "s_waitcnt vmcnt(13)\n"
        "v_accvgpr_read_b32 v28, a212\n"
        "v_accvgpr_read_b32 v29, a213\n"
        "v_accvgpr_read_b32 v30, a214\n"
        "v_accvgpr_read_b32 v31, a215\n"
        "v_lshlrev_b32 v17, 16, v16\n"
        "v_dot2c_f32_bf16 v18, v28, v16\n"
        "v_dot2c_f32_bf16 v19, v28, v17\n"
        "v_dot2c_f32_bf16 v20, v29, v16\n"
        "v_dot2c_f32_bf16 v21, v29, v17\n"
        "v_dot2c_f32_bf16 v22, v30, v16\n"
        "v_dot2c_f32_bf16 v23, v30, v17\n"
        "v_dot2c_f32_bf16 v24, v31, v16\n"
        "v_dot2c_f32_bf16 v25, v31, v17\n"
        "v_and_b32 v26, 0x70, v2\n"
        "v_lshlrev_b32 v26, 1, v26\n"
        "v_lshl_add_u32 v26, v10, 3, v26\n"
        "s_lshl_b32 s67, s52, 8\n"
        "s_add_u32 s67, s67, 129024\n"
        "v_add_u32 v26, s67, v26\n"
        "v_permlane32_swap_b32 v18, v22\n"
        "v_permlane32_swap_b32 v19, v23\n"
        "v_permlane32_swap_b32 v20, v24\n"
        "v_permlane32_swap_b32 v21, v25\n"
        "v_add_f32 v18, v18, v22\n"
        "v_add_f32 v19, v19, v23\n"
        "v_add_f32 v20, v20, v24\n"
        "v_add_f32 v21, v21, v25\n"
        "s_nop 0\n"
        "v_permlane16_swap_b32 v18, v20\n"
        "v_permlane16_swap_b32 v19, v21\n"
        "v_add_f32 v18, v18, v20\n"
        "v_add_f32 v19, v19, v21\n"
        "s_nop 0\n"
        "v_add_f32_dpp v18, v18, v18 row_ror:8 row_mask:0xf bank_mask:0xf\n"
        "v_add_f32_dpp v19, v19, v19 row_ror:8 row_mask:0xf bank_mask:0xf\n"
        "ds_write_b64 v26, v[18:19]\n"
        "s_waitcnt lgkmcnt(0)\n"
        "s_barrier\n"
        "buffer_load_dwordx4 a[32:35], v4, s[24:27], s58 offen sc1\n"
        "buffer_load_dwordx4 a[36:39], v4, s[24:27], s58 offen offset:1024 sc1\n"
        "buffer_load_dwordx4 a[40:43], v4, s[24:27], s58 offen offset:2048 sc1\n"
        "buffer_load_dwordx4 a[44:47], v4, s[24:27], s58 offen offset:3072 sc1\n"
        "s_cmp_eq_u32 s75, 1\n"
        "s_cbranch_scc0 .Lt11\n"
        "ds_read_b64_tr_b16 a[216:217], v35\n"
        "ds_read_b64_tr_b16 a[218:219], v35 offset:3200\n"
        "ds_read_b64_tr_b16 a[220:221], v35 offset:32\n"
        "ds_read_b64_tr_b16 a[222:223], v35 offset:3232\n"
        "ds_read_b64_tr_b16 a[224:225], v35 offset:64\n"
        "ds_read_b64_tr_b16 a[226:227], v35 offset:3264\n"
        "ds_read_b64_tr_b16 a[228:229], v35 offset:96\n"
        "ds_read_b64_tr_b16 a[230:231], v35 offset:3296\n"
        "ds_read_b64_tr_b16 a[232:233], v35 offset:128\n"
        "ds_read_b64_tr_b16 a[234:235], v35 offset:3328\n"
        "ds_read_b64_tr_b16 a[236:237], v35 offset:160\n"
        "ds_read_b64_tr_b16 a[238:239], v35 offset:3360\n"
        "s_waitcnt lgkmcnt(10)\n"
        "buffer_store_dwordx4 a[216:219], v2, s[24:27], s74 offen sc1\n"
        "s_waitcnt lgkmcnt(8)\n"
        "buffer_store_dwordx4 a[220:223], v2, s[24:27], s74 offen offset:1024 sc1\n"
        "s_waitcnt lgkmcnt(6)\n"
        "buffer_store_dwordx4 a[224:227], v2, s[24:27], s74 offen offset:2048 sc1\n"
        "s_add_u32 s74, s74, 58368\n"
        "s_waitcnt lgkmcnt(4)\n"
        "buffer_store_dwordx4 a[228:231], v2, s[24:27], s74 offen offset:3072 sc1\n"
        "s_waitcnt lgkmcnt(2)\n"
        "buffer_store_dwordx4 a[232:235], v3, s[24:27], s74 offen sc1\n"
        "s_waitcnt lgkmcnt(0)\n"
        "buffer_store_dwordx4 a[236:239], v3, s[24:27], s74 offen offset:1024 sc1\n"
        "s_branch .Le11\n"
        ".Lt11:\n"
        "s_cmp_eq_u32 s75, 0\n"
        "s_cbranch_scc0 .Lt12\n"
        "ds_read_b64_tr_b16 a[216:217], v35\n"
        "ds_read_b64_tr_b16 a[218:219], v35 offset:3200\n"
        "ds_read_b64_tr_b16 a[220:221], v35 offset:8\n"
        "ds_read_b64_tr_b16 a[222:223], v35 offset:3208\n"
        "ds_read_b64_tr_b16 a[224:225], v35 offset:16\n"
        "ds_read_b64_tr_b16 a[226:227], v35 offset:3216\n"
        "ds_read_b64_tr_b16 a[228:229], v35 offset:24\n"
        "ds_read_b64_tr_b16 a[230:231], v35 offset:3224\n"
        "ds_read_b64_tr_b16 a[232:233], v35 offset:32\n"
        "ds_read_b64_tr_b16 a[234:235], v35 offset:3232\n"
        "s_waitcnt lgkmcnt(8)\n"
        "buffer_store_dwordx4 a[216:219], v2, s[24:27], s74 offen sc1\n"
        "s_waitcnt lgkmcnt(6)\n"
        "buffer_store_dwordx4 a[220:223], v2, s[24:27], s74 offen offset:1024 sc1\n"
        "s_waitcnt lgkmcnt(4)\n"
        "buffer_store_dwordx4 a[224:227], v2, s[24:27], s74 offen offset:2048 sc1\n"
        "s_waitcnt lgkmcnt(2)\n"
        "buffer_store_dwordx4 a[228:231], v2, s[24:27], s74 offen offset:3072 sc1\n"
        "s_waitcnt lgkmcnt(0)\n"
        "buffer_store_dwordx4 a[232:235], v3, s[24:27], s74 offen sc1\n"
        ".Lt12:\n"
        ".Le11:\n"
        "s_cmp_lt_u32 s2, 96\n"
        "s_cselect_b32 s70, 0, s52\n"
        "s_cmp_eq_u32 s70, 3\n"
        "s_cbranch_scc0 .Lt13\n"
        "v_add_u32 v17, 129024, v11\n"
        "ds_read_b128 v[18:21], v17\n"
        "ds_read_b128 v[22:25], v17 offset:256\n"
        "ds_read_b128 v[26:29], v17 offset:512\n"
        "ds_read_b128 v[30:33], v17 offset:768\n"
        "s_and_b32 s67, s2, 3\n"
        "s_mul_i32 s67, s67, 15360\n"
        "s_lshl_b32 s68, s88, 8\n"
        "s_add_u32 s67, s67, s68\n"
        "s_add_u32 s67, s67, 32768\n"
        "s_waitcnt lgkmcnt(2)\n"
        "v_pk_add_f32 v[18:19], v[18:19], v[22:23]\n"
        "v_pk_add_f32 v[20:21], v[20:21], v[24:25]\n"
        "s_waitcnt lgkmcnt(0)\n"
        "v_pk_add_f32 v[26:27], v[26:27], v[30:31]\n"
        "v_pk_add_f32 v[28:29], v[28:29], v[32:33]\n"
        "v_pk_add_f32 v[18:19], v[18:19], v[26:27]\n"
        "v_pk_add_f32 v[20:21], v[20:21], v[28:29]\n"
        "s_mov_b64 exec, 0xffff\n"
        "buffer_store_dwordx4 v[18:21], v11, s[24:27], s67 offen sc1\n"
        "s_mov_b64 exec, -1\n"
        ".Lt13:\n"
        "s_mul_i32 s67, s52, 20\n"
        "v_add_u32 v24, s67, v10\n"
        "v_bfe_u32 v25, v2, 6, 2\n"
        "v_mul_u32_u24 v25, 5120, v25\n"
        "v_bfe_u32 v26, v2, 4, 2\n"
        "v_lshl_add_u32 v25, v26, 8, v25\n"
        "v_lshlrev_b32 v26, 4, v24\n"
        "v_xor_b32 v27, v26, v11\n"
        "v_and_b32 v27, 0xf0, v27\n"
        "v_lshrrev_b32 v26, 4, v24\n"
        "v_lshl_add_u32 v26, v26, 10, v25\n"
        "v_add_u32 v12, v26, v27\n"
        "v_add_u32 v24, 4, v24\n"
        "v_lshlrev_b32 v26, 4, v24\n"
        "v_xor_b32 v27, v26, v11\n"
        "v_and_b32 v27, 0xf0, v27\n"
        "v_lshrrev_b32 v26, 4, v24\n"
        "v_lshl_add_u32 v26, v26, 10, v25\n"
        "v_add_u32 v13, v26, v27\n"
        "v_add_u32 v24, 4, v24\n"
        "v_lshlrev_b32 v26, 4, v24\n"
        "v_xor_b32 v27, v26, v11\n"
        "v_and_b32 v27, 0xf0, v27\n"
        "v_lshrrev_b32 v26, 4, v24\n"
        "v_lshl_add_u32 v26, v26, 10, v25\n"
        "v_add_u32 v14, v26, v27\n"
        "v_add_u32 v24, 4, v24\n"
        "v_lshlrev_b32 v26, 4, v24\n"
        "v_xor_b32 v27, v26, v11\n"
        "v_and_b32 v27, 0xf0, v27\n"
        "v_lshrrev_b32 v26, 4, v24\n"
        "v_lshl_add_u32 v26, v26, 10, v25\n"
        "v_add_u32 v15, v26, v27\n"
        "v_add_u32 v24, 4, v24\n"
        "v_lshlrev_b32 v26, 4, v24\n"
        "v_xor_b32 v27, v26, v11\n"
        "v_and_b32 v27, 0xf0, v27\n"
        "v_lshrrev_b32 v26, 4, v24\n"
        "v_lshl_add_u32 v26, v26, 10, v25\n"
        "v_add_u32 v16, v26, v27\n"
        "v_lshlrev_b32 v24, 4, v10\n"
        "v_xor_b32 v24, v24, v2\n"
        "v_xor_b32 v18, 0, v24\n"
        "v_xor_b32 v19, 64, v24\n"
        "v_xor_b32 v20, 128, v24\n"
        "v_xor_b32 v21, 192, v24\n"
        "v_lshlrev_b32 v22, 1, v11\n"
        "v_add_u32 v22, 143360, v22\n"
        "s_mul_i32 s67, s52, 1280\n"
        "s_add_u32 s67, s67, 143360\n"
        "v_lshl_add_u32 v23, v10, 2, v11\n"
        "v_add_u32 v23, s67, v23\n"
        "s_cmp_lt_u32 s52, 2\n"
        "s_cselect_b32 s93, 0, 1.0\n"
        "s_waitcnt vmcnt(16)\n"
        "v_permlane32_swap_b32 v36, v38\n"
        "v_permlane32_swap_b32 v37, v39\n"
        "s_waitcnt vmcnt(15)\n"
        "v_permlane32_swap_b32 v40, v42\n"
        "v_permlane32_swap_b32 v41, v43\n"
        "s_waitcnt vmcnt(14)\n"
        "v_permlane32_swap_b32 v44, v46\n"
        "v_permlane32_swap_b32 v45, v47\n"
        "s_waitcnt vmcnt(13)\n"
        "v_permlane32_swap_b32 v48, v50\n"
        "v_permlane32_swap_b32 v49, v51\n"
        "s_waitcnt vmcnt(12)\n"
        "v_permlane32_swap_b32 v52, v54\n"
        "v_permlane32_swap_b32 v53, v55\n"
        "v_add_f32 v36, v36, v38\n"
        "v_add_f32 v37, v37, v39\n"
        "v_add_f32 v40, v40, v42\n"
        "v_add_f32 v41, v41, v43\n"
        "v_add_f32 v44, v44, v46\n"
        "v_add_f32 v45, v45, v47\n"
        "v_add_f32 v48, v48, v50\n"
        "v_add_f32 v49, v49, v51\n"
        "v_add_f32 v52, v52, v54\n"
        "v_add_f32 v53, v53, v55\n"
        "v_permlane16_swap_b32 v36, v37\n"
        "v_permlane16_swap_b32 v40, v41\n"
        "v_permlane16_swap_b32 v44, v45\n"
        "v_permlane16_swap_b32 v48, v49\n"
        "v_permlane16_swap_b32 v52, v53\n"
        "v_add_f32 v36, v36, v37\n"
        "v_add_f32 v40, v40, v41\n"
        "v_add_f32 v44, v44, v45\n"
        "v_add_f32 v48, v48, v49\n"
        "v_add_f32 v52, v52, v53\n"
        "v_cvt_pk_bf16_f32 v36, v36, v36\n"
        "v_cvt_pk_bf16_f32 v40, v40, v40\n"
        "v_cvt_pk_bf16_f32 v44, v44, v44\n"
        "v_cvt_pk_bf16_f32 v48, v48, v48\n"
        "v_cvt_pk_bf16_f32 v52, v52, v52\n"
        "v_lshlrev_b32 v36, 16, v36\n"
        "v_lshlrev_b32 v40, 16, v40\n"
        "v_lshlrev_b32 v44, 16, v44\n"
        "v_lshlrev_b32 v48, 16, v48\n"
        "v_lshlrev_b32 v52, 16, v52\n"
        "v_add_f32 v36, s93, v36\n"
        "v_add_f32 v40, s93, v40\n"
        "v_add_f32 v44, s93, v44\n"
        "v_add_f32 v48, s93, v48\n"
        "v_add_f32 v52, s93, v52\n"
        "v_cvt_pk_bf16_f32 v36, v36, v36\n"
        "v_cvt_pk_bf16_f32 v40, v40, v40\n"
        "v_cvt_pk_bf16_f32 v44, v44, v44\n"
        "v_cvt_pk_bf16_f32 v48, v48, v48\n"
        "v_cvt_pk_bf16_f32 v52, v52, v52\n"
        "v_lshlrev_b32 v36, 16, v36\n"
        "ds_write_b32 v23, v36\n"
        "v_lshlrev_b32 v40, 16, v40\n"
        "ds_write_b32 v23, v40 offset:256\n"
        "v_lshlrev_b32 v44, 16, v44\n"
        "ds_write_b32 v23, v44 offset:512\n"
        "v_lshlrev_b32 v48, 16, v48\n"
        "ds_write_b32 v23, v48 offset:768\n"
        "v_lshlrev_b32 v52, 16, v52\n"
        "ds_write_b32 v23, v52 offset:1024\n"
        "s_waitcnt lgkmcnt(0)\n"
        "s_barrier\n"
        "v_mov_b32 v23, v34\n"
        "ds_read_b128 v[24:27], v22\n"
        "ds_read_b128 v[32:35], v22 offset:2560\n"
        "ds_read_b128 v[28:31], v22 offset:16\n"
        "ds_read_b128 v[36:39], v22 offset:2576\n"
        "ds_read_b128 v[40:43], v22 offset:512\n"
        "ds_read_b128 v[48:51], v22 offset:3072\n"
        "ds_read_b128 v[44:47], v22 offset:528\n"
        "ds_read_b128 v[52:55], v22 offset:3088\n"
        "s_waitcnt lgkmcnt(6)\n"
        "v_pk_fma_f32 v[56:57], v[56:57], v[32:33], v[24:25]\n"
        "v_pk_fma_f32 v[58:59], v[58:59], v[34:35], v[26:27]\n"
        "s_waitcnt lgkmcnt(4)\n"
        "v_pk_fma_f32 v[60:61], v[60:61], v[36:37], v[28:29]\n"
        "v_pk_fma_f32 v[62:63], v[62:63], v[38:39], v[30:31]\n"
        "buffer_load_dwordx4 a[48:51], v5, s[24:27], s58 offen sc1\n"
        "v_cvt_pk_bf16_f32 v56, v56, v57\n"
        "v_cvt_pk_bf16_f32 v57, v58, v59\n"
        "v_cvt_pk_bf16_f32 v58, v60, v61\n"
        "buffer_load_dwordx4 a[52:55], v5, s[24:27], s58 offen offset:1024 sc1\n"
        "v_cvt_pk_bf16_f32 v59, v62, v63\n"
        "ds_write_b128 v12, v[56:59]\n"
        "v_pk_fma_f32 v[64:65], v[64:65], v[32:33], v[24:25]\n"
        "buffer_load_dwordx4 a[56:59], v5, s[24:27], s58 offen offset:2048 sc1\n"
        "v_pk_fma_f32 v[66:67], v[66:67], v[34:35], v[26:27]\n"
        "v_pk_fma_f32 v[68:69], v[68:69], v[36:37], v[28:29]\n"
        "v_pk_fma_f32 v[70:71], v[70:71], v[38:39], v[30:31]\n"
        "v_cvt_pk_bf16_f32 v64, v64, v65\n"
        "buffer_load_dwordx4 a[60:63], v5, s[24:27], s58 offen offset:3072 sc1\n"
        "v_cvt_pk_bf16_f32 v65, v66, v67\n"
        "v_cvt_pk_bf16_f32 v66, v68, v69\n"
        "v_cvt_pk_bf16_f32 v67, v70, v71\n"
        "s_add_u32 s86, s58, 16384\n"
        "ds_write_b128 v13, v[64:67]\n"
        "v_pk_fma_f32 v[72:73], v[72:73], v[32:33], v[24:25]\n"
        "v_pk_fma_f32 v[74:75], v[74:75], v[34:35], v[26:27]\n"
        "buffer_load_dwordx4 a[64:67], v2, s[24:27], s86 offen sc1\n"
        "v_pk_fma_f32 v[76:77], v[76:77], v[36:37], v[28:29]\n"
        "v_pk_fma_f32 v[78:79], v[78:79], v[38:39], v[30:31]\n"
        "v_cvt_pk_bf16_f32 v72, v72, v73\n"
        "v_cvt_pk_bf16_f32 v73, v74, v75\n"
        "buffer_load_dwordx4 a[68:71], v2, s[24:27], s86 offen offset:1024 sc1\n"
        "v_cvt_pk_bf16_f32 v74, v76, v77\n"
        "v_cvt_pk_bf16_f32 v75, v78, v79\n"
        "ds_write_b128 v14, v[72:75]\n"
        "buffer_load_dwordx4 a[72:75], v2, s[24:27], s86 offen offset:2048 sc1\n"
        "v_pk_fma_f32 v[80:81], v[80:81], v[32:33], v[24:25]\n"
        "v_pk_fma_f32 v[82:83], v[82:83], v[34:35], v[26:27]\n"
        "v_pk_fma_f32 v[84:85], v[84:85], v[36:37], v[28:29]\n"
        "buffer_load_dwordx4 a[76:79], v2, s[24:27], s86 offen offset:3072 sc1\n"
        "v_pk_fma_f32 v[86:87], v[86:87], v[38:39], v[30:31]\n"
        "v_cvt_pk_bf16_f32 v80, v80, v81\n"
        "v_cvt_pk_bf16_f32 v81, v82, v83\n"
        "v_cvt_pk_bf16_f32 v82, v84, v85\n"
        "buffer_load_dwordx4 a[80:83], v3, s[24:27], s86 offen sc1\n"
        "v_cvt_pk_bf16_f32 v83, v86, v87\n"
        "ds_write_b128 v15, v[80:83]\n"
        "v_pk_fma_f32 v[88:89], v[88:89], v[32:33], v[24:25]\n"
        "buffer_load_dwordx4 a[84:87], v3, s[24:27], s86 offen offset:1024 sc1\n"
        "v_pk_fma_f32 v[90:91], v[90:91], v[34:35], v[26:27]\n"
        "v_pk_fma_f32 v[92:93], v[92:93], v[36:37], v[28:29]\n"
        "v_pk_fma_f32 v[94:95], v[94:95], v[38:39], v[30:31]\n"
        "buffer_load_dwordx4 a[88:91], v3, s[24:27], s86 offen offset:2048 sc1\n"
        "v_cvt_pk_bf16_f32 v88, v88, v89\n"
        "v_cvt_pk_bf16_f32 v89, v90, v91\n"
        "v_cvt_pk_bf16_f32 v90, v92, v93\n"
        "v_cvt_pk_bf16_f32 v91, v94, v95\n"
        "buffer_load_dwordx4 a[92:95], v3, s[24:27], s86 offen offset:3072 sc1\n"
        "ds_write_b128 v16, v[88:91]\n"
        "s_waitcnt lgkmcnt(7)\n"
        "v_pk_fma_f32 v[96:97], v[96:97], v[48:49], v[40:41]\n"
        "v_pk_fma_f32 v[98:99], v[98:99], v[50:51], v[42:43]\n"
        "buffer_load_dwordx4 a[96:99], v4, s[24:27], s86 offen sc1\n"
        "s_waitcnt lgkmcnt(5)\n"
        "v_pk_fma_f32 v[100:101], v[100:101], v[52:53], v[44:45]\n"
        "v_pk_fma_f32 v[102:103], v[102:103], v[54:55], v[46:47]\n"
        "v_cvt_pk_bf16_f32 v96, v96, v97\n"
        "buffer_load_dwordx4 a[100:103], v4, s[24:27], s86 offen offset:1024 sc1\n"
        "v_cvt_pk_bf16_f32 v97, v98, v99\n"
        "v_cvt_pk_bf16_f32 v98, v100, v101\n"
        "v_cvt_pk_bf16_f32 v99, v102, v103\n"
        "ds_write_b128 v12, v[96:99] offset:20480\n"
        "buffer_load_dwordx4 a[104:107], v4, s[24:27], s86 offen offset:2048 sc1\n"
        "v_pk_fma_f32 v[104:105], v[104:105], v[48:49], v[40:41]\n"
        "v_pk_fma_f32 v[106:107], v[106:107], v[50:51], v[42:43]\n"
        "v_pk_fma_f32 v[108:109], v[108:109], v[52:53], v[44:45]\n"
        "buffer_load_dwordx4 a[108:111], v4, s[24:27], s86 offen offset:3072 sc1\n"
        "v_pk_fma_f32 v[110:111], v[110:111], v[54:55], v[46:47]\n"
        "v_cvt_pk_bf16_f32 v104, v104, v105\n"
        "v_cvt_pk_bf16_f32 v105, v106, v107\n"
        "buffer_load_dwordx4 a[112:115], v5, s[24:27], s86 offen sc1\n"
        "v_cvt_pk_bf16_f32 v106, v108, v109\n"
        "v_cvt_pk_bf16_f32 v107, v110, v111\n"
        "ds_write_b128 v13, v[104:107] offset:20480\n"
        "v_pk_fma_f32 v[112:113], v[112:113], v[48:49], v[40:41]\n"
        "buffer_load_dwordx4 a[116:119], v5, s[24:27], s86 offen offset:1024 sc1\n"
        "v_pk_fma_f32 v[114:115], v[114:115], v[50:51], v[42:43]\n"
        "v_pk_fma_f32 v[116:117], v[116:117], v[52:53], v[44:45]\n"
        "v_pk_fma_f32 v[118:119], v[118:119], v[54:55], v[46:47]\n"
        "buffer_load_dwordx4 a[120:123], v5, s[24:27], s86 offen offset:2048 sc1\n"
        "v_cvt_pk_bf16_f32 v112, v112, v113\n"
        "v_cvt_pk_bf16_f32 v113, v114, v115\n"
        "v_cvt_pk_bf16_f32 v114, v116, v117\n"
        "buffer_load_dwordx4 a[124:127], v5, s[24:27], s86 offen offset:3072 sc1\n"
        "v_cvt_pk_bf16_f32 v115, v118, v119\n"
        "ds_write_b128 v14, v[112:115] offset:20480\n"
        "v_pk_fma_f32 v[120:121], v[120:121], v[48:49], v[40:41]\n"
        "v_pk_fma_f32 v[122:123], v[122:123], v[50:51], v[42:43]\n"
        "s_add_u32 s86, s58, 32768\n"
        "v_pk_fma_f32 v[124:125], v[124:125], v[52:53], v[44:45]\n"
        "v_pk_fma_f32 v[126:127], v[126:127], v[54:55], v[46:47]\n"
        "v_cvt_pk_bf16_f32 v120, v120, v121\n"
        "buffer_load_dwordx4 a[128:131], v2, s[24:27], s86 offen sc1\n"
        "v_cvt_pk_bf16_f32 v121, v122, v123\n"
        "v_cvt_pk_bf16_f32 v122, v124, v125\n"
        "v_cvt_pk_bf16_f32 v123, v126, v127\n"
        "buffer_load_dwordx4 a[132:135], v2, s[24:27], s86 offen offset:1024 sc1\n"
        "ds_write_b128 v15, v[120:123] offset:20480\n"
        "v_pk_fma_f32 v[128:129], v[128:129], v[48:49], v[40:41]\n"
        "v_pk_fma_f32 v[130:131], v[130:131], v[50:51], v[42:43]\n"
        "v_pk_fma_f32 v[132:133], v[132:133], v[52:53], v[44:45]\n"
        "buffer_load_dwordx4 a[136:139], v2, s[24:27], s86 offen offset:2048 sc1\n"
        "v_pk_fma_f32 v[134:135], v[134:135], v[54:55], v[46:47]\n"
        "v_cvt_pk_bf16_f32 v128, v128, v129\n"
        "v_cvt_pk_bf16_f32 v129, v130, v131\n"
        "buffer_load_dwordx4 a[140:143], v2, s[24:27], s86 offen offset:3072 sc1\n"
        "v_cvt_pk_bf16_f32 v130, v132, v133\n"
        "v_cvt_pk_bf16_f32 v131, v134, v135\n"
        "ds_write_b128 v16, v[128:131] offset:20480\n"
        "v_mov_b32 v132, s47\n"
        "v_mov_b32 v133, s47\n"
        "v_mov_b32 v134, s47\n"
        "v_mov_b32 v135, s47\n"
        "ds_read_b128 v[24:27], v22 offset:1024\n"
        "ds_read_b128 v[32:35], v22 offset:3584\n"
        "ds_read_b128 v[28:31], v22 offset:1040\n"
        "ds_read_b128 v[36:39], v22 offset:3600\n"
        "v_add_u32 v12, 40960, v12\n"
        "v_add_u32 v13, 40960, v13\n"
        "v_add_u32 v14, 40960, v14\n"
        "v_add_u32 v15, 40960, v15\n"
        "v_add_u32 v16, 40960, v16\n"
        "s_waitcnt lgkmcnt(4)\n"
        "s_barrier\n"
        "ds_read_b128 a[216:219], v18\n"
        "ds_read_b128 a[220:223], v18 offset:1024\n"
        "ds_read_b128 a[224:227], v18 offset:2048\n"
        "ds_read_b128 a[228:231], v18 offset:3072\n"
        "ds_read_b128 a[232:235], v18 offset:4096\n"
        "s_waitcnt vmcnt(34) lgkmcnt(4)\n"
        "v_mfma_f32_16x16x32_bf16 v[56:59], a[4:7], a[216:219], 0\n"
        "ds_read_b128 a[236:239], v19 offset:5120\n"
        "ds_read_b128 a[240:243], v19 offset:6144\n"
        "s_waitcnt vmcnt(33)\n"
        "v_mfma_f32_16x16x32_bf16 v[76:79], a[216:219], a[8:11], 0\n"
        "ds_read_b128 a[244:247], v19 offset:7168\n"
        "ds_read_b128 a[248:251], v19 offset:8192\n"
        "s_waitcnt vmcnt(32)\n"
        "v_mfma_f32_16x16x32_bf16 v[96:99], a[12:15], a[216:219], 0\n"
        "ds_read_b128 a[252:255], v19 offset:9216\n"
        "s_waitcnt lgkmcnt(8)\n"
        "v_mfma_f32_16x16x32_bf16 v[60:63], a[4:7], a[220:223], 0\n"
        "ds_read_b128 v[40:43], v22 offset:1536\n"
        "ds_read_b128 v[48:51], v22 offset:4096\n"
        "v_mfma_f32_16x16x32_bf16 v[80:83], a[220:223], a[8:11], 0\n"
        "ds_read_b128 v[44:47], v22 offset:1552\n"
        "v_mfma_f32_16x16x32_bf16 v[100:103], a[12:15], a[220:223], 0\n"
        "ds_read_b128 v[52:55], v22 offset:4112\n"
        "v_fma_f32 v136, v136, v32, v24\n"
        "v_mfma_f32_16x16x32_bf16 v[116:119], a[0:3], a[220:223], 0\n"
        "buffer_store_dwordx4 v[132:135], v2, s[24:27], s45 offen sc1\n"
        "s_waitcnt lgkmcnt(11)\n"
        "v_mfma_f32_16x16x32_bf16 v[64:67], a[4:7], a[224:227], 0\n"
        "v_fma_f32 v137, v137, v33, v25\n"
        "v_fma_f32 v138, v138, v34, v26\n"
        "v_mfma_f32_16x16x32_bf16 v[84:87], a[224:227], a[8:11], 0\n"
        "v_fma_f32 v139, v139, v35, v27\n"
        "v_mfma_f32_16x16x32_bf16 v[104:107], a[12:15], a[224:227], 0\n"
        "v_fma_f32 v140, v140, v36, v28\n"
        "v_mfma_f32_16x16x32_bf16 v[120:123], a[0:3], a[224:227], 0\n"
        "buffer_load_dwordx4 a[144:147], v3, s[24:27], s86 offen sc1\n"
        "v_fma_f32 v141, v141, v37, v29\n"
        "s_waitcnt lgkmcnt(10)\n"
        "v_mfma_f32_16x16x32_bf16 v[68:71], a[4:7], a[228:231], 0\n"
        "v_fma_f32 v142, v142, v38, v30\n"
        "v_mfma_f32_16x16x32_bf16 v[88:91], a[228:231], a[8:11], 0\n"
        "v_fma_f32 v143, v143, v39, v31\n"
        "v_cvt_pk_bf16_f32 v136, v136, v137\n"
        "v_mfma_f32_16x16x32_bf16 v[108:111], a[12:15], a[228:231], 0\n"
        "v_cvt_pk_bf16_f32 v137, v138, v139\n"
        "v_mfma_f32_16x16x32_bf16 v[124:127], a[0:3], a[228:231], 0\n"
        "buffer_load_dwordx4 a[148:151], v3, s[24:27], s86 offen offset:1024 sc1\n"
        "v_cvt_pk_bf16_f32 v138, v140, v141\n"
        "s_waitcnt lgkmcnt(9)\n"
        "v_mfma_f32_16x16x32_bf16 v[72:75], a[4:7], a[232:235], 0\n"
        "v_cvt_pk_bf16_f32 v139, v142, v143\n"
        "v_mfma_f32_16x16x32_bf16 v[92:95], a[232:235], a[8:11], 0\n"
        "ds_write_b128 v12, v[136:139]\n"
        "v_mfma_f32_16x16x32_bf16 v[112:115], a[12:15], a[232:235], 0\n"
        "v_fma_f32 v144, v144, v32, v24\n"
        "buffer_load_dwordx4 a[152:155], v3, s[24:27], s86 offen offset:2048 sc1\n"
        "v_mfma_f32_16x16x32_bf16 v[128:131], a[0:3], a[232:235], 0\n"
        "s_waitcnt vmcnt(32) lgkmcnt(5)\n"
        "v_mfma_f32_16x16x32_bf16 v[56:59], a[20:23], a[236:239], v[56:59]\n"
        "ds_read_b128 a[216:219], v20 offset:10240\n"
        "ds_read_b128 a[220:223], v20 offset:11264\n"
        "v_mfma_f32_16x16x32_bf16 v[76:79], a[236:239], a[24:27], v[76:79]\n"
        "ds_read_b128 a[224:227], v20 offset:12288\n"
        "ds_read_b128 a[228:231], v20 offset:13312\n"
        "v_mfma_f32_16x16x32_bf16 v[96:99], a[28:31], a[236:239], v[96:99]\n"
        "ds_read_b128 a[232:235], v20 offset:14336\n"
        "v_mfma_f32_16x16x32_bf16 v[60:63], a[20:23], a[240:243], v[60:63]\n"
        "v_fma_f32 v145, v145, v33, v25\n"
        "v_fma_f32 v146, v146, v34, v26\n"
        "v_mfma_f32_16x16x32_bf16 v[80:83], a[240:243], a[24:27], v[80:83]\n"
        "v_fma_f32 v147, v147, v35, v27\n"
        "v_mfma_f32_16x16x32_bf16 v[100:103], a[28:31], a[240:243], v[100:103]\n"
        "v_fma_f32 v148, v148, v36, v28\n"
        "buffer_store_dwordx4 v[132:135], v2, s[24:27], s45 offen offset:1024 sc1\n"
        "v_mfma_f32_16x16x32_bf16 v[116:119], a[16:19], a[240:243], v[116:119]\n"
        "v_fma_f32 v149, v149, v37, v29\n"
        "v_mfma_f32_16x16x32_bf16 v[64:67], a[20:23], a[244:247], v[64:67]\n"
        "v_fma_f32 v150, v150, v38, v30\n"
        "v_fma_f32 v151, v151, v39, v31\n"
        "v_mfma_f32_16x16x32_bf16 v[84:87], a[244:247], a[24:27], v[84:87]\n"
        "v_cvt_pk_bf16_f32 v144, v144, v145\n"
        "v_mfma_f32_16x16x32_bf16 v[104:107], a[28:31], a[244:247], v[104:107]\n"
        "buffer_load_dwordx4 a[156:159], v3, s[24:27], s86 offen offset:3072 sc1\n"
        "v_cvt_pk_bf16_f32 v145, v146, v147\n"
        "v_mfma_f32_16x16x32_bf16 v[120:123], a[16:19], a[244:247], v[120:123]\n"
        "v_cvt_pk_bf16_f32 v146, v148, v149\n"
        "v_mfma_f32_16x16x32_bf16 v[68:71], a[20:23], a[248:251], v[68:71]\n"
        "v_cvt_pk_bf16_f32 v147, v150, v151\n"
        "buffer_load_dwordx4 a[160:163], v4, s[24:27], s86 offen sc1\n"
        "v_mfma_f32_16x16x32_bf16 v[88:91], a[248:251], a[24:27], v[88:91]\n"
        "ds_write_b128 v13, v[144:147]\n"
        "v_mfma_f32_16x16x32_bf16 v[108:111], a[28:31], a[248:251], v[108:111]\n"
        "v_fma_f32 v152, v152, v32, v24\n"
        "v_fma_f32 v153, v153, v33, v25\n"
        "v_mfma_f32_16x16x32_bf16 v[124:127], a[16:19], a[248:251], v[124:127]\n"
        "v_fma_f32 v154, v154, v34, v26\n"
        "v_mfma_f32_16x16x32_bf16 v[72:75], a[20:23], a[252:255], v[72:75]\n"
        "buffer_load_dwordx4 a[164:167], v4, s[24:27], s86 offen offset:1024 sc1\n"
        "v_fma_f32 v155, v155, v35, v27\n"
        "v_mfma_f32_16x16x32_bf16 v[92:95], a[252:255], a[24:27], v[92:95]\n"
        "v_fma_f32 v156, v156, v36, v28\n"
        "v_mfma_f32_16x16x32_bf16 v[112:115], a[28:31], a[252:255], v[112:115]\n"
        "v_fma_f32 v157, v157, v37, v29\n"
        "buffer_load_dwordx4 a[168:171], v4, s[24:27], s86 offen offset:2048 sc1\n"
        "v_mfma_f32_16x16x32_bf16 v[128:131], a[16:19], a[252:255], v[128:131]\n"
        "s_waitcnt vmcnt(33) lgkmcnt(1)\n"
        "v_mfma_f32_16x16x32_bf16 v[56:59], a[36:39], a[216:219], v[56:59]\n"
        "ds_read_b128 a[236:239], v21 offset:15360\n"
        "ds_read_b128 a[240:243], v21 offset:16384\n"
        "v_mfma_f32_16x16x32_bf16 v[76:79], a[216:219], a[40:43], v[76:79]\n"
        "ds_read_b128 a[244:247], v21 offset:17408\n"
        "ds_read_b128 a[248:251], v21 offset:18432\n"
        "v_mfma_f32_16x16x32_bf16 v[96:99], a[44:47], a[216:219], v[96:99]\n"
        "ds_read_b128 a[252:255], v21 offset:19456\n"
        "v_mfma_f32_16x16x32_bf16 v[60:63], a[36:39], a[220:223], v[60:63]\n"
        "v_fma_f32 v158, v158, v38, v30\n"
        "v_fma_f32 v159, v159, v39, v31\n"
        "v_mfma_f32_16x16x32_bf16 v[80:83], a[220:223], a[40:43], v[80:83]\n"
        "v_cvt_pk_bf16_f32 v152, v152, v153\n"
        "v_mfma_f32_16x16x32_bf16 v[100:103], a[44:47], a[220:223], v[100:103]\n"
        "v_cvt_pk_bf16_f32 v153, v154, v155\n"
        "buffer_store_dwordx4 v[132:135], v2, s[24:27], s45 offen offset:1536 sc1\n"
        "v_mfma_f32_16x16x32_bf16 v[116:119], a[32:35], a[220:223], v[116:119]\n"
        "v_cvt_pk_bf16_f32 v154, v156, v157\n"
        "v_mfma_f32_16x16x32_bf16 v[64:67], a[36:39], a[224:227], v[64:67]\n"
        "v_cvt_pk_bf16_f32 v155, v158, v159\n"
        "ds_write_b128 v14, v[152:155]\n"
        "v_mfma_f32_16x16x32_bf16 v[84:87], a[224:227], a[40:43], v[84:87]\n"
        "v_fma_f32 v160, v160, v32, v24\n"
        "v_mfma_f32_16x16x32_bf16 v[104:107], a[44:47], a[224:227], v[104:107]\n"
        "buffer_load_dwordx4 a[172:175], v4, s[24:27], s86 offen offset:3072 sc1\n"
        "v_fma_f32 v161, v161, v33, v25\n"
        "v_mfma_f32_16x16x32_bf16 v[120:123], a[32:35], a[224:227], v[120:123]\n"
        "v_fma_f32 v162, v162, v34, v26\n"
        "v_mfma_f32_16x16x32_bf16 v[68:71], a[36:39], a[228:231], v[68:71]\n"
        "v_fma_f32 v163, v163, v35, v27\n"
        "buffer_load_dwordx4 a[176:179], v5, s[24:27], s86 offen sc1\n"
        "v_mfma_f32_16x16x32_bf16 v[88:91], a[228:231], a[40:43], v[88:91]\n"
        "v_fma_f32 v164, v164, v36, v28\n"
        "v_mfma_f32_16x16x32_bf16 v[108:111], a[44:47], a[228:231], v[108:111]\n"
        "v_fma_f32 v165, v165, v37, v29\n"
        "v_fma_f32 v166, v166, v38, v30\n"
        "v_mfma_f32_16x16x32_bf16 v[124:127], a[32:35], a[228:231], v[124:127]\n"
        "v_fma_f32 v167, v167, v39, v31\n"
        "v_mfma_f32_16x16x32_bf16 v[72:75], a[36:39], a[232:235], v[72:75]\n"
        "buffer_load_dwordx4 a[180:183], v5, s[24:27], s86 offen offset:1024 sc1\n"
        "v_cvt_pk_bf16_f32 v160, v160, v161\n"
        "v_mfma_f32_16x16x32_bf16 v[92:95], a[232:235], a[40:43], v[92:95]\n"
        "v_cvt_pk_bf16_f32 v161, v162, v163\n"
        "v_mfma_f32_16x16x32_bf16 v[112:115], a[44:47], a[232:235], v[112:115]\n"
        "v_cvt_pk_bf16_f32 v162, v164, v165\n"
        "buffer_load_dwordx4 a[184:187], v5, s[24:27], s86 offen offset:2048 sc1\n"
        "v_mfma_f32_16x16x32_bf16 v[128:131], a[32:35], a[232:235], v[128:131]\n"
        "s_waitcnt vmcnt(34) lgkmcnt(1)\n"
        "v_mfma_f32_16x16x32_bf16 v[56:59], a[52:55], a[236:239], v[56:59]\n"
        "ds_read_b128 a[216:219], v18 offset:20480\n"
        "ds_read_b128 a[220:223], v18 offset:21504\n"
        "v_mfma_f32_16x16x32_bf16 v[76:79], a[236:239], a[56:59], v[76:79]\n"
        "ds_read_b128 a[224:227], v18 offset:22528\n"
        "ds_read_b128 a[228:231], v18 offset:23552\n"
        "v_mfma_f32_16x16x32_bf16 v[96:99], a[60:63], a[236:239], v[96:99]\n"
        "ds_read_b128 a[232:235], v18 offset:24576\n"
        "v_mfma_f32_16x16x32_bf16 v[60:63], a[52:55], a[240:243], v[60:63]\n"
        "v_cvt_pk_bf16_f32 v163, v166, v167\n"
        "ds_write_b128 v15, v[160:163]\n"
        "v_mfma_f32_16x16x32_bf16 v[80:83], a[240:243], a[56:59], v[80:83]\n"
        "v_fma_f32 v168, v168, v32, v24\n"
        "v_mfma_f32_16x16x32_bf16 v[100:103], a[60:63], a[240:243], v[100:103]\n"
        "buffer_load_dwordx4 a[188:191], v5, s[24:27], s86 offen offset:3072 sc1\n"
        "v_fma_f32 v169, v169, v33, v25\n"
        "v_mfma_f32_16x16x32_bf16 v[116:119], a[48:51], a[240:243], v[116:119]\n"
        "v_fma_f32 v170, v170, v34, v26\n"
        "v_mfma_f32_16x16x32_bf16 v[64:67], a[52:55], a[244:247], v[64:67]\n"
        "s_add_u32 s86, s58, 49152\n"
        "v_mfma_f32_16x16x32_bf16 v[84:87], a[244:247], a[56:59], v[84:87]\n"
        "v_fma_f32 v171, v171, v35, v27\n"
        "v_fma_f32 v172, v172, v36, v28\n"
        "v_mfma_f32_16x16x32_bf16 v[104:107], a[60:63], a[244:247], v[104:107]\n"
        "v_fma_f32 v173, v173, v37, v29\n"
        "v_mfma_f32_16x16x32_bf16 v[120:123], a[48:51], a[244:247], v[120:123]\n"
        "buffer_load_dwordx4 a[0:3], v2, s[24:27], s86 offen sc1\n"
        "v_mfma_f32_16x16x32_bf16 v[68:71], a[52:55], a[248:251], v[68:71]\n"
        "v_fma_f32 v174, v174, v38, v30\n"
        "v_fma_f32 v175, v175, v39, v31\n"
        "v_mfma_f32_16x16x32_bf16 v[88:91], a[248:251], a[56:59], v[88:91]\n"
        "buffer_load_dwordx4 a[4:7], v2, s[24:27], s86 offen offset:1024 sc1\n"
        "v_mfma_f32_16x16x32_bf16 v[108:111], a[60:63], a[248:251], v[108:111]\n"
        "v_cvt_pk_bf16_f32 v168, v168, v169\n"
        "v_mfma_f32_16x16x32_bf16 v[124:127], a[48:51], a[248:251], v[124:127]\n"
        "v_cvt_pk_bf16_f32 v169, v170, v171\n"
        "v_cvt_pk_bf16_f32 v170, v172, v173\n"
        "v_mfma_f32_16x16x32_bf16 v[72:75], a[52:55], a[252:255], v[72:75]\n"
        "buffer_load_dwordx4 a[8:11], v2, s[24:27], s86 offen offset:2048 sc1\n"
        "v_mfma_f32_16x16x32_bf16 v[92:95], a[252:255], a[56:59], v[92:95]\n"
        "v_cvt_pk_bf16_f32 v171, v174, v175\n"
        "v_mfma_f32_16x16x32_bf16 v[112:115], a[60:63], a[252:255], v[112:115]\n"
        "ds_write_b128 v16, v[168:171]\n"
        "buffer_load_dwordx4 a[12:15], v2, s[24:27], s86 offen offset:3072 sc1\n"
        "v_mfma_f32_16x16x32_bf16 v[128:131], a[48:51], a[252:255], v[128:131]\n"
        "s_waitcnt vmcnt(35) lgkmcnt(2)\n"
        "v_mfma_f32_16x16x32_bf16 v[56:59], a[68:71], a[216:219], v[56:59]\n"
        "ds_read_b128 a[236:239], v19 offset:25600\n"
        "ds_read_b128 a[240:243], v19 offset:26624\n"
        "v_mfma_f32_16x16x32_bf16 v[76:79], a[216:219], a[72:75], v[76:79]\n"
        "ds_read_b128 a[244:247], v19 offset:27648\n"
        "ds_read_b128 a[248:251], v19 offset:28672\n"
        "v_mfma_f32_16x16x32_bf16 v[96:99], a[76:79], a[216:219], v[96:99]\n"
        "ds_read_b128 a[252:255], v19 offset:29696\n"
        "v_mfma_f32_16x16x32_bf16 v[60:63], a[68:71], a[220:223], v[60:63]\n"
        "ds_read_b128 v[24:27], v22 offset:2048\n"
        "ds_read_b128 v[32:35], v22 offset:4608\n"
        "v_mfma_f32_16x16x32_bf16 v[80:83], a[220:223], a[72:75], v[80:83]\n"
        "ds_read_b128 v[28:31], v22 offset:2064\n"
        "v_mfma_f32_16x16x32_bf16 v[100:103], a[76:79], a[220:223], v[100:103]\n"
        "ds_read_b128 v[36:39], v22 offset:4624\n"
        "v_fma_f32 v176, v176, v48, v40\n"
        "v_mfma_f32_16x16x32_bf16 v[116:119], a[64:67], a[220:223], v[116:119]\n"
        "buffer_load_dwordx4 a[16:19], v3, s[24:27], s86 offen sc1\n"
        "v_mfma_f32_16x16x32_bf16 v[64:67], a[68:71], a[224:227], v[64:67]\n"
        "v_fma_f32 v177, v177, v49, v41\n"
        "v_fma_f32 v178, v178, v50, v42\n"
        "v_mfma_f32_16x16x32_bf16 v[84:87], a[224:227], a[72:75], v[84:87]\n"
        "v_fma_f32 v179, v179, v51, v43\n"
        "v_mfma_f32_16x16x32_bf16 v[104:107], a[76:79], a[224:227], v[104:107]\n"
        "v_fma_f32 v180, v180, v52, v44\n"
        "v_mfma_f32_16x16x32_bf16 v[120:123], a[64:67], a[224:227], v[120:123]\n"
        "buffer_load_dwordx4 a[20:23], v3, s[24:27], s86 offen offset:1024 sc1\n"
        "v_fma_f32 v181, v181, v53, v45\n"
        "v_mfma_f32_16x16x32_bf16 v[68:71], a[68:71], a[228:231], v[68:71]\n"
        "v_fma_f32 v182, v182, v54, v46\n"
        "v_mfma_f32_16x16x32_bf16 v[88:91], a[228:231], a[72:75], v[88:91]\n"
        "v_fma_f32 v183, v183, v55, v47\n"
        "v_cvt_pk_bf16_f32 v176, v176, v177\n"
        "v_mfma_f32_16x16x32_bf16 v[108:111], a[76:79], a[228:231], v[108:111]\n"
        "v_cvt_pk_bf16_f32 v177, v178, v179\n"
        "v_mfma_f32_16x16x32_bf16 v[124:127], a[64:67], a[228:231], v[124:127]\n"
        "buffer_load_dwordx4 a[24:27], v3, s[24:27], s86 offen offset:2048 sc1\n"
        "v_cvt_pk_bf16_f32 v178, v180, v181\n"
        "v_mfma_f32_16x16x32_bf16 v[72:75], a[68:71], a[232:235], v[72:75]\n"
        "v_cvt_pk_bf16_f32 v179, v182, v183\n"
        "v_mfma_f32_16x16x32_bf16 v[92:95], a[232:235], a[72:75], v[92:95]\n"
        "ds_write_b128 v12, v[176:179] offset:20480\n"
        "v_mfma_f32_16x16x32_bf16 v[112:115], a[76:79], a[232:235], v[112:115]\n"
        "v_fma_f32 v184, v184, v48, v40\n"
        "buffer_load_dwordx4 a[28:31], v3, s[24:27], s86 offen offset:3072 sc1\n"
        "v_mfma_f32_16x16x32_bf16 v[128:131], a[64:67], a[232:235], v[128:131]\n"
        "s_waitcnt vmcnt(35) lgkmcnt(5)\n"
        "v_mfma_f32_16x16x32_bf16 v[56:59], a[84:87], a[236:239], v[56:59]\n"
        "s_waitcnt vmcnt(13)\n"
        "s_waitcnt lgkmcnt(0)\n"
        "s_barrier\n"
        "v_mfma_f32_16x16x32_bf16 v[76:79], a[236:239], a[88:91], v[76:79]\n"
        "s_add_u32 s89, s76, 8192\n"
        "s_cmp_eq_u32 s52, 1\n"
        "s_cbranch_scc0 .Lt14\n"
        "s_mov_b64 exec, 1\n"
        "buffer_store_dwordx2 v[6:7], off, s[24:27], s89 sc1\n"
        "s_mov_b64 exec, -1\n"
        ".Lt14:\n"
        "v_mfma_f32_16x16x32_bf16 v[96:99], a[92:95], a[236:239], v[96:99]\n"
        "ds_read_b128 a[216:219], v20 offset:30720\n"
        "ds_read_b128 a[220:223], v20 offset:31744\n"
        "v_mfma_f32_16x16x32_bf16 v[60:63], a[84:87], a[240:243], v[60:63]\n"
        "ds_read_b128 a[224:227], v20 offset:32768\n"
        "ds_read_b128 a[228:231], v20 offset:33792\n"
        "v_mfma_f32_16x16x32_bf16 v[80:83], a[240:243], a[88:91], v[80:83]\n"
        "v_fma_f32 v185, v185, v49, v41\n"
        "ds_read_b128 a[232:235], v20 offset:34816\n"
        "v_mfma_f32_16x16x32_bf16 v[100:103], a[92:95], a[240:243], v[100:103]\n"
        "v_fma_f32 v186, v186, v50, v42\n"
        "v_fma_f32 v187, v187, v51, v43\n"
        "v_mfma_f32_16x16x32_bf16 v[116:119], a[80:83], a[240:243], v[116:119]\n"
        "v_fma_f32 v188, v188, v52, v44\n"
        "buffer_store_dwordx4 v[132:135], v2, s[24:27], s46 offen sc1\n"
        "v_mfma_f32_16x16x32_bf16 v[64:67], a[84:87], a[244:247], v[64:67]\n"
        "v_fma_f32 v189, v189, v53, v45\n"
        "v_mfma_f32_16x16x32_bf16 v[84:87], a[244:247], a[88:91], v[84:87]\n"
        "v_fma_f32 v190, v190, v54, v46\n"
        "v_fma_f32 v191, v191, v55, v47\n"
        "v_mfma_f32_16x16x32_bf16 v[104:107], a[92:95], a[244:247], v[104:107]\n"
        "v_cvt_pk_bf16_f32 v184, v184, v185\n"
        "buffer_load_dwordx4 a[32:35], v4, s[24:27], s86 offen sc1\n"
        "v_mfma_f32_16x16x32_bf16 v[120:123], a[80:83], a[244:247], v[120:123]\n"
        "v_cvt_pk_bf16_f32 v185, v186, v187\n"
        "v_mfma_f32_16x16x32_bf16 v[68:71], a[84:87], a[248:251], v[68:71]\n"
        "v_cvt_pk_bf16_f32 v186, v188, v189\n"
        "v_cvt_pk_bf16_f32 v187, v190, v191\n"
        "v_mfma_f32_16x16x32_bf16 v[88:91], a[248:251], a[88:91], v[88:91]\n"
        "buffer_load_dwordx4 a[36:39], v4, s[24:27], s86 offen offset:1024 sc1\n"
        "v_mfma_f32_16x16x32_bf16 v[108:111], a[92:95], a[248:251], v[108:111]\n"
        "ds_write_b128 v13, v[184:187] offset:20480\n"
        "v_fma_f32 v192, v192, v48, v40\n"
        "v_mfma_f32_16x16x32_bf16 v[124:127], a[80:83], a[248:251], v[124:127]\n"
        "v_fma_f32 v193, v193, v49, v41\n"
        "v_fma_f32 v194, v194, v50, v42\n"
        "v_mfma_f32_16x16x32_bf16 v[72:75], a[84:87], a[252:255], v[72:75]\n"
        "buffer_load_dwordx4 a[40:43], v4, s[24:27], s86 offen offset:2048 sc1\n"
        "v_mfma_f32_16x16x32_bf16 v[92:95], a[252:255], a[88:91], v[92:95]\n"
        "v_fma_f32 v195, v195, v51, v43\n"
        "v_fma_f32 v196, v196, v52, v44\n"
        "v_mfma_f32_16x16x32_bf16 v[112:115], a[92:95], a[252:255], v[112:115]\n"
        "v_fma_f32 v197, v197, v53, v45\n"
        "buffer_load_dwordx4 a[44:47], v4, s[24:27], s86 offen offset:3072 sc1\n"
        "v_mfma_f32_16x16x32_bf16 v[128:131], a[80:83], a[252:255], v[128:131]\n"
        "s_waitcnt lgkmcnt(1)\n"
        "v_mfma_f32_16x16x32_bf16 v[56:59], a[100:103], a[216:219], v[56:59]\n"
        "ds_read_b128 a[236:239], v21 offset:35840\n"
        "ds_read_b128 a[240:243], v21 offset:36864\n"
        "v_mfma_f32_16x16x32_bf16 v[76:79], a[216:219], a[104:107], v[76:79]\n"
        "ds_read_b128 a[244:247], v21 offset:37888\n"
        "ds_read_b128 a[248:251], v21 offset:38912\n"
        "v_mfma_f32_16x16x32_bf16 v[96:99], a[108:111], a[216:219], v[96:99]\n"
        "ds_read_b128 a[252:255], v21 offset:39936\n"
        "v_mfma_f32_16x16x32_bf16 v[60:63], a[100:103], a[220:223], v[60:63]\n"
        "v_fma_f32 v198, v198, v54, v46\n"
        "v_fma_f32 v199, v199, v55, v47\n"
        "v_mfma_f32_16x16x32_bf16 v[80:83], a[220:223], a[104:107], v[80:83]\n"
        "v_cvt_pk_bf16_f32 v192, v192, v193\n"
        "v_mfma_f32_16x16x32_bf16 v[100:103], a[108:111], a[220:223], v[100:103]\n"
        "v_cvt_pk_bf16_f32 v193, v194, v195\n"
        "buffer_store_dwordx4 v[132:135], v2, s[24:27], s46 offen offset:1024 sc1\n"
        "v_mfma_f32_16x16x32_bf16 v[116:119], a[96:99], a[220:223], v[116:119]\n"
        "v_cvt_pk_bf16_f32 v194, v196, v197\n"
        "v_mfma_f32_16x16x32_bf16 v[64:67], a[100:103], a[224:227], v[64:67]\n"
        "v_cvt_pk_bf16_f32 v195, v198, v199\n"
        "ds_write_b128 v14, v[192:195] offset:20480\n"
        "v_mfma_f32_16x16x32_bf16 v[84:87], a[224:227], a[104:107], v[84:87]\n"
        "v_fma_f32 v200, v200, v48, v40\n"
        "v_mfma_f32_16x16x32_bf16 v[104:107], a[108:111], a[224:227], v[104:107]\n"
        "buffer_load_dwordx4 a[48:51], v5, s[24:27], s86 offen sc1\n"
        "v_fma_f32 v201, v201, v49, v41\n"
        "v_mfma_f32_16x16x32_bf16 v[120:123], a[96:99], a[224:227], v[120:123]\n"
        "v_fma_f32 v202, v202, v50, v42\n"
        "v_mfma_f32_16x16x32_bf16 v[68:71], a[100:103], a[228:231], v[68:71]\n"
        "v_fma_f32 v203, v203, v51, v43\n"
        "buffer_load_dwordx4 a[52:55], v5, s[24:27], s86 offen offset:1024 sc1\n"
        "v_mfma_f32_16x16x32_bf16 v[88:91], a[228:231], a[104:107], v[88:91]\n"
        "v_fma_f32 v204, v204, v52, v44\n"
        "v_mfma_f32_16x16x32_bf16 v[108:111], a[108:111], a[228:231], v[108:111]\n"
        "v_fma_f32 v205, v205, v53, v45\n"
        "v_fma_f32 v206, v206, v54, v46\n"
        "v_mfma_f32_16x16x32_bf16 v[124:127], a[96:99], a[228:231], v[124:127]\n"
        "v_fma_f32 v207, v207, v55, v47\n"
        "v_mfma_f32_16x16x32_bf16 v[72:75], a[100:103], a[232:235], v[72:75]\n"
        "buffer_load_dwordx4 a[56:59], v5, s[24:27], s86 offen offset:2048 sc1\n"
        "v_cvt_pk_bf16_f32 v200, v200, v201\n"
        "v_mfma_f32_16x16x32_bf16 v[92:95], a[232:235], a[104:107], v[92:95]\n"
        "v_cvt_pk_bf16_f32 v201, v202, v203\n"
        "v_mfma_f32_16x16x32_bf16 v[112:115], a[108:111], a[232:235], v[112:115]\n"
        "v_cvt_pk_bf16_f32 v202, v204, v205\n"
        "buffer_load_dwordx4 a[60:63], v5, s[24:27], s86 offen offset:3072 sc1\n"
        "v_mfma_f32_16x16x32_bf16 v[128:131], a[96:99], a[232:235], v[128:131]\n"
        "s_waitcnt lgkmcnt(1)\n"
        "v_mfma_f32_16x16x32_bf16 v[56:59], a[116:119], a[236:239], v[56:59]\n"
        "ds_read_b128 a[216:219], v18 offset:40960\n"
        "ds_read_b128 a[220:223], v18 offset:41984\n"
        "v_mfma_f32_16x16x32_bf16 v[76:79], a[236:239], a[120:123], v[76:79]\n"
        "ds_read_b128 a[224:227], v18 offset:43008\n"
        "ds_read_b128 a[228:231], v18 offset:44032\n"
        "v_mfma_f32_16x16x32_bf16 v[96:99], a[124:127], a[236:239], v[96:99]\n"
        "ds_read_b128 a[232:235], v18 offset:45056\n"
        "v_mfma_f32_16x16x32_bf16 v[60:63], a[116:119], a[240:243], v[60:63]\n"
        "v_cvt_pk_bf16_f32 v203, v206, v207\n"
        "ds_write_b128 v15, v[200:203] offset:20480\n"
        "v_mfma_f32_16x16x32_bf16 v[80:83], a[240:243], a[120:123], v[80:83]\n"
        "v_fma_f32 v208, v208, v48, v40\n"
        "v_mfma_f32_16x16x32_bf16 v[100:103], a[124:127], a[240:243], v[100:103]\n"
        "s_add_u32 s86, s58, 65536\n"
        "v_fma_f32 v209, v209, v49, v41\n"
        "v_mfma_f32_16x16x32_bf16 v[116:119], a[112:115], a[240:243], v[116:119]\n"
        "v_fma_f32 v210, v210, v50, v42\n"
        "v_mfma_f32_16x16x32_bf16 v[64:67], a[116:119], a[244:247], v[64:67]\n"
        "buffer_load_dwordx4 a[64:67], v2, s[24:27], s86 offen sc1\n"
        "v_mfma_f32_16x16x32_bf16 v[84:87], a[244:247], a[120:123], v[84:87]\n"
        "v_fma_f32 v211, v211, v51, v43\n"
        "v_fma_f32 v212, v212, v52, v44\n"
        "v_mfma_f32_16x16x32_bf16 v[104:107], a[124:127], a[244:247], v[104:107]\n"
        "v_fma_f32 v213, v213, v53, v45\n"
        "v_mfma_f32_16x16x32_bf16 v[120:123], a[112:115], a[244:247], v[120:123]\n"
        "buffer_load_dwordx4 a[68:71], v2, s[24:27], s86 offen offset:1024 sc1\n"
        "v_mfma_f32_16x16x32_bf16 v[68:71], a[116:119], a[248:251], v[68:71]\n"
        "v_fma_f32 v214, v214, v54, v46\n"
        "v_fma_f32 v215, v215, v55, v47\n"
        "v_mfma_f32_16x16x32_bf16 v[88:91], a[248:251], a[120:123], v[88:91]\n"
        "buffer_load_dwordx4 a[72:75], v2, s[24:27], s86 offen offset:2048 sc1\n"
        "v_mfma_f32_16x16x32_bf16 v[108:111], a[124:127], a[248:251], v[108:111]\n"
        "v_cvt_pk_bf16_f32 v208, v208, v209\n"
        "v_mfma_f32_16x16x32_bf16 v[124:127], a[112:115], a[248:251], v[124:127]\n"
        "v_cvt_pk_bf16_f32 v209, v210, v211\n"
        "v_cvt_pk_bf16_f32 v210, v212, v213\n"
        "v_mfma_f32_16x16x32_bf16 v[72:75], a[116:119], a[252:255], v[72:75]\n"
        "buffer_load_dwordx4 a[76:79], v2, s[24:27], s86 offen offset:3072 sc1\n"
        "v_mfma_f32_16x16x32_bf16 v[92:95], a[252:255], a[120:123], v[92:95]\n"
        "v_cvt_pk_bf16_f32 v211, v214, v215\n"
        "v_mfma_f32_16x16x32_bf16 v[112:115], a[124:127], a[252:255], v[112:115]\n"
        "ds_write_b128 v16, v[208:211] offset:20480\n"
        "buffer_load_dwordx4 a[80:83], v3, s[24:27], s86 offen sc1\n"
        "v_mfma_f32_16x16x32_bf16 v[128:131], a[112:115], a[252:255], v[128:131]\n"
        "s_waitcnt lgkmcnt(2)\n"
        "v_mfma_f32_16x16x32_bf16 v[56:59], a[132:135], a[216:219], v[56:59]\n"
        "ds_read_b128 a[236:239], v19 offset:46080\n"
        "ds_read_b128 a[240:243], v19 offset:47104\n"
        "v_mfma_f32_16x16x32_bf16 v[76:79], a[216:219], a[136:139], v[76:79]\n"
        "ds_read_b128 a[244:247], v19 offset:48128\n"
        "ds_read_b128 a[248:251], v19 offset:49152\n"
        "v_mfma_f32_16x16x32_bf16 v[96:99], a[140:143], a[216:219], v[96:99]\n"
        "ds_read_b128 a[252:255], v19 offset:50176\n"
        "v_mfma_f32_16x16x32_bf16 v[60:63], a[132:135], a[220:223], v[60:63]\n"
        "v_fma_f32 v216, v216, v32, v24\n"
        "v_fma_f32 v217, v217, v33, v25\n"
        "v_mfma_f32_16x16x32_bf16 v[80:83], a[220:223], a[136:139], v[80:83]\n"
        "v_fma_f32 v218, v218, v34, v26\n"
        "v_mfma_f32_16x16x32_bf16 v[100:103], a[140:143], a[220:223], v[100:103]\n"
        "v_fma_f32 v219, v219, v35, v27\n"
        "buffer_store_dwordx4 v[132:135], v2, s[24:27], s46 offen offset:2048 sc1\n"
        "v_mfma_f32_16x16x32_bf16 v[116:119], a[128:131], a[220:223], v[116:119]\n"
        "v_fma_f32 v220, v220, v36, v28\n"
        "v_mfma_f32_16x16x32_bf16 v[64:67], a[132:135], a[224:227], v[64:67]\n"
        "v_fma_f32 v221, v221, v37, v29\n"
        "v_fma_f32 v222, v222, v38, v30\n"
        "v_mfma_f32_16x16x32_bf16 v[84:87], a[224:227], a[136:139], v[84:87]\n"
        "buffer_load_dwordx4 a[84:87], v3, s[24:27], s86 offen offset:1024 sc1\n"
        "v_mfma_f32_16x16x32_bf16 v[104:107], a[140:143], a[224:227], v[104:107]\n"
        "v_fma_f32 v223, v223, v39, v31\n"
        "v_mfma_f32_16x16x32_bf16 v[120:123], a[128:131], a[224:227], v[120:123]\n"
        "v_cvt_pk_bf16_f32 v216, v216, v217\n"
        "v_cvt_pk_bf16_f32 v217, v218, v219\n"
        "v_mfma_f32_16x16x32_bf16 v[68:71], a[132:135], a[228:231], v[68:71]\n"
        "v_cvt_pk_bf16_f32 v218, v220, v221\n"
        "v_mfma_f32_16x16x32_bf16 v[88:91], a[228:231], a[136:139], v[88:91]\n"
        "buffer_load_dwordx4 a[88:91], v3, s[24:27], s86 offen offset:2048 sc1\n"
        "v_cvt_pk_bf16_f32 v219, v222, v223\n"
        "v_mfma_f32_16x16x32_bf16 v[108:111], a[140:143], a[228:231], v[108:111]\n"
        "ds_write_b128 v12, v[216:219] offset:40960\n"
        "v_mfma_f32_16x16x32_bf16 v[124:127], a[128:131], a[228:231], v[124:127]\n"
        "v_fma_f32 v224, v224, v32, v24\n"
        "buffer_load_dwordx4 a[92:95], v3, s[24:27], s86 offen offset:3072 sc1\n"
        "v_mfma_f32_16x16x32_bf16 v[72:75], a[132:135], a[232:235], v[72:75]\n"
        "v_fma_f32 v225, v225, v33, v25\n"
        "v_mfma_f32_16x16x32_bf16 v[92:95], a[232:235], a[136:139], v[92:95]\n"
        "v_fma_f32 v226, v226, v34, v26\n"
        "v_mfma_f32_16x16x32_bf16 v[112:115], a[140:143], a[232:235], v[112:115]\n"
        "v_fma_f32 v227, v227, v35, v27\n"
        "buffer_load_dwordx4 a[96:99], v4, s[24:27], s86 offen sc1\n"
        "v_mfma_f32_16x16x32_bf16 v[128:131], a[128:131], a[232:235], v[128:131]\n"
        "s_waitcnt lgkmcnt(1)\n"
        "v_mfma_f32_16x16x32_bf16 v[56:59], a[148:151], a[236:239], v[56:59]\n"
        "s_waitcnt lgkmcnt(0)\n"
        "s_barrier\n"
        "ds_read_b128 a[216:219], v20 offset:51200\n"
        "v_mfma_f32_16x16x32_bf16 v[76:79], a[236:239], a[152:155], v[76:79]\n"
        "ds_read_b128 a[220:223], v20 offset:52224\n"
        "ds_read_b128 a[224:227], v20 offset:53248\n"
        "v_mfma_f32_16x16x32_bf16 v[96:99], a[156:159], a[236:239], v[96:99]\n"
        "ds_read_b128 a[228:231], v20 offset:54272\n"
        "ds_read_b128 a[232:235], v20 offset:55296\n"
        "v_mfma_f32_16x16x32_bf16 v[60:63], a[148:151], a[240:243], v[60:63]\n"
        "v_fma_f32 v228, v228, v36, v28\n"
        "v_fma_f32 v229, v229, v37, v29\n"
        "v_mfma_f32_16x16x32_bf16 v[80:83], a[240:243], a[152:155], v[80:83]\n"
        "v_fma_f32 v230, v230, v38, v30\n"
        "v_mfma_f32_16x16x32_bf16 v[100:103], a[156:159], a[240:243], v[100:103]\n"
        "s_mov_b32 s89, 8192\n"
        "v_fma_f32 v231, v231, v39, v31\n"
        "v_mfma_f32_16x16x32_bf16 v[116:119], a[144:147], a[240:243], v[116:119]\n"
        "v_cvt_pk_bf16_f32 v224, v224, v225\n"
        "v_mfma_f32_16x16x32_bf16 v[64:67], a[148:151], a[244:247], v[64:67]\n"
        "v_cvt_pk_bf16_f32 v225, v226, v227\n"
        "buffer_load_dwordx4 v[136:139], v2, s[24:27], s89 offen sc1\n"
        "v_mfma_f32_16x16x32_bf16 v[84:87], a[244:247], a[152:155], v[84:87]\n"
        "v_cvt_pk_bf16_f32 v226, v228, v229\n"
        "v_mfma_f32_16x16x32_bf16 v[104:107], a[156:159], a[244:247], v[104:107]\n"
        "v_cvt_pk_bf16_f32 v227, v230, v231\n"
        "ds_write_b128 v13, v[224:227] offset:40960\n"
        "v_mfma_f32_16x16x32_bf16 v[120:123], a[144:147], a[244:247], v[120:123]\n"
        "buffer_load_dwordx4 v[140:143], v2, s[24:27], s89 offen offset:1024 sc1\n"
        "v_mfma_f32_16x16x32_bf16 v[68:71], a[148:151], a[248:251], v[68:71]\n"
        "v_fma_f32 v232, v232, v32, v24\n"
        "v_fma_f32 v233, v233, v33, v25\n"
        "v_mfma_f32_16x16x32_bf16 v[88:91], a[248:251], a[152:155], v[88:91]\n"
        "v_fma_f32 v234, v234, v34, v26\n"
        "v_mfma_f32_16x16x32_bf16 v[108:111], a[156:159], a[248:251], v[108:111]\n"
        "buffer_store_dwordx4 v[132:135], v2, s[24:27], s3 offen sc1\n"
        "v_fma_f32 v235, v235, v35, v27\n"
        "v_mfma_f32_16x16x32_bf16 v[124:127], a[144:147], a[248:251], v[124:127]\n"
        "v_fma_f32 v236, v236, v36, v28\n"
        "v_mfma_f32_16x16x32_bf16 v[72:75], a[148:151], a[252:255], v[72:75]\n"
        "v_fma_f32 v237, v237, v37, v29\n"
        "buffer_load_dwordx4 a[100:103], v4, s[24:27], s86 offen offset:1024 sc1\n"
        "v_mfma_f32_16x16x32_bf16 v[92:95], a[252:255], a[152:155], v[92:95]\n"
        "v_fma_f32 v238, v238, v38, v30\n"
        "v_mfma_f32_16x16x32_bf16 v[112:115], a[156:159], a[252:255], v[112:115]\n"
        "v_fma_f32 v239, v239, v39, v31\n"
        "buffer_load_dwordx4 a[104:107], v4, s[24:27], s86 offen offset:2048 sc1\n"
        "v_mfma_f32_16x16x32_bf16 v[128:131], a[144:147], a[252:255], v[128:131]\n"
        "s_waitcnt vmcnt(37) lgkmcnt(1)\n"
        "v_mfma_f32_16x16x32_bf16 v[56:59], a[164:167], a[216:219], v[56:59]\n"
        "ds_read_b128 a[236:239], v21 offset:56320\n"
        "ds_read_b128 a[240:243], v21 offset:57344\n"
        "v_mfma_f32_16x16x32_bf16 v[76:79], a[216:219], a[168:171], v[76:79]\n"
        "ds_read_b128 a[244:247], v21 offset:58368\n"
        "ds_read_b128 a[248:251], v21 offset:59392\n"
        "v_mfma_f32_16x16x32_bf16 v[96:99], a[172:175], a[216:219], v[96:99]\n"
        "ds_read_b128 a[252:255], v21 offset:60416\n"
        "v_mfma_f32_16x16x32_bf16 v[60:63], a[164:167], a[220:223], v[60:63]\n"
        "v_cvt_pk_bf16_f32 v232, v232, v233\n"
        "v_cvt_pk_bf16_f32 v233, v234, v235\n"
        "v_mfma_f32_16x16x32_bf16 v[80:83], a[220:223], a[168:171], v[80:83]\n"
        "v_cvt_pk_bf16_f32 v234, v236, v237\n"
        "v_mfma_f32_16x16x32_bf16 v[100:103], a[172:175], a[220:223], v[100:103]\n"
        "v_cvt_pk_bf16_f32 v235, v238, v239\n"
        "buffer_load_dwordx4 a[108:111], v4, s[24:27], s86 offen offset:3072 sc1\n"
        "v_mfma_f32_16x16x32_bf16 v[116:119], a[160:163], a[220:223], v[116:119]\n"
        "ds_write_b128 v14, v[232:235] offset:40960\n"
        "v_mfma_f32_16x16x32_bf16 v[64:67], a[164:167], a[224:227], v[64:67]\n"
        "v_fma_f32 v240, v240, v32, v24\n"
        "v_fma_f32 v241, v241, v33, v25\n"
        "v_mfma_f32_16x16x32_bf16 v[84:87], a[224:227], a[168:171], v[84:87]\n"
        "buffer_load_dwordx4 a[112:115], v5, s[24:27], s86 offen sc1\n"
        "v_mfma_f32_16x16x32_bf16 v[104:107], a[172:175], a[224:227], v[104:107]\n"
        "v_fma_f32 v242, v242, v34, v26\n"
        "v_mfma_f32_16x16x32_bf16 v[120:123], a[160:163], a[224:227], v[120:123]\n"
        "v_fma_f32 v243, v243, v35, v27\n"
        "v_fma_f32 v244, v244, v36, v28\n"
        "v_mfma_f32_16x16x32_bf16 v[68:71], a[164:167], a[228:231], v[68:71]\n"
        "v_fma_f32 v245, v245, v37, v29\n"
        "v_mfma_f32_16x16x32_bf16 v[88:91], a[228:231], a[168:171], v[88:91]\n"
        "buffer_load_dwordx4 a[116:119], v5, s[24:27], s86 offen offset:1024 sc1\n"
        "v_fma_f32 v246, v246, v38, v30\n"
        "v_mfma_f32_16x16x32_bf16 v[108:111], a[172:175], a[228:231], v[108:111]\n"
        "v_fma_f32 v247, v247, v39, v31\n"
        "v_mfma_f32_16x16x32_bf16 v[124:127], a[160:163], a[228:231], v[124:127]\n"
        "v_cvt_pk_bf16_f32 v240, v240, v241\n"
        "buffer_load_dwordx4 a[120:123], v5, s[24:27], s86 offen offset:2048 sc1\n"
        "v_mfma_f32_16x16x32_bf16 v[72:75], a[164:167], a[232:235], v[72:75]\n"
        "v_cvt_pk_bf16_f32 v241, v242, v243\n"
        "v_mfma_f32_16x16x32_bf16 v[92:95], a[232:235], a[168:171], v[92:95]\n"
        "v_cvt_pk_bf16_f32 v242, v244, v245\n"
        "v_mfma_f32_16x16x32_bf16 v[112:115], a[172:175], a[232:235], v[112:115]\n"
        "v_cvt_pk_bf16_f32 v243, v246, v247\n"
        "buffer_load_dwordx4 a[124:127], v5, s[24:27], s86 offen offset:3072 sc1\n"
        "v_mfma_f32_16x16x32_bf16 v[128:131], a[160:163], a[232:235], v[128:131]\n"
        "s_waitcnt vmcnt(38) lgkmcnt(1)\n"
        "v_mfma_f32_16x16x32_bf16 v[56:59], a[180:183], a[236:239], v[56:59]\n"
        "v_add_u32 v18, 61440, v18\n"
        "v_add_u32 v19, 61440, v19\n"
        "v_mfma_f32_16x16x32_bf16 v[76:79], a[236:239], a[184:187], v[76:79]\n"
        "v_add_u32 v20, 61440, v20\n"
        "v_add_u32 v21, 61440, v21\n"
        "v_mfma_f32_16x16x32_bf16 v[96:99], a[188:191], a[236:239], v[96:99]\n"
        "ds_read_b128 a[216:219], v18\n"
        "ds_read_b128 a[220:223], v18 offset:1024\n"
        "v_mfma_f32_16x16x32_bf16 v[60:63], a[180:183], a[240:243], v[60:63]\n"
        "ds_read_b128 a[224:227], v18 offset:2048\n"
        "ds_read_b128 a[228:231], v18 offset:3072\n"
        "v_mfma_f32_16x16x32_bf16 v[80:83], a[240:243], a[184:187], v[80:83]\n"
        "ds_read_b128 a[232:235], v18 offset:4096\n"
        "s_waitcnt vmcnt(9)\n"
        "v_cmp_ne_u64_e64 s[66:67], v[136:137], v[6:7]\n"
        "v_mfma_f32_16x16x32_bf16 v[100:103], a[188:191], a[240:243], v[100:103]\n"
        "v_cmp_ne_u64_e64 s[68:69], v[138:139], v[6:7]\n"
        "s_waitcnt vmcnt(8)\n"
        "v_cmp_ne_u64_e64 s[70:71], v[140:141], v[6:7]\n"
        "v_mfma_f32_16x16x32_bf16 v[116:119], a[176:179], a[240:243], v[116:119]\n"
        "v_cmp_ne_u64_e64 s[72:73], v[142:143], v[6:7]\n"
        "s_or_b64 s[66:67], s[66:67], s[68:69]\n"
        "v_mfma_f32_16x16x32_bf16 v[64:67], a[180:183], a[244:247], v[64:67]\n"
        "s_or_b64 s[66:67], s[66:67], s[70:71]\n"
        "s_or_b64 s[66:67], s[66:67], s[72:73]\n"
        "v_mfma_f32_16x16x32_bf16 v[84:87], a[244:247], a[184:187], v[84:87]\n"
        "s_cmp_eq_u64 s[66:67], 0\n"
        "s_cbranch_scc0 .Ls15\n"
        ".Lb15:\n"
        "v_mfma_f32_16x16x32_bf16 v[104:107], a[188:191], a[244:247], v[104:107]\n"
        "ds_write_b128 v15, v[240:243] offset:40960\n"
        "v_fma_f32 v248, v248, v32, v24\n"
        "v_mfma_f32_16x16x32_bf16 v[120:123], a[176:179], a[244:247], v[120:123]\n"
        "v_fma_f32 v249, v249, v33, v25\n"
        "buffer_store_dwordx4 v[132:135], v2, s[24:27], s3 offen offset:1024 sc1\n"
        "v_mfma_f32_16x16x32_bf16 v[68:71], a[180:183], a[248:251], v[68:71]\n"
        "v_fma_f32 v250, v250, v34, v26\n"
        "v_fma_f32 v251, v251, v35, v27\n"
        "v_mfma_f32_16x16x32_bf16 v[88:91], a[248:251], a[184:187], v[88:91]\n"
        "v_fma_f32 v252, v252, v36, v28\n"
        "buffer_load_dwordx4 a[192:195], v2, s[24:27], s59 offen sc1\n"
        "v_mfma_f32_16x16x32_bf16 v[108:111], a[188:191], a[248:251], v[108:111]\n"
        "v_fma_f32 v253, v253, v37, v29\n"
        "v_fma_f32 v254, v254, v38, v30\n"
        "v_mfma_f32_16x16x32_bf16 v[124:127], a[176:179], a[248:251], v[124:127]\n"
        "v_fma_f32 v255, v255, v39, v31\n"
        "buffer_load_dwordx4 a[196:199], v2, s[24:27], s59 offen offset:1024 sc1\n"
        "v_mfma_f32_16x16x32_bf16 v[72:75], a[180:183], a[252:255], v[72:75]\n"
        "v_cvt_pk_bf16_f32 v248, v248, v249\n"
        "v_cvt_pk_bf16_f32 v249, v250, v251\n"
        "v_mfma_f32_16x16x32_bf16 v[92:95], a[252:255], a[184:187], v[92:95]\n"
        "v_cvt_pk_bf16_f32 v250, v252, v253\n"
        "buffer_load_dwordx4 a[200:203], v2, s[24:27], s59 offen offset:2048 sc1\n"
        "v_mfma_f32_16x16x32_bf16 v[112:115], a[188:191], a[252:255], v[112:115]\n"
        "v_cvt_pk_bf16_f32 v251, v254, v255\n"
        "ds_write_b128 v16, v[248:251] offset:40960\n"
        "v_mfma_f32_16x16x32_bf16 v[128:131], a[176:179], a[252:255], v[128:131]\n"
        "buffer_load_dwordx4 a[204:207], v2, s[24:27], s59 offen offset:3072 sc1\n"
        "s_waitcnt lgkmcnt(2)\n"
        "v_mfma_f32_16x16x32_bf16 v[56:59], a[4:7], a[216:219], v[56:59]\n"
        "ds_read_b128 a[236:239], v19 offset:5120\n"
        "ds_read_b128 a[240:243], v19 offset:6144\n"
        "v_mfma_f32_16x16x32_bf16 v[76:79], a[216:219], a[8:11], v[76:79]\n"
        "ds_read_b128 a[244:247], v19 offset:7168\n"
        "ds_read_b128 a[248:251], v19 offset:8192\n"
        "v_mfma_f32_16x16x32_bf16 v[96:99], a[12:15], a[216:219], v[96:99]\n"
        "ds_read_b128 a[252:255], v19 offset:9216\n"
        "v_mfma_f32_16x16x32_bf16 v[60:63], a[4:7], a[220:223], v[60:63]\n"
        "buffer_store_dwordx4 v[132:135], v2, s[24:27], s3 offen offset:2048 sc1\n"
        "v_mfma_f32_16x16x32_bf16 v[80:83], a[220:223], a[8:11], v[80:83]\n"
        "v_mfma_f32_16x16x32_bf16 v[100:103], a[12:15], a[220:223], v[100:103]\n"
        "v_mfma_f32_16x16x32_bf16 v[116:119], a[0:3], a[220:223], v[116:119]\n"
        "buffer_load_dwordx4 a[208:211], v3, s[24:27], s59 offen sc1\n"
        "v_mfma_f32_16x16x32_bf16 v[64:67], a[4:7], a[224:227], v[64:67]\n"
        "v_mfma_f32_16x16x32_bf16 v[84:87], a[224:227], a[8:11], v[84:87]\n"
        "v_mfma_f32_16x16x32_bf16 v[104:107], a[12:15], a[224:227], v[104:107]\n"
        "buffer_load_dwordx4 a[212:215], v3, s[24:27], s59 offen offset:1024 sc1\n"
        "v_mfma_f32_16x16x32_bf16 v[120:123], a[0:3], a[224:227], v[120:123]\n"
        "v_mfma_f32_16x16x32_bf16 v[68:71], a[4:7], a[228:231], v[68:71]\n"
        "v_mfma_f32_16x16x32_bf16 v[88:91], a[228:231], a[8:11], v[88:91]\n"
        "buffer_load_dwordx4 a[128:131], v3, s[24:27], s59 offen offset:2048 sc1\n"
        "v_mfma_f32_16x16x32_bf16 v[108:111], a[12:15], a[228:231], v[108:111]\n"
        "v_mfma_f32_16x16x32_bf16 v[124:127], a[0:3], a[228:231], v[124:127]\n"
        "v_mfma_f32_16x16x32_bf16 v[72:75], a[4:7], a[232:235], v[72:75]\n"
        "buffer_load_dwordx4 a[132:135], v3, s[24:27], s59 offen offset:3072 sc1\n"
        "v_mfma_f32_16x16x32_bf16 v[92:95], a[232:235], a[8:11], v[92:95]\n"
        "v_mfma_f32_16x16x32_bf16 v[112:115], a[12:15], a[232:235], v[112:115]\n"
        "v_mfma_f32_16x16x32_bf16 v[128:131], a[0:3], a[232:235], v[128:131]\n"
        "s_waitcnt lgkmcnt(0)\n"
        "v_mfma_f32_16x16x32_bf16 v[56:59], a[20:23], a[236:239], v[56:59]\n"
        "s_barrier\n"
        "ds_read_b128 a[216:219], v20 offset:10240\n"
        "v_mfma_f32_16x16x32_bf16 v[76:79], a[236:239], a[24:27], v[76:79]\n"
        "ds_read_b128 a[220:223], v20 offset:11264\n"
        "ds_read_b128 a[224:227], v20 offset:12288\n"
        "v_mfma_f32_16x16x32_bf16 v[96:99], a[28:31], a[236:239], v[96:99]\n"
        "ds_read_b128 a[228:231], v20 offset:13312\n"
        "ds_read_b128 a[232:235], v20 offset:14336\n"
        "v_mfma_f32_16x16x32_bf16 v[60:63], a[20:23], a[240:243], v[60:63]\n"
        "buffer_load_dwordx4 a[136:139], v4, s[24:27], s59 offen sc1\n"
        "v_mfma_f32_16x16x32_bf16 v[80:83], a[240:243], a[24:27], v[80:83]\n"
        "v_mfma_f32_16x16x32_bf16 v[100:103], a[28:31], a[240:243], v[100:103]\n"
        "v_mfma_f32_16x16x32_bf16 v[116:119], a[16:19], a[240:243], v[116:119]\n"
        "buffer_load_dwordx4 a[140:143], v4, s[24:27], s59 offen offset:1024 sc1\n"
        "v_mfma_f32_16x16x32_bf16 v[64:67], a[20:23], a[244:247], v[64:67]\n"
        "v_mfma_f32_16x16x32_bf16 v[84:87], a[244:247], a[24:27], v[84:87]\n"
        "v_mfma_f32_16x16x32_bf16 v[104:107], a[28:31], a[244:247], v[104:107]\n"
        "buffer_load_dwordx4 a[144:147], v4, s[24:27], s59 offen offset:2048 sc1\n"
        "v_mfma_f32_16x16x32_bf16 v[120:123], a[16:19], a[244:247], v[120:123]\n"
        "v_mfma_f32_16x16x32_bf16 v[68:71], a[20:23], a[248:251], v[68:71]\n"
        "v_mfma_f32_16x16x32_bf16 v[88:91], a[248:251], a[24:27], v[88:91]\n"
        "buffer_load_dwordx4 a[148:151], v4, s[24:27], s59 offen offset:3072 sc1\n"
        "v_mfma_f32_16x16x32_bf16 v[108:111], a[28:31], a[248:251], v[108:111]\n"
        "v_mfma_f32_16x16x32_bf16 v[124:127], a[16:19], a[248:251], v[124:127]\n"
        "v_mfma_f32_16x16x32_bf16 v[72:75], a[20:23], a[252:255], v[72:75]\n"
        "buffer_load_dwordx4 a[152:155], v5, s[24:27], s59 offen sc1\n"
        "v_mfma_f32_16x16x32_bf16 v[92:95], a[252:255], a[24:27], v[92:95]\n"
        "v_mfma_f32_16x16x32_bf16 v[112:115], a[28:31], a[252:255], v[112:115]\n"
        "v_mfma_f32_16x16x32_bf16 v[128:131], a[16:19], a[252:255], v[128:131]\n"
        "s_waitcnt lgkmcnt(0)\n"
        "v_mfma_f32_16x16x32_bf16 v[56:59], a[36:39], a[216:219], v[56:59]\n"
        "ds_read_b128 a[236:239], v21 offset:15360\n"
        "ds_read_b128 a[240:243], v21 offset:16384\n"
        "v_mfma_f32_16x16x32_bf16 v[76:79], a[216:219], a[40:43], v[76:79]\n"
        "ds_read_b128 a[244:247], v21 offset:17408\n"
        "ds_read_b128 a[248:251], v21 offset:18432\n"
        "v_mfma_f32_16x16x32_bf16 v[96:99], a[44:47], a[216:219], v[96:99]\n"
        "ds_read_b128 a[252:255], v21 offset:19456\n"
        "v_mfma_f32_16x16x32_bf16 v[60:63], a[36:39], a[220:223], v[60:63]\n"
        "v_mov_b32 v139, 0\n"
        "v_mov_b32 v143, 0\n"
        "v_mfma_f32_16x16x32_bf16 v[80:83], a[220:223], a[40:43], v[80:83]\n"
        "v_mov_b32 v147, 0\n"
        "v_mfma_f32_16x16x32_bf16 v[100:103], a[44:47], a[220:223], v[100:103]\n"
        "buffer_store_dwordx4 v[132:135], v2, s[24:27], s3 offen offset:3072 sc1\n"
        "v_mfma_f32_16x16x32_bf16 v[116:119], a[32:35], a[220:223], v[116:119]\n"
        "v_mov_b32 v151, 0\n"
        "v_mfma_f32_16x16x32_bf16 v[64:67], a[36:39], a[224:227], v[64:67]\n"
        "v_mov_b32 v155, 0\n"
        "v_mfma_f32_16x16x32_bf16 v[84:87], a[224:227], a[40:43], v[84:87]\n"
        "v_mov_b32 v159, 0\n"
        "v_mfma_f32_16x16x32_bf16 v[104:107], a[44:47], a[224:227], v[104:107]\n"
        "buffer_load_dwordx4 a[156:159], v5, s[24:27], s59 offen offset:1024 sc1\n"
        "v_mfma_f32_16x16x32_bf16 v[120:123], a[32:35], a[224:227], v[120:123]\n"
        "v_mov_b32 v163, 0\n"
        "v_mfma_f32_16x16x32_bf16 v[68:71], a[36:39], a[228:231], v[68:71]\n"
        "v_mov_b32 v167, 0\n"
        "v_mfma_f32_16x16x32_bf16 v[88:91], a[228:231], a[40:43], v[88:91]\n"
        "v_mov_b32 v171, 0\n"
        "v_mfma_f32_16x16x32_bf16 v[108:111], a[44:47], a[228:231], v[108:111]\n"
        "buffer_load_dwordx4 a[160:163], v5, s[24:27], s59 offen offset:2048 sc1\n"
        "v_mfma_f32_16x16x32_bf16 v[124:127], a[32:35], a[228:231], v[124:127]\n"
        "v_mov_b32 v208, 0\n"
        "v_mfma_f32_16x16x32_bf16 v[72:75], a[36:39], a[232:235], v[72:75]\n"
        "v_mov_b32 v209, 0\n"
        "v_mfma_f32_16x16x32_bf16 v[92:95], a[232:235], a[40:43], v[92:95]\n"
        "v_mov_b32 v211, 0\n"
        "v_mfma_f32_16x16x32_bf16 v[112:115], a[44:47], a[232:235], v[112:115]\n"
        "buffer_load_dwordx4 a[164:167], v5, s[24:27], s59 offen offset:3072 sc1\n"
        "v_mfma_f32_16x16x32_bf16 v[128:131], a[32:35], a[232:235], v[128:131]\n"
        "s_waitcnt lgkmcnt(0)\n"
        "v_mfma_f32_16x16x32_bf16 v[56:59], a[52:55], a[236:239], v[56:59]\n"
        "ds_read_b128 a[216:219], v18 offset:20480\n"
        "ds_read_b128 a[220:223], v18 offset:21504\n"
        "v_mfma_f32_16x16x32_bf16 v[76:79], a[236:239], a[56:59], v[76:79]\n"
        "ds_read_b128 a[224:227], v18 offset:22528\n"
        "ds_read_b128 a[228:231], v18 offset:23552\n"
        "v_mfma_f32_16x16x32_bf16 v[96:99], a[60:63], a[236:239], v[96:99]\n"
        "ds_read_b128 a[232:235], v18 offset:24576\n"
        "v_mfma_f32_16x16x32_bf16 v[60:63], a[52:55], a[240:243], v[60:63]\n"
        "v_bfe_u32 v24, v2, 4, 2\n"
        "v_bfe_u32 v25, v2, 4, 4\n"
        "v_mfma_f32_16x16x32_bf16 v[80:83], a[240:243], a[56:59], v[80:83]\n"
        "buffer_store_dwordx4 v[132:135], v3, s[24:27], s3 offen sc1\n"
        "v_mfma_f32_16x16x32_bf16 v[100:103], a[60:63], a[240:243], v[100:103]\n"
        "v_and_b32 v26, 1, v25\n"
        "v_mfma_f32_16x16x32_bf16 v[116:119], a[48:51], a[240:243], v[116:119]\n"
        "v_lshl_add_u32 v27, v10, 2, v26\n"
        "v_mfma_f32_16x16x32_bf16 v[64:67], a[52:55], a[244:247], v[64:67]\n"
        "s_add_u32 s86, s59, 16384\n"
        "v_mfma_f32_16x16x32_bf16 v[84:87], a[244:247], a[56:59], v[84:87]\n"
        "v_cmp_eq_u32 vcc, v24, v10\n"
        "v_mfma_f32_16x16x32_bf16 v[104:107], a[60:63], a[244:247], v[104:107]\n"
        "v_cmp_eq_u32_e64 s[78:79], 1, v26\n"
        "buffer_load_dwordx4 a[168:171], v2, s[24:27], s86 offen sc1\n"
        "v_mfma_f32_16x16x32_bf16 v[120:123], a[48:51], a[244:247], v[120:123]\n"
        "v_cmp_le_u32_e64 s[80:81], v27, v25\n"
        "v_mfma_f32_16x16x32_bf16 v[68:71], a[52:55], a[248:251], v[68:71]\n"
        "v_add_u32 v27, 2, v27\n"
        "v_mfma_f32_16x16x32_bf16 v[88:91], a[248:251], a[56:59], v[88:91]\n"
        "buffer_load_dwordx4 a[172:175], v2, s[24:27], s86 offen offset:1024 sc1\n"
        "v_mfma_f32_16x16x32_bf16 v[108:111], a[60:63], a[248:251], v[108:111]\n"
        "v_cndmask_b32_e64 v210, 0, 1.0, vcc\n"
        "v_mfma_f32_16x16x32_bf16 v[124:127], a[48:51], a[248:251], v[124:127]\n"
        "v_cmp_le_u32_e64 s[82:83], v27, v25\n"
        "v_mfma_f32_16x16x32_bf16 v[72:75], a[52:55], a[252:255], v[72:75]\n"
        "buffer_load_dwordx4 a[176:179], v2, s[24:27], s86 offen offset:2048 sc1\n"
        "v_mov_b32 v24, 0x0c0c0100\n"
        "v_mfma_f32_16x16x32_bf16 v[92:95], a[252:255], a[56:59], v[92:95]\n"
        "v_mov_b32 v25, 0x01000c0c\n"
        "v_mfma_f32_16x16x32_bf16 v[112:115], a[60:63], a[252:255], v[112:115]\n"
        "buffer_load_dwordx4 a[180:183], v2, s[24:27], s86 offen offset:3072 sc1\n"
        "v_mfma_f32_16x16x32_bf16 v[128:131], a[48:51], a[252:255], v[128:131]\n"
        "s_waitcnt lgkmcnt(0)\n"
        "v_mfma_f32_16x16x32_bf16 v[56:59], a[68:71], a[216:219], v[56:59]\n"
        "ds_read_b128 a[236:239], v19 offset:25600\n"
        "ds_read_b128 a[240:243], v19 offset:26624\n"
        "v_mfma_f32_16x16x32_bf16 v[76:79], a[216:219], a[72:75], v[76:79]\n"
        "ds_read_b128 a[244:247], v19 offset:27648\n"
        "ds_read_b128 a[248:251], v19 offset:28672\n"
        "v_mfma_f32_16x16x32_bf16 v[96:99], a[76:79], a[216:219], v[96:99]\n"
        "ds_read_b128 a[252:255], v19 offset:29696\n"
        "v_mfma_f32_16x16x32_bf16 v[60:63], a[68:71], a[220:223], v[60:63]\n"
        "v_mov_b32 v26, 0x0c0c0302\n"
        "v_mov_b32 v27, 0x03020c0c\n"
        "v_mfma_f32_16x16x32_bf16 v[80:83], a[220:223], a[72:75], v[80:83]\n"
        "v_mov_b32 v28, 0x0c0c0c0c\n"
        "v_mfma_f32_16x16x32_bf16 v[100:103], a[76:79], a[220:223], v[100:103]\n"
        "buffer_load_dwordx4 a[184:187], v3, s[24:27], s86 offen sc1\n"
        "v_mfma_f32_16x16x32_bf16 v[116:119], a[64:67], a[220:223], v[116:119]\n"
        "v_cndmask_b32_e64 v24, v24, v25, s[78:79]\n"
        "v_mfma_f32_16x16x32_bf16 v[64:67], a[68:71], a[224:227], v[64:67]\n"
        "v_cndmask_b32_e64 v26, v26, v27, s[78:79]\n"
        "v_mfma_f32_16x16x32_bf16 v[84:87], a[224:227], a[72:75], v[84:87]\n"
        "buffer_load_dwordx4 a[188:191], v3, s[24:27], s86 offen offset:1024 sc1\n"
        "v_mfma_f32_16x16x32_bf16 v[104:107], a[76:79], a[224:227], v[104:107]\n"
        "v_cndmask_b32_e64 v212, v28, v24, s[80:81]\n"
        "v_mfma_f32_16x16x32_bf16 v[120:123], a[64:67], a[224:227], v[120:123]\n"
        "v_cndmask_b32_e64 v213, v28, v26, s[82:83]\n"
        "v_mfma_f32_16x16x32_bf16 v[68:71], a[68:71], a[228:231], v[68:71]\n"
        "v_cndmask_b32_e64 v214, v24, v28, s[80:81]\n"
        "v_mfma_f32_16x16x32_bf16 v[88:91], a[228:231], a[72:75], v[88:91]\n"
        "buffer_load_dwordx4 a[0:3], v3, s[24:27], s86 offen offset:2048 sc1\n"
        "v_mfma_f32_16x16x32_bf16 v[108:111], a[76:79], a[228:231], v[108:111]\n"
        "v_cndmask_b32_e64 v215, v26, v28, s[82:83]\n"
        "v_mov_b32 v28, 0xcf854780\n"
        "v_mfma_f32_16x16x32_bf16 v[124:127], a[64:67], a[228:231], v[124:127]\n"
        "buffer_load_dwordx4 a[4:7], v3, s[24:27], s86 offen offset:3072 sc1\n"
        "v_mfma_f32_16x16x32_bf16 v[72:75], a[68:71], a[232:235], v[72:75]\n"
        "s_cmp_eq_u32 s53, 0\n"
        "s_cselect_b64 s[84:85], -1, 0\n"
        "v_mfma_f32_16x16x32_bf16 v[92:95], a[232:235], a[72:75], v[92:95]\n"
        "v_mfma_f32_16x16x32_bf16 v[112:115], a[76:79], a[232:235], v[112:115]\n"
        "buffer_load_dwordx4 a[8:11], v4, s[24:27], s86 offen sc1\n"
        "v_mfma_f32_16x16x32_bf16 v[128:131], a[64:67], a[232:235], v[128:131]\n"
        "s_waitcnt lgkmcnt(0)\n"
        "v_mfma_f32_16x16x32_bf16 v[56:59], a[84:87], a[236:239], v[56:59]\n"
        "ds_read_b128 a[216:219], v20 offset:30720\n"
        "ds_read_b128 a[220:223], v20 offset:31744\n"
        "v_mfma_f32_16x16x32_bf16 v[76:79], a[236:239], a[88:91], v[76:79]\n"
        "ds_read_b128 a[224:227], v20 offset:32768\n"
        "ds_read_b128 a[228:231], v20 offset:33792\n"
        "v_mfma_f32_16x16x32_bf16 v[96:99], a[92:95], a[236:239], v[96:99]\n"
        "ds_read_b128 a[232:235], v20 offset:34816\n"
        "v_mfma_f32_16x16x32_bf16 v[60:63], a[84:87], a[240:243], v[60:63]\n"
        "buffer_store_dwordx4 v[132:135], v3, s[24:27], s3 offen offset:1024 sc1\n"
        "v_mfma_f32_16x16x32_bf16 v[80:83], a[240:243], a[88:91], v[80:83]\n"
        "v_mfma_f32_16x16x32_bf16 v[100:103], a[92:95], a[240:243], v[100:103]\n"
        "s_add_u32 s87, s87, 7680\n"
        "v_mfma_f32_16x16x32_bf16 v[116:119], a[80:83], a[240:243], v[116:119]\n"
        "v_mfma_f32_16x16x32_bf16 v[64:67], a[84:87], a[244:247], v[64:67]\n"
        "v_mfma_f32_16x16x32_bf16 v[84:87], a[244:247], a[88:91], v[84:87]\n"
        "buffer_load_dwordx4 v[216:219], v23, s[24:27], s87 offen sc1\n"
        "v_mfma_f32_16x16x32_bf16 v[104:107], a[92:95], a[244:247], v[104:107]\n"
        "v_mfma_f32_16x16x32_bf16 v[120:123], a[80:83], a[244:247], v[120:123]\n"
        "buffer_load_dwordx4 v[220:223], v23, s[24:27], s87 offen offset:256 sc1\n"
        "v_mfma_f32_16x16x32_bf16 v[68:71], a[84:87], a[248:251], v[68:71]\n"
        "v_mfma_f32_16x16x32_bf16 v[88:91], a[248:251], a[88:91], v[88:91]\n"
        "v_mfma_f32_16x16x32_bf16 v[108:111], a[92:95], a[248:251], v[108:111]\n"
        "buffer_load_dwordx4 v[224:227], v23, s[24:27], s87 offen offset:512 sc1\n"
        "v_mfma_f32_16x16x32_bf16 v[124:127], a[80:83], a[248:251], v[124:127]\n"
        "v_mfma_f32_16x16x32_bf16 v[72:75], a[84:87], a[252:255], v[72:75]\n"
        "buffer_load_dwordx4 v[228:231], v23, s[24:27], s87 offen offset:768 sc1\n"
        "v_mfma_f32_16x16x32_bf16 v[92:95], a[252:255], a[88:91], v[92:95]\n"
        "v_mfma_f32_16x16x32_bf16 v[112:115], a[92:95], a[252:255], v[112:115]\n"
        "v_mfma_f32_16x16x32_bf16 v[128:131], a[80:83], a[252:255], v[128:131]\n"
        "s_waitcnt vmcnt(38) lgkmcnt(0)\n"
        "v_mfma_f32_16x16x32_bf16 v[56:59], a[100:103], a[216:219], v[56:59]\n"
        "ds_read_b128 a[236:239], v21 offset:35840\n"
        "ds_read_b128 a[240:243], v21 offset:36864\n"
        "v_mfma_f32_16x16x32_bf16 v[76:79], a[216:219], a[104:107], v[76:79]\n"
        "ds_read_b128 a[244:247], v21 offset:37888\n"
        "ds_read_b128 a[248:251], v21 offset:38912\n"
        "v_mfma_f32_16x16x32_bf16 v[96:99], a[108:111], a[216:219], v[96:99]\n"
        "ds_read_b128 a[252:255], v21 offset:39936\n"
        "v_mfma_f32_16x16x32_bf16 v[60:63], a[100:103], a[220:223], v[60:63]\n"
        "buffer_load_dwordx4 v[232:235], v23, s[24:27], s87 offen offset:1024 sc1\n"
        "s_lshr_b32 s87, s52, 1\n"
        "v_mfma_f32_16x16x32_bf16 v[80:83], a[220:223], a[104:107], v[80:83]\n"
        "s_mul_i32 s87, s87, 7168\n"
        "v_mfma_f32_16x16x32_bf16 v[100:103], a[108:111], a[220:223], v[100:103]\n"
        "s_lshl_b32 s88, s52, 8\n"
        "v_mfma_f32_16x16x32_bf16 v[116:119], a[96:99], a[220:223], v[116:119]\n"
        "s_add_u32 s87, s87, s88\n"
        "v_mfma_f32_16x16x32_bf16 v[64:67], a[100:103], a[224:227], v[64:67]\n"
        "s_mul_i32 s88, s54, 320\n"
        "s_add_u32 s87, s87, s88\n"
        "v_mfma_f32_16x16x32_bf16 v[84:87], a[224:227], a[104:107], v[84:87]\n"
        "s_add_u32 s87, s87, 37888\n"
        "v_mfma_f32_16x16x32_bf16 v[104:107], a[108:111], a[224:227], v[104:107]\n"
        "buffer_load_dwordx4 v[236:239], v23, s[24:27], s87 offen sc1\n"
        "v_mfma_f32_16x16x32_bf16 v[120:123], a[96:99], a[224:227], v[120:123]\n"
        "s_lshl_b32 s67, s53, 2\n"
        "v_mfma_f32_16x16x32_bf16 v[68:71], a[100:103], a[228:231], v[68:71]\n"
        "s_add_u32 s67, s67, s52\n"
        "s_mul_i32 s67, s67, 20480\n"
        "v_mfma_f32_16x16x32_bf16 v[88:91], a[228:231], a[104:107], v[88:91]\n"
        "s_mul_i32 s68, s54, 160\n"
        "v_mfma_f32_16x16x32_bf16 v[108:111], a[108:111], a[228:231], v[108:111]\n"
        "s_add_u32 s67, s67, s68\n"
        "v_mfma_f32_16x16x32_bf16 v[124:127], a[96:99], a[228:231], v[124:127]\n"
        "v_lshl_add_u32 v8, v11, 1, v10\n"
        "v_mfma_f32_16x16x32_bf16 v[72:75], a[100:103], a[232:235], v[72:75]\n"
        "v_mad_u32_u24 v8, v8, 40, s67\n"
        "v_mfma_f32_16x16x32_bf16 v[92:95], a[232:235], a[104:107], v[92:95]\n"
        "buffer_load_dwordx4 v[240:243], v8, s[28:31], 0 offen\n"
        "buffer_load_dwordx4 v[244:247], v8, s[28:31], 0 offen offset:16\n"
        "v_mfma_f32_16x16x32_bf16 v[112:115], a[108:111], a[232:235], v[112:115]\n"
        "buffer_load_dwordx2 v[248:249], v8, s[28:31], 0 offen offset:32\n"
        "v_mfma_f32_16x16x32_bf16 v[128:131], a[96:99], a[232:235], v[128:131]\n"
        "s_waitcnt vmcnt(39) lgkmcnt(0)\n"
        "v_mfma_f32_16x16x32_bf16 v[56:59], a[116:119], a[236:239], v[56:59]\n"
        "v_mfma_f32_16x16x32_bf16 v[76:79], a[236:239], a[120:123], v[76:79]\n"
        "v_mfma_f32_16x16x32_bf16 v[96:99], a[124:127], a[236:239], v[96:99]\n"
        "v_mfma_f32_16x16x32_bf16 v[60:63], a[116:119], a[240:243], v[60:63]\n"
        "v_mfma_f32_16x16x32_bf16 v[80:83], a[240:243], a[120:123], v[80:83]\n"
        "v_mfma_f32_16x16x32_bf16 v[100:103], a[124:127], a[240:243], v[100:103]\n"
        "v_mfma_f32_16x16x32_bf16 v[116:119], a[112:115], a[240:243], v[116:119]\n"
        "v_mfma_f32_16x16x32_bf16 v[64:67], a[116:119], a[244:247], v[64:67]\n"
        "v_mfma_f32_16x16x32_bf16 v[84:87], a[244:247], a[120:123], v[84:87]\n"
        "v_mfma_f32_16x16x32_bf16 v[104:107], a[124:127], a[244:247], v[104:107]\n"
        "v_mfma_f32_16x16x32_bf16 v[120:123], a[112:115], a[244:247], v[120:123]\n"
        "v_mfma_f32_16x16x32_bf16 v[68:71], a[116:119], a[248:251], v[68:71]\n"
        "v_mfma_f32_16x16x32_bf16 v[88:91], a[248:251], a[120:123], v[88:91]\n"
        "v_mfma_f32_16x16x32_bf16 v[108:111], a[124:127], a[248:251], v[108:111]\n"
        "v_mfma_f32_16x16x32_bf16 v[124:127], a[112:115], a[248:251], v[124:127]\n"
        "v_mfma_f32_16x16x32_bf16 v[72:75], a[116:119], a[252:255], v[72:75]\n"
        "v_mfma_f32_16x16x32_bf16 v[92:95], a[252:255], a[120:123], v[92:95]\n"
        "v_mfma_f32_16x16x32_bf16 v[112:115], a[124:127], a[252:255], v[112:115]\n"
        "v_mfma_f32_16x16x32_bf16 v[128:131], a[112:115], a[252:255], v[128:131]\n"
        "buffer_load_dwordx4 a[12:15], v2, s[24:27], s60 offen sc1\n"
        "v_cvt_pk_bf16_f32 v136, v56, v57\n"
        "v_cvt_pk_bf16_f32 v137, v58, v59\n"
        "v_cvt_pk_bf16_f32 v138, v97, v98\n"
        "v_cvt_pk_bf16_f32 v140, v60, v61\n"
        "v_cvt_pk_bf16_f32 v141, v62, v63\n"
        "v_cvt_pk_bf16_f32 v142, v101, v102\n"
        "v_cvt_pk_bf16_f32 v156, v116, v117\n"
        "v_cvt_pk_bf16_f32 v157, v118, v119\n"
        "v_cvt_pk_bf16_f32 v158, v100, 0\n"
        "v_cvt_pk_bf16_f32 v144, v64, v65\n"
        "v_cvt_pk_bf16_f32 v145, v66, v67\n"
        "v_cvt_pk_bf16_f32 v146, v105, v106\n"
        "v_cvt_pk_bf16_f32 v160, v120, v121\n"
        "v_cvt_pk_bf16_f32 v161, v122, v123\n"
        "v_cvt_pk_bf16_f32 v162, v104, 0\n"
        "v_cvt_pk_bf16_f32 v148, v68, v69\n"
        "v_cvt_pk_bf16_f32 v149, v70, v71\n"
        "v_cvt_pk_bf16_f32 v150, v109, v110\n"
        "v_cvt_pk_bf16_f32 v164, v124, v125\n"
        "v_cvt_pk_bf16_f32 v165, v126, v127\n"
        "v_cvt_pk_bf16_f32 v166, v108, 0\n"
        "v_cvt_pk_bf16_f32 v152, v72, v73\n"
        "v_cvt_pk_bf16_f32 v153, v74, v75\n"
        "v_cvt_pk_bf16_f32 v154, v113, v114\n"
        "v_cvt_pk_bf16_f32 v168, v128, v129\n"
        "v_cvt_pk_bf16_f32 v169, v130, v131\n"
        "v_cvt_pk_bf16_f32 v170, v112, 0\n"
        "v_mfma_f32_16x16x32_bf16 v[60:63], v[140:143], v[156:159], 0\n"
        "buffer_load_dwordx4 a[16:19], v2, s[24:27], s60 offen offset:1024 sc1\n"
        "v_cvt_pk_bf16_f32 v180, v76, v77\n"
        "v_cvt_pk_bf16_f32 v181, v78, v79\n"
        "v_mfma_f32_16x16x32_bf16 v[116:119], v[136:139], v[156:159], 0\n"
        "v_cvt_pk_bf16_f32 v178, v80, v81\n"
        "v_cvt_pk_bf16_f32 v179, v82, v83\n"
        "v_mfma_f32_16x16x32_bf16 v[64:67], v[144:147], v[160:163], 0\n"
        "v_cvt_pk_bf16_f32 v176, v84, v85\n"
        "v_cvt_pk_bf16_f32 v177, v86, v87\n"
        "v_mfma_f32_16x16x32_bf16 v[120:123], v[140:143], v[160:163], 0\n"
        "v_cvt_pk_bf16_f32 v174, v88, v89\n"
        "v_cvt_pk_bf16_f32 v175, v90, v91\n"
        "v_mfma_f32_16x16x32_bf16 v[68:71], v[148:151], v[164:167], 0\n"
        "buffer_load_dwordx4 a[20:23], v2, s[24:27], s60 offen offset:2048 sc1\n"
        "v_cvt_pk_bf16_f32 v172, v92, v93\n"
        "v_cvt_pk_bf16_f32 v173, v94, v95\n"
        "v_mfma_f32_16x16x32_bf16 v[124:127], v[144:147], v[164:167], 0\n"
        "v_cndmask_b32_e64 v60, v60, v61, s[78:79]\n"
        "v_cndmask_b32_e64 v116, v116, v117, s[78:79]\n"
        "v_mfma_f32_16x16x32_bf16 v[72:75], v[152:155], v[168:171], 0\n"
        "v_cndmask_b32_e64 v116, v116, v28, s[84:85]\n"
        "v_cndmask_b32_e64 v32, v116, v60, s[80:81]\n"
        "v_mfma_f32_16x16x32_bf16 v[128:131], v[148:151], v[168:171], 0\n"
        "v_cndmask_b32_e64 v62, v62, v63, s[78:79]\n"
        "v_cndmask_b32_e64 v118, v118, v119, s[78:79]\n"
        "v_mfma_f32_16x16x32_bf16 v[96:99], v[136:139], v[208:211], 0\n"
        "v_cndmask_b32_e64 v118, v118, v28, s[84:85]\n"
        "v_cndmask_b32_e64 v33, v118, v62, s[82:83]\n"
        "v_mfma_f32_16x16x32_bf16 v[100:103], v[140:143], v[208:211], 0\n"
        "buffer_load_dwordx4 a[24:27], v2, s[24:27], s60 offen offset:3072 sc1\n"
        "v_max_f32 v48, v32, v33\n"
        "v_cndmask_b32_e64 v64, v64, v65, s[78:79]\n"
        "v_mfma_f32_16x16x32_bf16 v[104:107], v[144:147], v[208:211], 0\n"
        "v_cndmask_b32_e64 v120, v120, v121, s[78:79]\n"
        "v_cndmask_b32_e64 v34, v120, v64, s[80:81]\n"
        "v_mfma_f32_16x16x32_bf16 v[108:111], v[148:151], v[208:211], 0\n"
        "v_cndmask_b32_e64 v66, v66, v67, s[78:79]\n"
        "v_cndmask_b32_e64 v122, v122, v123, s[78:79]\n"
        "v_mfma_f32_16x16x32_bf16 v[112:115], v[152:155], v[208:211], 0\n"
        "v_cndmask_b32_e64 v35, v122, v66, s[82:83]\n"
        "v_max_f32 v49, v34, v35\n"
        "v_cndmask_b32_e64 v68, v68, v69, s[78:79]\n"
        "v_cndmask_b32_e64 v124, v124, v125, s[78:79]\n"
        "v_cndmask_b32_e64 v36, v124, v68, s[80:81]\n"
        "v_cndmask_b32_e64 v70, v70, v71, s[78:79]\n"
        "v_cndmask_b32_e64 v126, v126, v127, s[78:79]\n"
        "buffer_load_dwordx4 a[28:31], v3, s[24:27], s60 offen sc1\n"
        "v_cndmask_b32_e64 v37, v126, v70, s[82:83]\n"
        "v_max_f32 v50, v36, v37\n"
        "v_cndmask_b32_e64 v72, v72, v73, s[78:79]\n"
        "v_cndmask_b32_e64 v128, v128, v129, s[78:79]\n"
        "v_cndmask_b32_e64 v38, v128, v72, s[80:81]\n"
        "v_cndmask_b32_e64 v74, v74, v75, s[78:79]\n"
        "v_cndmask_b32_e64 v130, v130, v131, s[78:79]\n"
        "v_cndmask_b32_e64 v39, v130, v74, s[82:83]\n"
        "v_max_f32 v51, v38, v39\n"
        "v_permlane32_swap_b32 v48, v49\n"
        "v_cvt_pk_bf16_f32 v190, v96, v97\n"
        "v_permlane32_swap_b32 v50, v51\n"
        "v_cvt_pk_bf16_f32 v191, v98, v99\n"
        "v_max_f32 v48, v48, v49\n"
        "v_cvt_pk_bf16_f32 v188, v100, v101\n"
        "v_max_f32 v50, v50, v51\n"
        "v_cvt_pk_bf16_f32 v189, v102, v103\n"
        "s_nop 0\n"
        "v_permlane16_swap_b32 v48, v50\n"
        "v_cvt_pk_bf16_f32 v186, v104, v105\n"
        "v_max_f32 v49, v48, v50\n"
        "v_cvt_pk_bf16_f32 v187, v106, v107\n"
        "v_mul_f32 v48, 0xbea52b5c, v49\n"
        "v_cvt_pk_bf16_f32 v184, v108, v109\n"
        "v_mul_f32 v50, 0xbea52b5c, v49\n"
        "v_cvt_pk_bf16_f32 v185, v110, v111\n"
        "buffer_load_dwordx4 a[32:35], v3, s[24:27], s60 offen offset:1024 sc1\n"
        "v_permlane16_swap_b32 v48, v50\n"
        "v_cvt_pk_bf16_f32 v182, v112, v113\n"
        "v_mov_b32 v49, v48\n"
        "v_cvt_pk_bf16_f32 v183, v114, v115\n"
        "v_mov_b32 v51, v50\n"
        "v_permlane32_swap_b32 v48, v49\n"
        "s_nop 0\n"
        "v_permlane32_swap_b32 v50, v51\n"
        "v_fmamk_f32 v32, v32, 0x3ea52b5c, v48\n"
        "v_fmamk_f32 v33, v33, 0x3ea52b5c, v48\n"
        "v_fmamk_f32 v34, v34, 0x3ea52b5c, v49\n"
        "v_fmamk_f32 v35, v35, 0x3ea52b5c, v49\n"
        "v_fmamk_f32 v36, v36, 0x3ea52b5c, v50\n"
        "v_fmamk_f32 v37, v37, 0x3ea52b5c, v50\n"
        "v_fmamk_f32 v38, v38, 0x3ea52b5c, v51\n"
        "v_fmamk_f32 v39, v39, 0x3ea52b5c, v51\n"
        "v_exp_f32 v32, v32\n"
        "v_exp_f32 v33, v33\n"
        "v_exp_f32 v34, v34\n"
        "v_exp_f32 v35, v35\n"
        "v_exp_f32 v36, v36\n"
        "buffer_load_dwordx4 a[36:39], v3, s[24:27], s60 offen offset:2048 sc1\n"
        "v_exp_f32 v37, v37\n"
        "v_exp_f32 v38, v38\n"
        "v_exp_f32 v39, v39\n"
        "v_add_f32 v48, v32, v33\n"
        "v_add_f32 v49, v34, v35\n"
        "v_add_f32 v50, v36, v37\n"
        "v_add_f32 v51, v38, v39\n"
        "v_permlane32_swap_b32 v48, v49\n"
        "s_nop 0\n"
        "v_permlane32_swap_b32 v50, v51\n"
        "v_add_f32 v48, v48, v49\n"
        "v_add_f32 v50, v50, v51\n"
        "s_nop 1\n"
        "v_permlane16_swap_b32 v48, v50\n"
        "v_add_f32 v49, v48, v50\n"
        "v_rcp_f32 v48, v49\n"
        "v_rcp_f32 v50, v49\n"
        "s_nop 1\n"
        "v_permlane16_swap_b32 v48, v50\n"
        "v_mov_b32 v49, v48\n"
        "v_mov_b32 v51, v50\n"
        "buffer_load_dwordx4 a[40:43], v3, s[24:27], s60 offen offset:3072 sc1\n"
        "v_permlane32_swap_b32 v48, v49\n"
        "v_permlane32_swap_b32 v50, v51\n"
        "v_mul_f32 v32, v32, v48\n"
        "v_mul_f32 v33, v33, v48\n"
        "v_mul_f32 v34, v34, v49\n"
        "v_mul_f32 v35, v35, v49\n"
        "v_mul_f32 v36, v36, v50\n"
        "v_mul_f32 v37, v37, v50\n"
        "v_mul_f32 v38, v38, v51\n"
        "v_mul_f32 v39, v39, v51\n"
        "v_cvt_pk_bf16_f32 v32, v32, v33\n"
        "v_cvt_pk_bf16_f32 v34, v34, v35\n"
        "v_cvt_pk_bf16_f32 v36, v36, v37\n"
        "v_cvt_pk_bf16_f32 v38, v38, v39\n"
        "v_perm_b32 v192, v32, v32, v212\n"
        "v_perm_b32 v193, v32, v32, v213\n"
        "v_perm_b32 v194, v32, v32, v214\n"
        "v_perm_b32 v195, v32, v32, v215\n"
        "v_perm_b32 v196, v34, v34, v212\n"
        "v_perm_b32 v197, v34, v34, v213\n"
        "v_perm_b32 v198, v34, v34, v214\n"
        "v_perm_b32 v199, v34, v34, v215\n"
        "v_mfma_f32_16x16x32_bf16 v[60:63], v[178:181], v[192:195], 0\n"
        "buffer_load_dwordx4 a[44:47], v4, s[24:27], s60 offen sc1\n"
        "v_perm_b32 v200, v36, v36, v212\n"
        "v_perm_b32 v201, v36, v36, v213\n"
        "v_mfma_f32_16x16x32_bf16 v[116:119], v[188:191], v[192:195], 0\n"
        "v_perm_b32 v202, v36, v36, v214\n"
        "v_perm_b32 v203, v36, v36, v215\n"
        "v_mfma_f32_16x16x32_bf16 v[64:67], v[176:179], v[196:199], 0\n"
        "v_perm_b32 v204, v38, v38, v212\n"
        "v_perm_b32 v205, v38, v38, v213\n"
        "v_mfma_f32_16x16x32_bf16 v[120:123], v[186:189], v[196:199], 0\n"
        "v_perm_b32 v206, v38, v38, v214\n"
        "v_perm_b32 v207, v38, v38, v215\n"
        "v_mfma_f32_16x16x32_bf16 v[68:71], v[174:177], v[200:203], 0\n"
        "v_mfma_f32_16x16x32_bf16 v[124:127], v[184:187], v[200:203], 0\n"
        "buffer_load_dwordx4 a[48:51], v4, s[24:27], s60 offen offset:1024 sc1\n"
        "v_mfma_f32_16x16x32_bf16 v[72:75], v[172:175], v[204:207], 0\n"
        "v_mfma_f32_16x16x32_bf16 v[128:131], v[182:185], v[204:207], 0\n"
        "s_lshl_b32 s66, s54, 2\n"
        "s_add_u32 s66, s66, s52\n"
        "s_lshl_b32 s67, s66, 9\n"
        "s_add_u32 s67, s67, s62\n"
        "s_add_u32 s68, s67, 32768\n"
        "s_and_b32 s69, s66, 1\n"
        "s_mul_i32 s69, s69, 120\n"
        "s_lshl_b32 s66, s66, 7\n"
        "s_sub_u32 s69, s66, s69\n"
        "s_add_u32 s69, s69, s62\n"
        "s_add_u32 s69, s69, 65536\n"
        "v_lshl_add_u32 v14, v10, 12, v11\n"
        "v_cvt_pk_bf16_f32 v136, v60, v61\n"
        "v_cvt_pk_bf16_f32 v137, v62, v63\n"
        "v_cvt_pk_bf16_f32 v138, v64, v65\n"
        "v_cvt_pk_bf16_f32 v139, v66, v67\n"
        "v_cvt_pk_bf16_f32 v140, v68, v69\n"
        "v_cvt_pk_bf16_f32 v141, v70, v71\n"
        "v_cvt_pk_bf16_f32 v142, v72, v73\n"
        "v_cvt_pk_bf16_f32 v143, v74, v75\n"
        "v_permlane32_swap_b32 v136, v138\n"
        "v_permlane32_swap_b32 v137, v139\n"
        "buffer_store_dwordx4 v[136:139], v9, s[24:27], s67 offen sc1\n"
        "v_permlane32_swap_b32 v140, v142\n"
        "v_permlane32_swap_b32 v141, v143\n"
        "buffer_store_dwordx4 v[140:143], v9, s[24:27], s68 offen sc1\n"
        "v_cvt_pk_bf16_f32 v144, v116, v117\n"
        "v_cvt_pk_bf16_f32 v145, v118, v119\n"
        "s_lshl_b64 exec, exec, 16\n"
        "v_cvt_pk_bf16_f32 v144, v120, v121\n"
        "v_cvt_pk_bf16_f32 v145, v122, v123\n"
        "s_lshl_b64 exec, exec, 16\n"
        "v_cvt_pk_bf16_f32 v144, v124, v125\n"
        "v_cvt_pk_bf16_f32 v145, v126, v127\n"
        "s_lshl_b64 exec, exec, 16\n"
        "v_cvt_pk_bf16_f32 v144, v128, v129\n"
        "v_cvt_pk_bf16_f32 v145, v130, v131\n"
        "s_mov_b64 exec, -1\n"
        "buffer_store_dwordx2 v[144:145], v14, s[24:27], s69 offen sc1\n"
        "s_waitcnt vmcnt(21)\n"
        "v_permlane32_swap_b32 v216, v218\n"
        "v_permlane32_swap_b32 v217, v219\n"
        "s_waitcnt vmcnt(20)\n"
        "v_permlane32_swap_b32 v220, v222\n"
        "v_permlane32_swap_b32 v221, v223\n"
        "s_waitcnt vmcnt(19)\n"
        "v_permlane32_swap_b32 v224, v226\n"
        "v_permlane32_swap_b32 v225, v227\n"
        "s_waitcnt vmcnt(18)\n"
        "v_permlane32_swap_b32 v228, v230\n"
        "v_permlane32_swap_b32 v229, v231\n"
        "s_waitcnt vmcnt(17)\n"
        "v_permlane32_swap_b32 v232, v234\n"
        "v_permlane32_swap_b32 v233, v235\n"
        "s_waitcnt vmcnt(16)\n"
        "v_permlane32_swap_b32 v236, v238\n"
        "v_permlane32_swap_b32 v237, v239\n"
        "buffer_load_dwordx4 a[52:55], v4, s[24:27], s60 offen offset:2048 sc1\n"
        "v_add_f32 v216, v216, v218\n"
        "v_add_f32 v217, v217, v219\n"
        "v_add_f32 v220, v220, v222\n"
        "v_add_f32 v221, v221, v223\n"
        "v_add_f32 v224, v224, v226\n"
        "v_add_f32 v225, v225, v227\n"
        "v_add_f32 v228, v228, v230\n"
        "v_add_f32 v229, v229, v231\n"
        "v_add_f32 v232, v232, v234\n"
        "v_add_f32 v233, v233, v235\n"
        "v_add_f32 v236, v236, v238\n"
        "v_add_f32 v237, v237, v239\n"
        "buffer_load_dwordx4 a[56:59], v4, s[24:27], s60 offen offset:3072 sc1\n"
        "v_permlane16_swap_b32 v216, v217\n"
        "v_permlane16_swap_b32 v220, v221\n"
        "v_permlane16_swap_b32 v224, v225\n"
        "v_permlane16_swap_b32 v228, v229\n"
        "v_permlane16_swap_b32 v232, v233\n"
        "v_permlane16_swap_b32 v236, v237\n"
        "v_add_f32 v216, v216, v217\n"
        "v_add_f32 v220, v220, v221\n"
        "v_add_f32 v224, v224, v225\n"
        "v_add_f32 v228, v228, v229\n"
        "v_add_f32 v232, v232, v233\n"
        "v_add_f32 v236, v236, v237\n"
        "buffer_load_dwordx4 a[60:63], v5, s[24:27], s60 offen sc1\n"
        "v_lshl_add_u32 v24, v10, 2, v11\n"
        "v_lshrrev_b32 v25, 1, v24\n"
        "s_mul_i32 s67, s52, 640\n"
        "s_add_u32 s67, s67, 148480\n"
        "v_add_u32 v25, s67, v25\n"
        "s_lshl_b32 s67, s52, 8\n"
        "s_add_u32 s67, s67, 151040\n"
        "v_add_u32 v24, s67, v24\n"
        "v_cvt_pk_bf16_f32 v216, v216, v216\n"
        "v_cvt_pk_bf16_f32 v220, v220, v220\n"
        "v_cvt_pk_bf16_f32 v224, v224, v224\n"
        "v_cvt_pk_bf16_f32 v228, v228, v228\n"
        "buffer_load_dwordx4 a[64:67], v5, s[24:27], s60 offen offset:1024 sc1\n"
        "v_cvt_pk_bf16_f32 v232, v232, v232\n"
        "v_cvt_pk_bf16_f32 v236, v236, v236\n"
        "v_lshlrev_b32 v216, 16, v216\n"
        "v_lshlrev_b32 v220, 16, v220\n"
        "v_lshlrev_b32 v224, 16, v224\n"
        "v_lshlrev_b32 v228, 16, v228\n"
        "v_lshlrev_b32 v232, 16, v232\n"
        "v_lshlrev_b32 v236, 16, v236\n"
        "ds_write_b32 v24, v236\n"
        "v_add_f32 v216, s93, v216\n"
        "v_add_f32 v220, s93, v220\n"
        "v_add_f32 v224, s93, v224\n"
        "buffer_load_dwordx4 a[68:71], v5, s[24:27], s60 offen offset:2048 sc1\n"
        "v_add_f32 v228, s93, v228\n"
        "v_add_f32 v232, s93, v232\n"
        "v_cvt_pk_bf16_f32 v216, v216, v216\n"
        "ds_write_b16 v25, v216\n"
        "v_cvt_pk_bf16_f32 v220, v220, v220\n"
        "ds_write_b16 v25, v220 offset:128\n"
        "v_cvt_pk_bf16_f32 v224, v224, v224\n"
        "ds_write_b16 v25, v224 offset:256\n"
        "v_cvt_pk_bf16_f32 v228, v228, v228\n"
        "ds_write_b16 v25, v228 offset:384\n"
        "v_cvt_pk_bf16_f32 v232, v232, v232\n"
        "ds_write_b16 v25, v232 offset:512\n"
        "buffer_load_dwordx4 a[72:75], v5, s[24:27], s60 offen offset:3072 sc1\n"
        "s_waitcnt lgkmcnt(0)\n"
        "s_barrier\n"
        "v_mul_u32_u24 v16, 80, v10\n"
        "v_add_u32 v15, 151040, v16\n"
        "v_lshrrev_b32 v16, 1, v16\n"
        "s_mul_i32 s67, s54, 160\n"
        "s_add_u32 s67, s67, 149760\n"
        "v_add_u32 v16, s67, v16\n"
        "ds_read_b128 v[196:199], v15\n"
        "ds_read_b128 v[200:203], v15 offset:16\n"
        "ds_read_b128 v[204:207], v15 offset:32\n"
        "ds_read_b128 v[208:211], v15 offset:48\n"
        "ds_read_b128 v[212:215], v15 offset:64\n"
        "ds_read_b64 v[24:25], v16\n"
        "s_add_u32 s86, s60, 16384\n"
        "ds_read_b64 v[26:27], v16 offset:8\n"
        "ds_read_b64 v[28:29], v16 offset:16\n"
        "ds_read_b64 v[30:31], v16 offset:24\n"
        "ds_read_b64 v[32:33], v16 offset:32\n"
        "s_waitcnt vmcnt(21)\n"
        "v_lshlrev_b32 v176, 16, v240\n"
        "v_and_b32 v177, s56, v240\n"
        "v_lshlrev_b32 v178, 16, v241\n"
        "v_and_b32 v179, s56, v241\n"
        "v_lshlrev_b32 v180, 16, v242\n"
        "v_and_b32 v181, s56, v242\n"
        "v_lshlrev_b32 v182, 16, v243\n"
        "v_and_b32 v183, s56, v243\n"
        "buffer_load_dwordx4 a[76:79], v2, s[24:27], s86 offen sc1\n"
        "s_waitcnt vmcnt(21)\n"
        "v_lshlrev_b32 v184, 16, v244\n"
        "v_and_b32 v185, s56, v244\n"
        "v_lshlrev_b32 v186, 16, v245\n"
        "v_and_b32 v187, s56, v245\n"
        "v_lshlrev_b32 v188, 16, v246\n"
        "v_and_b32 v189, s56, v246\n"
        "v_lshlrev_b32 v190, 16, v247\n"
        "v_and_b32 v191, s56, v247\n"
        "s_waitcnt vmcnt(20)\n"
        "v_lshlrev_b32 v192, 16, v248\n"
        "v_and_b32 v193, s56, v248\n"
        "v_lshlrev_b32 v194, 16, v249\n"
        "v_and_b32 v195, s56, v249\n"
        "buffer_load_dwordx4 a[80:83], v2, s[24:27], s86 offen offset:1024 sc1\n"
        "s_waitcnt lgkmcnt(4)\n"
        "v_lshlrev_b32 v216, 16, v24\n"
        "v_and_b32 v217, s56, v24\n"
        "v_lshlrev_b32 v218, 16, v25\n"
        "v_and_b32 v219, s56, v25\n"
        "s_waitcnt lgkmcnt(3)\n"
        "v_lshlrev_b32 v220, 16, v26\n"
        "v_and_b32 v221, s56, v26\n"
        "v_lshlrev_b32 v222, 16, v27\n"
        "v_and_b32 v223, s56, v27\n"
        "s_waitcnt lgkmcnt(2)\n"
        "v_lshlrev_b32 v224, 16, v28\n"
        "v_and_b32 v225, s56, v28\n"
        "v_lshlrev_b32 v226, 16, v29\n"
        "v_and_b32 v227, s56, v29\n"
        "buffer_load_dwordx4 a[84:87], v2, s[24:27], s86 offen offset:2048 sc1\n"
        "s_waitcnt lgkmcnt(1)\n"
        "v_lshlrev_b32 v228, 16, v30\n"
        "v_and_b32 v229, s56, v30\n"
        "v_lshlrev_b32 v230, 16, v31\n"
        "v_and_b32 v231, s56, v31\n"
        "s_waitcnt lgkmcnt(0)\n"
        "v_lshlrev_b32 v232, 16, v32\n"
        "v_and_b32 v233, s56, v32\n"
        "v_lshlrev_b32 v234, 16, v33\n"
        "v_and_b32 v235, s56, v33\n"
        "s_mul_i32 s67, s52, 15360\n"
        "s_add_u32 s67, s67, 81920\n"
        "v_add_u32 v12, s67, v2\n"
        "s_xor_b32 s67, s52, 3\n"
        "buffer_load_dwordx4 a[88:91], v2, s[24:27], s86 offen offset:3072 sc1\n"
        "s_mul_i32 s67, s67, 15360\n"
        "s_add_u32 s90, s67, 81920\n"
        "s_xor_b32 s67, s52, 2\n"
        "s_mul_i32 s67, s67, 15360\n"
        "s_add_u32 s91, s67, 87040\n"
        "s_xor_b32 s67, s52, 1\n"
        "s_mul_i32 s67, s67, 15360\n"
        "s_add_u32 s92, s67, 92160\n"
        "s_lshl_b32 s67, s52, 2\n"
        "s_add_u32 s67, s67, s52\n"
        "s_lshl_b32 s67, s67, 10\n"
        "s_add_u32 s69, s62, 65536\n"
        "buffer_load_dwordx4 a[92:95], v3, s[24:27], s86 offen sc1\n"
        "s_xor_b32 s68, s67, 12288\n"
        "s_add_u32 s8, s68, s69\n"
        "s_lshl2_add_u32 s4, s68, s62\n"
        "s_xor_b32 s68, s67, 8192\n"
        "s_add_u32 s9, s68, s69\n"
        "s_lshl2_add_u32 s5, s68, s62\n"
        "s_xor_b32 s68, s67, 4096\n"
        "s_add_u32 s10, s68, s69\n"
        "s_lshl2_add_u32 s6, s68, s62\n"
        "s_xor_b32 s68, s67, 0\n"
        "s_add_u32 s11, s68, s69\n"
        "s_lshl2_add_u32 s7, s68, s62\n"
        "buffer_load_dwordx4 a[96:99], v3, s[24:27], s86 offen offset:1024 sc1\n"
        "s_waitcnt vmcnt(12)\n"
        "buffer_load_dwordx4 v[56:59], v2, s[24:27], s4 offen sc1\n"
        "buffer_load_dwordx4 v[60:63], v2, s[24:27], s4 offen offset:1024 sc1\n"
        "buffer_load_dwordx4 v[64:67], v2, s[24:27], s4 offen offset:2048 sc1\n"
        "buffer_load_dwordx4 v[68:71], v2, s[24:27], s4 offen offset:3072 sc1\n"
        "buffer_load_dwordx4 v[72:75], v2, s[24:27], s8 offen sc1\n"
        "buffer_load_dwordx4 v[76:79], v2, s[24:27], s5 offen sc1\n"
        "buffer_load_dwordx4 v[80:83], v2, s[24:27], s5 offen offset:1024 sc1\n"
        "buffer_load_dwordx4 v[84:87], v2, s[24:27], s5 offen offset:2048 sc1\n"
        "s_waitcnt vmcnt(7)\n"
        "v_mfma_f32_16x16x32_bf16 v[136:139], a[192:195], v[56:59], 0\n"
        "buffer_load_dwordx4 v[88:91], v2, s[24:27], s5 offen offset:3072 sc1\n"
        "v_mfma_f32_16x16x32_bf16 v[140:143], a[196:199], v[56:59], 0\n"
        "v_mfma_f32_16x16x32_bf16 v[144:147], a[200:203], v[56:59], 0\n"
        "v_mfma_f32_16x16x32_bf16 v[148:151], a[204:207], v[56:59], 0\n"
        "v_mfma_f32_16x16x32_bf16 v[152:155], a[208:211], v[56:59], 0\n"
        "s_waitcnt vmcnt(7)\n"
        "v_mfma_f32_16x16x32_bf16 v[136:139], a[212:215], v[60:63], v[136:139]\n"
        "buffer_load_dwordx4 v[92:95], v2, s[24:27], s9 offen sc1\n"
        "v_mfma_f32_16x16x32_bf16 v[140:143], a[128:131], v[60:63], v[140:143]\n"
        "v_mfma_f32_16x16x32_bf16 v[144:147], a[132:135], v[60:63], v[144:147]\n"
        "v_mfma_f32_16x16x32_bf16 v[148:151], a[136:139], v[60:63], v[148:151]\n"
        "v_mfma_f32_16x16x32_bf16 v[152:155], a[140:143], v[60:63], v[152:155]\n"
        "s_waitcnt vmcnt(7)\n"
        "v_mfma_f32_16x16x32_bf16 v[136:139], a[144:147], v[64:67], v[136:139]\n"
        "buffer_load_dwordx4 v[96:99], v2, s[24:27], s6 offen sc1\n"
        "v_mfma_f32_16x16x32_bf16 v[140:143], a[148:151], v[64:67], v[140:143]\n"
        "v_mfma_f32_16x16x32_bf16 v[144:147], a[152:155], v[64:67], v[144:147]\n"
        "v_mfma_f32_16x16x32_bf16 v[148:151], a[156:159], v[64:67], v[148:151]\n"
        "v_mfma_f32_16x16x32_bf16 v[152:155], a[160:163], v[64:67], v[152:155]\n"
        "s_waitcnt vmcnt(7)\n"
        "v_mfma_f32_16x16x32_bf16 v[136:139], a[164:167], v[68:71], v[136:139]\n"
        "buffer_load_dwordx4 v[100:103], v2, s[24:27], s6 offen offset:1024 sc1\n"
        "v_mfma_f32_16x16x32_bf16 v[140:143], a[168:171], v[68:71], v[140:143]\n"
        "v_mfma_f32_16x16x32_bf16 v[144:147], a[172:175], v[68:71], v[144:147]\n"
        "v_mfma_f32_16x16x32_bf16 v[148:151], a[176:179], v[68:71], v[148:151]\n"
        "v_mfma_f32_16x16x32_bf16 v[152:155], a[180:183], v[68:71], v[152:155]\n"
        "s_waitcnt vmcnt(7)\n"
        "v_mfma_f32_16x16x32_bf16 v[136:139], a[184:187], v[72:75], v[136:139]\n"
        "buffer_load_dwordx4 v[104:107], v2, s[24:27], s6 offen offset:2048 sc1\n"
        "v_mfma_f32_16x16x32_bf16 v[140:143], a[188:191], v[72:75], v[140:143]\n"
        "v_mfma_f32_16x16x32_bf16 v[144:147], a[0:3], v[72:75], v[144:147]\n"
        "v_mfma_f32_16x16x32_bf16 v[148:151], a[4:7], v[72:75], v[148:151]\n"
        "v_mfma_f32_16x16x32_bf16 v[152:155], a[8:11], v[72:75], v[152:155]\n"
        "s_waitcnt vmcnt(7)\n"
        "v_mfma_f32_16x16x32_bf16 v[156:159], a[192:195], v[76:79], 0\n"
        "buffer_load_dwordx4 v[108:111], v2, s[24:27], s6 offen offset:3072 sc1\n"
        "v_mfma_f32_16x16x32_bf16 v[160:163], a[196:199], v[76:79], 0\n"
        "v_mfma_f32_16x16x32_bf16 v[164:167], a[200:203], v[76:79], 0\n"
        "v_mfma_f32_16x16x32_bf16 v[168:171], a[204:207], v[76:79], 0\n"
        "buffer_load_dwordx4 v[112:115], v2, s[24:27], s10 offen sc1\n"
        "v_cmp_u_f32 vcc, v136, v136\n"
        "v_mfma_f32_16x16x32_bf16 v[172:175], a[208:211], v[76:79], 0\n"
        "s_waitcnt vmcnt(8)\n"
        "v_mfma_f32_16x16x32_bf16 v[156:159], a[212:215], v[80:83], v[156:159]\n"
        "v_mfma_f32_16x16x32_bf16 v[160:163], a[128:131], v[80:83], v[160:163]\n"
        "s_cbranch_vccnz .Ls16\n"
        ".Lb16:\n"
        "v_mfma_f32_16x16x32_bf16 v[164:167], a[132:135], v[80:83], v[164:167]\n"
        "buffer_load_dwordx4 v[116:119], v2, s[24:27], s7 offen sc1\n"
        "v_mfma_f32_16x16x32_bf16 v[168:171], a[136:139], v[80:83], v[168:171]\n"
        "v_mfma_f32_16x16x32_bf16 v[172:175], a[140:143], v[80:83], v[172:175]\n"
        "v_add_u32 v13, s90, v2\n"
        "s_waitcnt vmcnt(8)\n"
        "v_mfma_f32_16x16x32_bf16 v[156:159], a[144:147], v[84:87], v[156:159]\n"
        "buffer_load_dwordx4 v[120:123], v2, s[24:27], s7 offen offset:1024 sc1\n"
        "v_mfma_f32_16x16x32_bf16 v[160:163], a[148:151], v[84:87], v[160:163]\n"
        "v_mfma_f32_16x16x32_bf16 v[164:167], a[152:155], v[84:87], v[164:167]\n"
        "ds_write_b128 v13, v[136:139]\n"
        "v_mfma_f32_16x16x32_bf16 v[168:171], a[156:159], v[84:87], v[168:171]\n"
        "v_mfma_f32_16x16x32_bf16 v[172:175], a[160:163], v[84:87], v[172:175]\n"
        "buffer_load_dwordx4 v[124:127], v2, s[24:27], s7 offen offset:2048 sc1\n"
        "s_waitcnt vmcnt(9)\n"
        "v_mfma_f32_16x16x32_bf16 v[156:159], a[164:167], v[88:91], v[156:159]\n"
        "ds_write_b128 v13, v[140:143] offset:1024\n"
        "v_mfma_f32_16x16x32_bf16 v[160:163], a[168:171], v[88:91], v[160:163]\n"
        "v_mfma_f32_16x16x32_bf16 v[164:167], a[172:175], v[88:91], v[164:167]\n"
        "buffer_load_dwordx4 v[128:131], v2, s[24:27], s7 offen offset:3072 sc1\n"
        "v_mfma_f32_16x16x32_bf16 v[168:171], a[176:179], v[88:91], v[168:171]\n"
        "ds_write_b128 v13, v[144:147] offset:2048\n"
        "v_mfma_f32_16x16x32_bf16 v[172:175], a[180:183], v[88:91], v[172:175]\n"
        "s_waitcnt vmcnt(9)\n"
        "v_mfma_f32_16x16x32_bf16 v[156:159], a[184:187], v[92:95], v[156:159]\n"
        "v_mfma_f32_16x16x32_bf16 v[160:163], a[188:191], v[92:95], v[160:163]\n"
        "buffer_load_dwordx4 v[132:135], v2, s[24:27], s11 offen sc1\n"
        "ds_write_b128 v13, v[148:151] offset:3072\n"
        "v_mfma_f32_16x16x32_bf16 v[164:167], a[0:3], v[92:95], v[164:167]\n"
        "v_mfma_f32_16x16x32_bf16 v[168:171], a[4:7], v[92:95], v[168:171]\n"
        "ds_write_b128 v13, v[152:155] offset:4096\n"
        "v_mfma_f32_16x16x32_bf16 v[172:175], a[8:11], v[92:95], v[172:175]\n"
        "s_waitcnt vmcnt(9)\n"
        "v_mfma_f32_16x16x32_bf16 v[136:139], a[192:195], v[96:99], 0\n"
        "buffer_load_dwordx4 a[100:103], v3, s[24:27], s86 offen offset:2048 sc1\n"
        "v_mfma_f32_16x16x32_bf16 v[140:143], a[196:199], v[96:99], 0\n"
        "v_mfma_f32_16x16x32_bf16 v[144:147], a[200:203], v[96:99], 0\n"
        "v_mfma_f32_16x16x32_bf16 v[148:151], a[204:207], v[96:99], 0\n"
        "buffer_load_dwordx4 a[104:107], v3, s[24:27], s86 offen offset:3072 sc1\n"
        "v_cmp_u_f32 vcc, v156, v156\n"
        "v_mfma_f32_16x16x32_bf16 v[152:155], a[208:211], v[96:99], 0\n"
        "s_waitcnt vmcnt(10)\n"
        "v_mfma_f32_16x16x32_bf16 v[136:139], a[212:215], v[100:103], v[136:139]\n"
        "v_mfma_f32_16x16x32_bf16 v[140:143], a[128:131], v[100:103], v[140:143]\n"
        "s_cbranch_vccnz .Ls17\n"
        ".Lb17:\n"
        "v_mfma_f32_16x16x32_bf16 v[144:147], a[132:135], v[100:103], v[144:147]\n"
        "buffer_load_dwordx4 a[108:111], v4, s[24:27], s86 offen sc1\n"
        "v_mfma_f32_16x16x32_bf16 v[148:151], a[136:139], v[100:103], v[148:151]\n"
        "v_mfma_f32_16x16x32_bf16 v[152:155], a[140:143], v[100:103], v[152:155]\n"
        "v_add_u32 v13, s91, v2\n"
        "s_waitcnt vmcnt(10)\n"
        "v_mfma_f32_16x16x32_bf16 v[136:139], a[144:147], v[104:107], v[136:139]\n"
        "buffer_load_dwordx4 a[112:115], v4, s[24:27], s86 offen offset:1024 sc1\n"
        "v_mfma_f32_16x16x32_bf16 v[140:143], a[148:151], v[104:107], v[140:143]\n"
        "v_mfma_f32_16x16x32_bf16 v[144:147], a[152:155], v[104:107], v[144:147]\n"
        "ds_write_b128 v13, v[156:159]\n"
        "v_mfma_f32_16x16x32_bf16 v[148:151], a[156:159], v[104:107], v[148:151]\n"
        "v_mfma_f32_16x16x32_bf16 v[152:155], a[160:163], v[104:107], v[152:155]\n"
        "buffer_load_dwordx4 a[116:119], v4, s[24:27], s86 offen offset:2048 sc1\n"
        "s_waitcnt vmcnt(11)\n"
        "v_mfma_f32_16x16x32_bf16 v[136:139], a[164:167], v[108:111], v[136:139]\n"
        "ds_write_b128 v13, v[160:163] offset:1024\n"
        "v_mfma_f32_16x16x32_bf16 v[140:143], a[168:171], v[108:111], v[140:143]\n"
        "v_mfma_f32_16x16x32_bf16 v[144:147], a[172:175], v[108:111], v[144:147]\n"
        "buffer_load_dwordx4 a[120:123], v4, s[24:27], s86 offen offset:3072 sc1\n"
        "v_mfma_f32_16x16x32_bf16 v[148:151], a[176:179], v[108:111], v[148:151]\n"
        "ds_write_b128 v13, v[164:167] offset:2048\n"
        "v_mfma_f32_16x16x32_bf16 v[152:155], a[180:183], v[108:111], v[152:155]\n"
        "s_waitcnt vmcnt(11)\n"
        "v_mfma_f32_16x16x32_bf16 v[136:139], a[184:187], v[112:115], v[136:139]\n"
        "v_mfma_f32_16x16x32_bf16 v[140:143], a[188:191], v[112:115], v[140:143]\n"
        "buffer_load_dwordx4 a[124:127], v5, s[24:27], s86 offen sc1\n"
        "ds_write_b128 v13, v[168:171] offset:3072\n"
        "v_mfma_f32_16x16x32_bf16 v[144:147], a[0:3], v[112:115], v[144:147]\n"
        "v_mfma_f32_16x16x32_bf16 v[148:151], a[4:7], v[112:115], v[148:151]\n"
        "ds_write_b128 v13, v[172:175] offset:4096\n"
        "v_mfma_f32_16x16x32_bf16 v[152:155], a[8:11], v[112:115], v[152:155]\n"
        "s_waitcnt vmcnt(11)\n"
        "v_mfma_f32_16x16x32_bf16 v[156:159], a[192:195], v[116:119], 0\n"
        "v_mfma_f32_16x16x32_bf16 v[160:163], a[196:199], v[116:119], 0\n"
        "v_mfma_f32_16x16x32_bf16 v[164:167], a[200:203], v[116:119], 0\n"
        "v_mfma_f32_16x16x32_bf16 v[168:171], a[204:207], v[116:119], 0\n"
        "v_cmp_u_f32 vcc, v136, v136\n"
        "s_cbranch_vccnz .Ls18\n"
        ".Lb18:\n"
        "v_add_u32 v13, s92, v2\n"
        "ds_write_b128 v13, v[136:139]\n"
        "ds_write_b128 v13, v[140:143] offset:1024\n"
        "v_mfma_f32_16x16x32_bf16 v[172:175], a[208:211], v[116:119], 0\n"
        "ds_write_b128 v13, v[144:147] offset:2048\n"
        "ds_write_b128 v13, v[148:151] offset:3072\n"
        "ds_write_b128 v13, v[152:155] offset:4096\n"
        "s_waitcnt lgkmcnt(0)\n"
        "s_barrier\n"
        "s_waitcnt vmcnt(10)\n"
        "v_mfma_f32_16x16x32_bf16 v[156:159], a[212:215], v[120:123], v[156:159]\n"
        "buffer_load_dwordx4 a[216:219], v5, s[24:27], s86 offen offset:1024 sc1\n"
        "ds_read_b128 v[56:59], v12\n"
        "ds_read_b128 v[76:79], v12 offset:5120\n"
        "ds_read_b128 v[24:27], v12 offset:10240\n"
        "v_mfma_f32_16x16x32_bf16 v[160:163], a[128:131], v[120:123], v[160:163]\n"
        "ds_read_b128 v[60:63], v12 offset:1024\n"
        "ds_read_b128 v[80:83], v12 offset:6144\n"
        "ds_read_b128 v[28:31], v12 offset:11264\n"
        "ds_read_b128 v[64:67], v12 offset:2048\n"
        "ds_read_b128 v[84:87], v12 offset:7168\n"
        "v_mfma_f32_16x16x32_bf16 v[164:167], a[132:135], v[120:123], v[164:167]\n"
        "buffer_load_dwordx4 a[220:223], v5, s[24:27], s86 offen offset:2048 sc1\n"
        "ds_read_b128 v[32:35], v12 offset:12288\n"
        "ds_read_b128 v[68:71], v12 offset:3072\n"
        "ds_read_b128 v[88:91], v12 offset:8192\n"
        "v_mfma_f32_16x16x32_bf16 v[168:171], a[136:139], v[120:123], v[168:171]\n"
        "ds_read_b128 v[36:39], v12 offset:13312\n"
        "ds_read_b128 v[72:75], v12 offset:4096\n"
        "ds_read_b128 v[92:95], v12 offset:9216\n"
        "ds_read_b128 v[40:43], v12 offset:14336\n"
        "v_mfma_f32_16x16x32_bf16 v[172:175], a[140:143], v[120:123], v[172:175]\n"
        "buffer_load_dwordx4 a[224:227], v5, s[24:27], s86 offen offset:3072 sc1\n"
        "s_waitcnt lgkmcnt(13)\n"
        "v_add_f32 v56, v56, v76\n"
        "s_waitcnt lgkmcnt(12)\n"
        "v_fmac_f32 v176, v196, v24\n"
        "v_fmac_f32 v176, v196, v56\n"
        "v_add_f32 v57, v57, v77\n"
        "s_waitcnt vmcnt(12)\n"
        "v_mfma_f32_16x16x32_bf16 v[156:159], a[144:147], v[124:127], v[156:159]\n"
        "v_fmac_f32 v177, v197, v25\n"
        "v_fmac_f32 v177, v197, v57\n"
        "v_add_f32 v58, v58, v78\n"
        "v_fmac_f32 v178, v198, v26\n"
        "v_mfma_f32_16x16x32_bf16 v[160:163], a[148:151], v[124:127], v[160:163]\n"
        "s_add_u32 s86, s60, 32768\n"
        "v_fmac_f32 v178, v198, v58\n"
        "v_add_f32 v59, v59, v79\n"
        "v_fmac_f32 v179, v199, v27\n"
        "v_mfma_f32_16x16x32_bf16 v[164:167], a[152:155], v[124:127], v[164:167]\n"
        "v_fmac_f32 v179, v199, v59\n"
        "s_waitcnt lgkmcnt(10)\n"
        "v_add_f32 v60, v60, v80\n"
        "s_waitcnt lgkmcnt(9)\n"
        "v_fmac_f32 v180, v200, v28\n"
        "v_fmac_f32 v180, v200, v60\n"
        "v_add_f32 v61, v61, v81\n"
        "v_mfma_f32_16x16x32_bf16 v[168:171], a[156:159], v[124:127], v[168:171]\n"
        "buffer_load_dwordx4 a[228:231], v2, s[24:27], s86 offen sc1\n"
        "v_fmac_f32 v181, v201, v29\n"
        "v_fmac_f32 v181, v201, v61\n"
        "v_add_f32 v62, v62, v82\n"
        "v_mfma_f32_16x16x32_bf16 v[172:175], a[160:163], v[124:127], v[172:175]\n"
        "v_fmac_f32 v182, v202, v30\n"
        "v_fmac_f32 v182, v202, v62\n"
        "v_add_f32 v63, v63, v83\n"
        "v_fmac_f32 v183, v203, v31\n"
        "s_waitcnt vmcnt(12)\n"
        "v_mfma_f32_16x16x32_bf16 v[156:159], a[164:167], v[128:131], v[156:159]\n"
        "buffer_load_dwordx4 a[232:235], v2, s[24:27], s86 offen offset:1024 sc1\n"
        "v_fmac_f32 v183, v203, v63\n"
        "s_waitcnt lgkmcnt(7)\n"
        "v_add_f32 v64, v64, v84\n"
        "s_waitcnt lgkmcnt(6)\n"
        "v_fmac_f32 v184, v204, v32\n"
        "v_fmac_f32 v184, v204, v64\n"
        "v_mfma_f32_16x16x32_bf16 v[160:163], a[168:171], v[128:131], v[160:163]\n"
        "v_add_f32 v65, v65, v85\n"
        "v_fmac_f32 v185, v205, v33\n"
        "v_fmac_f32 v185, v205, v65\n"
        "v_add_f32 v66, v66, v86\n"
        "v_mfma_f32_16x16x32_bf16 v[164:167], a[172:175], v[128:131], v[164:167]\n"
        "buffer_load_dwordx4 a[236:239], v2, s[24:27], s86 offen offset:2048 sc1\n"
        "v_fmac_f32 v186, v206, v34\n"
        "v_fmac_f32 v186, v206, v66\n"
        "v_add_f32 v67, v67, v87\n"
        "v_mfma_f32_16x16x32_bf16 v[168:171], a[176:179], v[128:131], v[168:171]\n"
        "v_fmac_f32 v187, v207, v35\n"
        "v_fmac_f32 v187, v207, v67\n"
        "s_waitcnt lgkmcnt(4)\n"
        "v_add_f32 v68, v68, v88\n"
        "s_waitcnt lgkmcnt(3)\n"
        "v_fmac_f32 v188, v208, v36\n"
        "v_fmac_f32 v188, v208, v68\n"
        "v_mfma_f32_16x16x32_bf16 v[172:175], a[180:183], v[128:131], v[172:175]\n"
        "buffer_load_dwordx4 a[240:243], v2, s[24:27], s86 offen offset:3072 sc1\n"
        "v_add_f32 v69, v69, v89\n"
        "v_fmac_f32 v189, v209, v37\n"
        "v_fmac_f32 v189, v209, v69\n"
        "s_waitcnt vmcnt(14)\n"
        "v_mfma_f32_16x16x32_bf16 v[156:159], a[184:187], v[132:135], v[156:159]\n"
        "v_add_f32 v70, v70, v90\n"
        "v_fmac_f32 v190, v210, v38\n"
        "v_fmac_f32 v190, v210, v70\n"
        "v_add_f32 v71, v71, v91\n"
        "v_mfma_f32_16x16x32_bf16 v[160:163], a[188:191], v[132:135], v[160:163]\n"
        "buffer_load_dwordx4 a[244:247], v3, s[24:27], s86 offen sc1\n"
        "v_fmac_f32 v191, v211, v39\n"
        "v_fmac_f32 v191, v211, v71\n"
        "s_waitcnt lgkmcnt(1)\n"
        "v_add_f32 v72, v72, v92\n"
        "s_waitcnt lgkmcnt(0)\n"
        "v_fmac_f32 v192, v212, v40\n"
        "v_mfma_f32_16x16x32_bf16 v[164:167], a[0:3], v[132:135], v[164:167]\n"
        "v_fmac_f32 v192, v212, v72\n"
        "v_add_f32 v73, v73, v93\n"
        "v_fmac_f32 v193, v213, v41\n"
        "v_fmac_f32 v193, v213, v73\n"
        "v_mfma_f32_16x16x32_bf16 v[168:171], a[4:7], v[132:135], v[168:171]\n"
        "buffer_load_dwordx4 a[248:251], v3, s[24:27], s86 offen offset:1024 sc1\n"
        "v_add_f32 v74, v74, v94\n"
        "v_fmac_f32 v194, v214, v42\n"
        "v_fmac_f32 v194, v214, v74\n"
        "v_mfma_f32_16x16x32_bf16 v[172:175], a[8:11], v[132:135], v[172:175]\n"
        "v_add_f32 v75, v75, v95\n"
        "v_fmac_f32 v195, v215, v43\n"
        "v_fmac_f32 v195, v215, v75\n"
        "buffer_load_dwordx4 a[252:255], v3, s[24:27], s86 offen offset:2048 sc1\n"
        "v_cmp_u_f32 vcc, v156, v156\n"
        "s_cbranch_vccnz .Ls19\n"
        ".Lb19:\n"
        "v_pk_fma_f32 v[176:177], v[196:197], v[156:157], v[176:177]\n"
        "v_pk_fma_f32 v[178:179], v[198:199], v[158:159], v[178:179]\n"
        "v_pk_fma_f32 v[180:181], v[200:201], v[160:161], v[180:181]\n"
        "v_pk_fma_f32 v[182:183], v[202:203], v[162:163], v[182:183]\n"
        "v_pk_fma_f32 v[184:185], v[204:205], v[164:165], v[184:185]\n"
        "v_pk_fma_f32 v[186:187], v[206:207], v[166:167], v[186:187]\n"
        "v_pk_fma_f32 v[188:189], v[208:209], v[168:169], v[188:189]\n"
        "v_pk_fma_f32 v[190:191], v[210:211], v[170:171], v[190:191]\n"
        "v_pk_fma_f32 v[192:193], v[212:213], v[172:173], v[192:193]\n"
        "v_pk_fma_f32 v[194:195], v[214:215], v[174:175], v[194:195]\n"
        "v_mov_b32 v66, 0\n"
        "v_mov_b32 v67, 0\n"
        "s_lshl_b32 s67, s52, 14\n"
        "s_lshl_b32 s68, s54, 11\n"
        "s_add_u32 s67, s67, s68\n"
        "s_lshr_b32 s68, s67, 2\n"
        "s_add_u32 s67, s67, s63\n"
        "s_add_u32 s68, s68, s63\n"
        "s_add_u32 s68, s68, 65536\n"
        "v_cvt_pk_bf16_f32 v244, v176, v177\n"
        "v_cvt_pk_bf16_f32 v245, v178, v179\n"
        "v_cvt_pk_bf16_f32 v246, v180, v181\n"
        "v_cvt_pk_bf16_f32 v247, v182, v183\n"
        "v_cvt_pk_bf16_f32 v248, v184, v185\n"
        "v_cvt_pk_bf16_f32 v249, v186, v187\n"
        "v_cvt_pk_bf16_f32 v250, v188, v189\n"
        "v_cvt_pk_bf16_f32 v251, v190, v191\n"
        "v_cvt_pk_bf16_f32 v252, v192, v193\n"
        "v_cvt_pk_bf16_f32 v253, v194, v195\n"
        "v_lshlrev_b32 v176, 16, v244\n"
        "v_lshlrev_b32 v178, 16, v245\n"
        "v_lshlrev_b32 v180, 16, v246\n"
        "v_lshlrev_b32 v182, 16, v247\n"
        "v_and_b32 v177, s56, v244\n"
        "v_and_b32 v179, s56, v245\n"
        "v_and_b32 v181, s56, v246\n"
        "v_and_b32 v183, s56, v247\n"
        "v_pk_mul_f32 v[176:177], v[176:177], v[216:217]\n"
        "v_pk_mul_f32 v[178:179], v[178:179], v[218:219]\n"
        "v_pk_mul_f32 v[180:181], v[180:181], v[220:221]\n"
        "v_pk_mul_f32 v[182:183], v[182:183], v[222:223]\n"
        "v_cvt_pk_bf16_f32 v56, v176, v177\n"
        "v_cvt_pk_bf16_f32 v57, v178, v179\n"
        "v_cvt_pk_bf16_f32 v58, v180, v181\n"
        "v_cvt_pk_bf16_f32 v59, v182, v183\n"
        "buffer_store_dwordx4 v[56:59], v2, s[24:27], s67 offen sc1\n"
        "v_lshlrev_b32 v184, 16, v248\n"
        "v_lshlrev_b32 v186, 16, v249\n"
        "v_lshlrev_b32 v188, 16, v250\n"
        "v_lshlrev_b32 v190, 16, v251\n"
        "v_and_b32 v185, s56, v248\n"
        "v_and_b32 v187, s56, v249\n"
        "v_and_b32 v189, s56, v250\n"
        "v_and_b32 v191, s56, v251\n"
        "v_pk_mul_f32 v[184:185], v[184:185], v[224:225]\n"
        "v_pk_mul_f32 v[186:187], v[186:187], v[226:227]\n"
        "v_pk_mul_f32 v[188:189], v[188:189], v[228:229]\n"
        "v_pk_mul_f32 v[190:191], v[190:191], v[230:231]\n"
        "v_cvt_pk_bf16_f32 v60, v184, v185\n"
        "v_cvt_pk_bf16_f32 v61, v186, v187\n"
        "v_cvt_pk_bf16_f32 v62, v188, v189\n"
        "v_cvt_pk_bf16_f32 v63, v190, v191\n"
        "buffer_store_dwordx4 v[60:63], v2, s[24:27], s67 offen offset:1024 sc1\n"
        "v_dot2c_f32_bf16 v66, s57, v244\n"
        "v_dot2c_f32_bf16 v67, v244, v244\n"
        "v_dot2c_f32_bf16 v66, s57, v245\n"
        "v_dot2c_f32_bf16 v67, v245, v245\n"
        "v_dot2c_f32_bf16 v66, s57, v246\n"
        "v_dot2c_f32_bf16 v67, v246, v246\n"
        "v_dot2c_f32_bf16 v66, s57, v247\n"
        "v_dot2c_f32_bf16 v67, v247, v247\n"
        "v_dot2c_f32_bf16 v66, s57, v248\n"
        "v_dot2c_f32_bf16 v67, v248, v248\n"
        "v_dot2c_f32_bf16 v66, s57, v249\n"
        "v_dot2c_f32_bf16 v67, v249, v249\n"
        "v_dot2c_f32_bf16 v66, s57, v250\n"
        "v_dot2c_f32_bf16 v67, v250, v250\n"
        "v_dot2c_f32_bf16 v66, s57, v251\n"
        "v_dot2c_f32_bf16 v67, v251, v251\n"
        "v_dot2c_f32_bf16 v66, s57, v252\n"
        "v_dot2c_f32_bf16 v67, v252, v252\n"
        "v_dot2c_f32_bf16 v66, s57, v253\n"
        "v_dot2c_f32_bf16 v67, v253, v253\n"
        "v_lshlrev_b32 v192, 16, v252\n"
        "v_lshlrev_b32 v194, 16, v253\n"
        "v_and_b32 v193, s56, v252\n"
        "v_and_b32 v195, s56, v253\n"
        "v_pk_mul_f32 v[192:193], v[192:193], v[232:233]\n"
        "v_pk_mul_f32 v[194:195], v[194:195], v[234:235]\n"
        "v_cvt_pk_bf16_f32 v64, v192, v193\n"
        "v_cvt_pk_bf16_f32 v65, v194, v195\n"
        "s_nop 0\n"
        "v_permlane32_swap_b32 v64, v66\n"
        "v_permlane32_swap_b32 v65, v67\n"
        "buffer_store_dwordx4 v[64:67], v9, s[24:27], s68 offen sc1\n"
        "v_bfe_u32 v17, v2, 4, 1\n"
        "v_mul_u32_u24 v17, 1280, v17\n"
        "v_mul_u32_u24 v18, 40, v10\n"
        "v_sub_u32 v17, v18, v17\n"
        "v_add_u32 v17, 149760, v17\n"
        "ds_read2_b64 v[72:75], v17 offset0:0 offset1:1\n"
        "ds_read2_b64 v[76:79], v17 offset0:2 offset1:3\n"
        "ds_read2_b64 v[80:83], v17 offset0:20 offset1:21\n"
        "ds_read2_b64 v[84:87], v17 offset0:22 offset1:23\n"
        "ds_read2_b64 v[88:91], v17 offset0:40 offset1:41\n"
        "s_waitcnt lgkmcnt(4)\n"
        "v_mfma_f32_16x16x32_bf16 v[20:23], a[12:15], v[72:75], 0\n"
        "s_lshl_b32 s70, s52, 12\n"
        "v_add_u32 v13, s70, v2\n"
        "s_lshr_b32 s72, s54, 1\n"
        "v_mfma_f32_16x16x32_bf16 v[24:27], a[16:19], v[72:75], 0\n"
        "s_lshl_b32 s67, s72, 2\n"
        "s_add_u32 s67, s67, s52\n"
        "buffer_load_dwordx4 a[128:131], v3, s[24:27], s86 offen offset:3072 sc1\n"
        "v_mfma_f32_16x16x32_bf16 v[28:31], a[20:23], v[72:75], 0\n"
        "s_lshl_b32 s67, s67, 10\n"
        "s_add_u32 s69, s63, 65536\n"
        "ds_read2_b64 v[92:95], v17 offset0:42 offset1:43\n"
        "s_waitcnt lgkmcnt(4)\n"
        "v_mfma_f32_16x16x32_bf16 v[20:23], a[24:27], v[76:79], v[20:23]\n"
        "s_xor_b32 s68, s67, 12288\n"
        "s_add_u32 s8, s68, s69\n"
        "buffer_load_dwordx4 a[132:135], v4, s[24:27], s86 offen sc1\n"
        "v_mfma_f32_16x16x32_bf16 v[24:27], a[28:31], v[76:79], v[24:27]\n"
        "s_lshl2_add_u32 s4, s68, s63\n"
        "s_xor_b32 s68, s67, 8192\n"
        "s_add_u32 s9, s68, s69\n"
        "v_mfma_f32_16x16x32_bf16 v[28:31], a[32:35], v[76:79], v[28:31]\n"
        "s_lshl2_add_u32 s5, s68, s63\n"
        "ds_read2_b64 v[72:75], v17 offset0:60 offset1:61\n"
        "s_waitcnt lgkmcnt(4)\n"
        "v_mfma_f32_16x16x32_bf16 v[20:23], a[36:39], v[80:83], v[20:23]\n"
        "buffer_load_dwordx4 a[136:139], v4, s[24:27], s86 offen offset:1024 sc1\n"
        "s_xor_b32 s68, s67, 4096\n"
        "s_add_u32 s10, s68, s69\n"
        "v_mfma_f32_16x16x32_bf16 v[24:27], a[40:43], v[80:83], v[24:27]\n"
        "s_lshl2_add_u32 s6, s68, s63\n"
        "s_xor_b32 s68, s67, 0\n"
        "buffer_load_dwordx4 a[140:143], v4, s[24:27], s86 offen offset:2048 sc1\n"
        "v_mfma_f32_16x16x32_bf16 v[28:31], a[44:47], v[80:83], v[28:31]\n"
        "s_add_u32 s11, s68, s69\n"
        "ds_read2_b64 v[76:79], v17 offset0:62 offset1:63\n"
        "s_waitcnt lgkmcnt(4)\n"
        "v_mfma_f32_16x16x32_bf16 v[20:23], a[48:51], v[84:87], v[20:23]\n"
        "s_lshl2_add_u32 s7, s68, s63\n"
        "s_xor_b32 s72, s72, s52\n"
        "s_xor_b32 s72, s72, 3\n"
        "v_mfma_f32_16x16x32_bf16 v[24:27], a[52:55], v[84:87], v[24:27]\n"
        "buffer_load_dwordx4 a[144:147], v4, s[24:27], s86 offen offset:3072 sc1\n"
        "s_lshl_b32 s72, s72, 12\n"
        "s_add_u32 s72, s72, s63\n"
        "v_mfma_f32_16x16x32_bf16 v[28:31], a[56:59], v[84:87], v[28:31]\n"
        "s_add_u32 s72, s72, 81920\n"
        "s_lshl_b32 s71, s52, 10\n"
        "ds_read2_b64 v[80:83], v17 offset0:80 offset1:81\n"
        "s_waitcnt lgkmcnt(4)\n"
        "v_mfma_f32_16x16x32_bf16 v[20:23], a[60:63], v[88:91], v[20:23]\n"
        "buffer_load_dwordx4 a[148:151], v5, s[24:27], s86 offen sc1\n"
        "s_add_u32 s67, s71, 16384\n"
        "v_lshrrev_b32 v19, 9, v2\n"
        "v_mfma_f32_16x16x32_bf16 v[24:27], a[64:67], v[88:91], v[24:27]\n"
        "v_and_b32 v14, 0x1f0, v2\n"
        "v_lshl_add_u32 v14, v19, 3, v14\n"
        "buffer_load_dwordx4 a[152:155], v5, s[24:27], s86 offen offset:1024 sc1\n"
        "v_mfma_f32_16x16x32_bf16 v[28:31], a[68:71], v[88:91], v[28:31]\n"
        "v_add_u32 v14, s67, v14\n"
        "ds_read2_b64 v[84:87], v17 offset0:82 offset1:83\n"
        "s_waitcnt lgkmcnt(4)\n"
        "v_mfma_f32_16x16x32_bf16 v[20:23], a[72:75], v[92:95], v[20:23]\n"
        "v_add_u32 v16, 40960, v2\n"
        "v_mov_b32 v69, 0xbfb8aa3b\n"
        "v_lshrrev_b32 v18, 1, v11\n"
        "v_mfma_f32_16x16x32_bf16 v[24:27], a[76:79], v[92:95], v[24:27]\n"
        "buffer_load_dwordx4 a[156:159], v5, s[24:27], s86 offen offset:2048 sc1\n"
        "v_add_u32 v18, 152064, v18\n"
        "s_lshl_b32 s67, s52, 7\n"
        "v_mfma_f32_16x16x32_bf16 v[28:31], a[80:83], v[92:95], v[28:31]\n"
        "v_add_u32 v19, s67, v18\n"
        "ds_read2_b64 v[88:91], v17 offset0:100 offset1:101\n"
        "s_waitcnt lgkmcnt(4)\n"
        "v_mfma_f32_16x16x32_bf16 v[20:23], a[84:87], v[72:75], v[20:23]\n"
        "s_mul_i32 s87, s54, 24576\n"
        "buffer_load_dwordx4 a[160:163], v5, s[24:27], s86 offen offset:3072 sc1\n"
        "s_add_u32 s87, s87, s64\n"
        "v_mfma_f32_16x16x32_bf16 v[24:27], a[88:91], v[72:75], v[24:27]\n"
        "s_lshl_b32 s67, s52, 9\n"
        "s_add_u32 s87, s87, s67\n"
        "s_add_u32 s88, s87, 4096\n"
        "v_mfma_f32_16x16x32_bf16 v[28:31], a[92:95], v[72:75], v[28:31]\n"
        "s_add_u32 s86, s60, 49152\n"
        "s_add_u32 s87, s87, s67\n"
        "ds_read2_b64 v[92:95], v17 offset0:102 offset1:103\n"
        "s_waitcnt lgkmcnt(4)\n"
        "v_mfma_f32_16x16x32_bf16 v[20:23], a[96:99], v[76:79], v[20:23]\n"
        "v_lshrrev_b32 v9, 9, v2\n"
        "v_lshl_add_u32 v9, v9, 30, v2\n"
        "s_mul_i32 s12, s52, 49152\n"
        "s_waitcnt vmcnt(28)\n"
        "v_mfma_f32_16x16x32_bf16 v[24:27], a[100:103], v[76:79], v[24:27]\n"
        "buffer_load_dwordx4 a[164:167], v2, s[24:27], s86 offen sc1\n"
        "s_add_u32 s12, s12, s64\n"
        "s_add_u32 s13, s12, 12288\n"
        "s_waitcnt vmcnt(28)\n"
        "v_mfma_f32_16x16x32_bf16 v[28:31], a[104:107], v[76:79], v[28:31]\n"
        "s_add_u32 s14, s12, 24576\n"
        "ds_read2_b64 v[72:75], v17 offset0:120 offset1:121\n"
        "s_waitcnt vmcnt(27) lgkmcnt(4)\n"
        "v_mfma_f32_16x16x32_bf16 v[20:23], a[108:111], v[80:83], v[20:23]\n"
        "s_add_u32 s15, s12, 36864\n"
        "buffer_load_dwordx4 a[168:171], v2, s[24:27], s86 offen offset:1024 sc1\n"
        "s_mov_b32 s48, s18\n"
        "s_waitcnt vmcnt(27)\n"
        "v_mfma_f32_16x16x32_bf16 v[24:27], a[112:115], v[80:83], v[24:27]\n"
        "s_and_b32 s49, s19, 0xffff\n"
        "s_mov_b32 s50, 2621440\n"
        "s_mov_b32 s51, 0x20000\n"
        "s_waitcnt vmcnt(26)\n"
        "v_mfma_f32_16x16x32_bf16 v[28:31], a[116:119], v[80:83], v[28:31]\n"
        "buffer_load_dwordx4 a[172:175], v2, s[24:27], s86 offen offset:2048 sc1\n"
        "ds_read2_b64 v[76:79], v17 offset0:122 offset1:123\n"
        "s_waitcnt vmcnt(12)\n"
        "buffer_load_dwordx4 v[136:139], v2, s[24:27], s4 offen sc1\n"
        "buffer_load_dwordx4 v[140:143], v2, s[24:27], s4 offen offset:1024 sc1\n"
        "buffer_load_dwordx4 v[144:147], v2, s[24:27], s4 offen offset:2048 sc1\n"
        "buffer_load_dwordx4 v[148:151], v2, s[24:27], s4 offen offset:3072 sc1\n"
        "buffer_load_dwordx4 v[152:155], v2, s[24:27], s8 offen sc1\n"
        "s_waitcnt lgkmcnt(4)\n"
        "v_mfma_f32_16x16x32_bf16 v[20:23], a[120:123], v[84:87], v[20:23]\n"
        "buffer_load_dwordx4 v[156:159], v2, s[24:27], s5 offen sc1\n"
        "v_mfma_f32_16x16x32_bf16 v[24:27], a[124:127], v[84:87], v[24:27]\n"
        "v_mfma_f32_16x16x32_bf16 v[28:31], a[216:219], v[84:87], v[28:31]\n"
        "ds_read2_b64 v[80:83], v17 offset0:140 offset1:141\n"
        "s_waitcnt lgkmcnt(4)\n"
        "v_mfma_f32_16x16x32_bf16 v[20:23], a[220:223], v[88:91], v[20:23]\n"
        "buffer_load_dwordx4 v[160:163], v2, s[24:27], s5 offen offset:1024 sc1\n"
        "v_mfma_f32_16x16x32_bf16 v[24:27], a[224:227], v[88:91], v[24:27]\n"
        "v_mfma_f32_16x16x32_bf16 v[28:31], a[228:231], v[88:91], v[28:31]\n"
        "ds_read2_b64 v[84:87], v17 offset0:142 offset1:143\n"
        "s_waitcnt lgkmcnt(4)\n"
        "v_mfma_f32_16x16x32_bf16 v[20:23], a[232:235], v[92:95], v[20:23]\n"
        "buffer_load_dwordx4 v[164:167], v2, s[24:27], s5 offen offset:2048 sc1\n"
        "v_mfma_f32_16x16x32_bf16 v[24:27], a[236:239], v[92:95], v[24:27]\n"
        "v_mfma_f32_16x16x32_bf16 v[28:31], a[240:243], v[92:95], v[28:31]\n"
        "ds_read2_b64 v[88:91], v17 offset0:4 offset1:24\n"
        "s_waitcnt lgkmcnt(4)\n"
        "v_mfma_f32_16x16x32_bf16 v[20:23], a[244:247], v[72:75], v[20:23]\n"
        "buffer_load_dwordx4 v[168:171], v2, s[24:27], s5 offen offset:3072 sc1\n"
        "v_mfma_f32_16x16x32_bf16 v[24:27], a[248:251], v[72:75], v[24:27]\n"
        "v_mfma_f32_16x16x32_bf16 v[28:31], a[252:255], v[72:75], v[28:31]\n"
        "ds_read2_b64 v[56:59], v17 offset0:44 offset1:64\n"
        "s_waitcnt vmcnt(20) lgkmcnt(4)\n"
        "v_mfma_f32_16x16x32_bf16 v[20:23], a[128:131], v[76:79], v[20:23]\n"
        "buffer_load_dwordx4 v[172:175], v2, s[24:27], s9 offen sc1\n"
        "s_waitcnt vmcnt(20)\n"
        "v_mfma_f32_16x16x32_bf16 v[24:27], a[132:135], v[76:79], v[24:27]\n"
        "s_waitcnt vmcnt(19)\n"
        "v_mfma_f32_16x16x32_bf16 v[28:31], a[136:139], v[76:79], v[28:31]\n"
        "ds_read2_b64 v[60:63], v17 offset0:84 offset1:104\n"
        "s_waitcnt vmcnt(18) lgkmcnt(4)\n"
        "v_mfma_f32_16x16x32_bf16 v[20:23], a[140:143], v[80:83], v[20:23]\n"
        "buffer_load_dwordx4 v[216:219], v2, s[24:27], s72 offen sc1\n"
        "s_waitcnt vmcnt(18)\n"
        "v_mfma_f32_16x16x32_bf16 v[24:27], a[144:147], v[80:83], v[24:27]\n"
        "s_waitcnt vmcnt(17)\n"
        "v_mfma_f32_16x16x32_bf16 v[28:31], a[148:151], v[80:83], v[28:31]\n"
        "ds_read2_b64 v[64:67], v17 offset0:124 offset1:144\n"
        "s_waitcnt vmcnt(16) lgkmcnt(4)\n"
        "v_mfma_f32_16x16x32_bf16 v[20:23], a[152:155], v[84:87], v[20:23]\n"
        "buffer_load_dwordx4 v[220:223], v2, s[24:27], s72 offen offset:1024 sc1\n"
        "s_waitcnt vmcnt(16)\n"
        "v_mfma_f32_16x16x32_bf16 v[24:27], a[156:159], v[84:87], v[24:27]\n"
        "s_waitcnt vmcnt(15)\n"
        "v_mfma_f32_16x16x32_bf16 v[28:31], a[160:163], v[84:87], v[28:31]\n"
        "s_waitcnt vmcnt(14) lgkmcnt(3)\n"
        "v_mfma_f32_16x16x32_bf16 v[20:23], a[164:167], v[88:91], v[20:23]\n"
        "buffer_load_dwordx4 v[224:227], v2, s[24:27], s72 offen offset:2048 sc1\n"
        "s_waitcnt vmcnt(14)\n"
        "v_mfma_f32_16x16x32_bf16 v[24:27], a[168:171], v[88:91], v[24:27]\n"
        "s_waitcnt vmcnt(13)\n"
        "v_mfma_f32_16x16x32_bf16 v[28:31], a[172:175], v[88:91], v[28:31]\n"
        "s_waitcnt vmcnt(12)\n"
        "v_max_u32 v68, v136, v137\n"
        "v_max3_u32 v68, v68, v138, v139\n"
        "s_waitcnt vmcnt(11)\n"
        "v_max3_u32 v68, v68, v140, v141\n"
        "v_max3_u32 v68, v68, v142, v143\n"
        "s_waitcnt vmcnt(10)\n"
        "v_max3_u32 v68, v68, v144, v145\n"
        "v_max3_u32 v68, v68, v146, v147\n"
        "s_waitcnt vmcnt(9)\n"
        "v_max3_u32 v68, v68, v148, v149\n"
        "v_max3_u32 v68, v68, v150, v151\n"
        "s_waitcnt vmcnt(8)\n"
        "v_max3_u32 v68, v68, v152, v153\n"
        "buffer_load_dwordx4 v[228:231], v2, s[24:27], s72 offen offset:3072 sc1\n"
        "v_max3_u32 v68, v68, v154, v155\n"
        "v_cmp_le_u32 vcc, s47, v68\n"
        "s_cbranch_vccnz .Ls20\n"
        ".Lb20:\n"
        "ds_write_b128 v13, v[136:139]\n"
        "ds_write_b128 v13, v[140:143] offset:1024\n"
        "ds_write_b128 v13, v[144:147] offset:2048\n"
        "ds_write_b128 v13, v[148:151] offset:3072\n"
        "ds_write2st64_b64 v14, v[152:153], v[154:155] offset0:0 offset1:1\n"
        "buffer_load_dwordx4 a[176:179], v2, s[24:27], s86 offen offset:3072 sc1\n"
        "s_waitcnt lgkmcnt(0)\n"
        "s_barrier\n"
        "ds_read_b128 v[72:75], v2\n"
        "ds_read_b128 v[76:79], v2 offset:1024\n"
        "ds_read_b128 v[80:83], v2 offset:2048\n"
        "ds_read_b128 v[84:87], v2 offset:3072\n"
        "ds_read_b128 v[88:91], v2 offset:4096\n"
        "s_waitcnt lgkmcnt(4)\n"
        "v_mfma_f32_16x16x32_bf16 v[96:99], a[12:15], v[72:75], 0\n"
        "buffer_load_dwordx4 a[180:183], v3, s[24:27], s86 offen sc1\n"
        "buffer_load_dwordx4 a[184:187], v3, s[24:27], s86 offen offset:1024 sc1\n"
        "v_mfma_f32_16x16x32_bf16 v[100:103], a[16:19], v[72:75], 0\n"
        "buffer_load_dwordx4 a[188:191], v3, s[24:27], s86 offen offset:2048 sc1\n"
        "v_mfma_f32_16x16x32_bf16 v[104:107], a[20:23], v[72:75], 0\n"
        "ds_read_b128 v[92:95], v2 offset:5120\n"
        "s_waitcnt lgkmcnt(4)\n"
        "v_mfma_f32_16x16x32_bf16 v[96:99], a[24:27], v[76:79], v[96:99]\n"
        "buffer_load_dwordx4 a[192:195], v3, s[24:27], s86 offen offset:3072 sc1\n"
        "v_mfma_f32_16x16x32_bf16 v[100:103], a[28:31], v[76:79], v[100:103]\n"
        "buffer_load_dwordx4 a[196:199], v4, s[24:27], s86 offen sc1\n"
        "buffer_load_dwordx4 a[200:203], v4, s[24:27], s86 offen offset:1024 sc1\n"
        "v_mfma_f32_16x16x32_bf16 v[104:107], a[32:35], v[76:79], v[104:107]\n"
        "ds_read_b128 v[72:75], v2 offset:6144\n"
        "s_waitcnt lgkmcnt(3)\n"
        "v_mfma_f32_16x16x32_bf16 v[96:99], a[36:39], v[80:83], v[96:99]\n"
        "buffer_load_dwordx4 a[204:207], v4, s[24:27], s86 offen offset:2048 sc1\n"
        "v_mfma_f32_16x16x32_bf16 v[100:103], a[40:43], v[80:83], v[100:103]\n"
        "buffer_load_dwordx4 a[208:211], v4, s[24:27], s86 offen offset:3072 sc1\n"
        "v_mfma_f32_16x16x32_bf16 v[104:107], a[44:47], v[80:83], v[104:107]\n"
        "ds_read_b128 v[76:79], v2 offset:7168\n"
        "v_mfma_f32_16x16x32_bf16 v[96:99], a[48:51], v[84:87], v[96:99]\n"
        "buffer_load_dwordx4 v[176:179], v2, s[24:27], s6 offen sc1\n"
        "buffer_load_dwordx4 v[180:183], v2, s[24:27], s6 offen offset:1024 sc1\n"
        "v_mfma_f32_16x16x32_bf16 v[100:103], a[52:55], v[84:87], v[100:103]\n"
        "s_waitcnt vmcnt(19)\n"
        "v_max_u32 v68, v156, v157\n"
        "v_max3_u32 v68, v68, v158, v159\n"
        "s_waitcnt vmcnt(18)\n"
        "v_max3_u32 v68, v68, v160, v161\n"
        "v_mfma_f32_16x16x32_bf16 v[104:107], a[56:59], v[84:87], v[104:107]\n"
        "ds_read_b128 v[80:83], v2 offset:8192\n"
        "s_waitcnt lgkmcnt(3)\n"
        "v_mfma_f32_16x16x32_bf16 v[96:99], a[60:63], v[88:91], v[96:99]\n"
        "v_max3_u32 v68, v68, v162, v163\n"
        "s_waitcnt vmcnt(17)\n"
        "v_max3_u32 v68, v68, v164, v165\n"
        "v_max3_u32 v68, v68, v166, v167\n"
        "v_mfma_f32_16x16x32_bf16 v[100:103], a[64:67], v[88:91], v[100:103]\n"
        "s_waitcnt vmcnt(16)\n"
        "v_max3_u32 v68, v68, v168, v169\n"
        "v_max3_u32 v68, v68, v170, v171\n"
        "v_mfma_f32_16x16x32_bf16 v[104:107], a[68:71], v[88:91], v[104:107]\n"
        "s_waitcnt vmcnt(15)\n"
        "v_max3_u32 v68, v68, v172, v173\n"
        "ds_read_b128 v[84:87], v2 offset:9216\n"
        "v_mfma_f32_16x16x32_bf16 v[96:99], a[72:75], v[92:95], v[96:99]\n"
        "v_max3_u32 v68, v68, v174, v175\n"
        "v_cmp_le_u32 vcc, s47, v68\n"
        "s_cbranch_vccnz .Ls21\n"
        ".Lb21:\n"
        "v_mfma_f32_16x16x32_bf16 v[100:103], a[76:79], v[92:95], v[100:103]\n"
        "s_waitcnt vmcnt(14)\n"
        "v_add_f32 v216, v216, v218\n"
        "v_add_f32 v217, v217, v219\n"
        "v_mfma_f32_16x16x32_bf16 v[104:107], a[80:83], v[92:95], v[104:107]\n"
        "ds_read_b128 v[88:91], v2 offset:10240\n"
        "s_waitcnt lgkmcnt(3)\n"
        "v_mfma_f32_16x16x32_bf16 v[96:99], a[84:87], v[72:75], v[96:99]\n"
        "s_waitcnt vmcnt(13)\n"
        "v_add_f32 v220, v220, v222\n"
        "v_add_f32 v221, v221, v223\n"
        "s_waitcnt vmcnt(12)\n"
        "v_add_f32 v224, v224, v226\n"
        "v_mfma_f32_16x16x32_bf16 v[100:103], a[88:91], v[72:75], v[100:103]\n"
        "v_add_f32 v225, v225, v227\n"
        "s_waitcnt vmcnt(11)\n"
        "v_add_f32 v228, v228, v230\n"
        "v_mfma_f32_16x16x32_bf16 v[104:107], a[92:95], v[72:75], v[104:107]\n"
        "v_add_f32 v229, v229, v231\n"
        "ds_write_b128 v13, v[156:159] offset:20480\n"
        "ds_read_b128 v[92:95], v2 offset:11264\n"
        "v_mfma_f32_16x16x32_bf16 v[96:99], a[96:99], v[76:79], v[96:99]\n"
        "v_add_f32 v216, v216, v220\n"
        "v_add_f32 v217, v217, v221\n"
        "v_mfma_f32_16x16x32_bf16 v[100:103], a[100:103], v[76:79], v[100:103]\n"
        "v_add_f32 v224, v224, v228\n"
        "v_add_f32 v225, v225, v229\n"
        "v_add_f32 v216, v216, v224\n"
        "v_mfma_f32_16x16x32_bf16 v[104:107], a[104:107], v[76:79], v[104:107]\n"
        "ds_read_b128 v[72:75], v2 offset:12288\n"
        "s_waitcnt lgkmcnt(4)\n"
        "v_mfma_f32_16x16x32_bf16 v[96:99], a[108:111], v[80:83], v[96:99]\n"
        "v_add_f32 v217, v217, v225\n"
        "v_cmp_u_f32 vcc, v216, v217\n"
        "v_mfma_f32_16x16x32_bf16 v[100:103], a[112:115], v[80:83], v[100:103]\n"
        "ds_write_b128 v13, v[160:163] offset:21504\n"
        "s_cbranch_vccnz .Ls22\n"
        ".Lb22:\n"
        "v_mov_b32 v218, v216\n"
        "v_mfma_f32_16x16x32_bf16 v[104:107], a[116:119], v[80:83], v[104:107]\n"
        "v_mov_b32 v219, v217\n"
        "ds_read_b128 v[76:79], v2 offset:13312\n"
        "v_mfma_f32_16x16x32_bf16 v[96:99], a[120:123], v[84:87], v[96:99]\n"
        "v_permlane32_swap_b32 v216, v218\n"
        "v_mfma_f32_16x16x32_bf16 v[100:103], a[124:127], v[84:87], v[100:103]\n"
        "v_permlane32_swap_b32 v217, v219\n"
        "v_add_f32 v216, v216, v218\n"
        "v_mfma_f32_16x16x32_bf16 v[104:107], a[216:219], v[84:87], v[104:107]\n"
        "ds_read_b128 v[80:83], v2 offset:14336\n"
        "s_waitcnt lgkmcnt(4)\n"
        "v_mfma_f32_16x16x32_bf16 v[96:99], a[220:223], v[88:91], v[96:99]\n"
        "v_add_f32 v217, v217, v219\n"
        "v_mov_b32 v218, v216\n"
        "v_mfma_f32_16x16x32_bf16 v[100:103], a[224:227], v[88:91], v[100:103]\n"
        "v_mov_b32 v219, v217\n"
        "ds_write_b128 v13, v[164:167] offset:22528\n"
        "v_mfma_f32_16x16x32_bf16 v[104:107], a[228:231], v[88:91], v[104:107]\n"
        "v_permlane16_swap_b32 v216, v218\n"
        "ds_read_b128 v[84:87], v2 offset:15360\n"
        "v_mfma_f32_16x16x32_bf16 v[96:99], a[232:235], v[92:95], v[96:99]\n"
        "v_permlane16_swap_b32 v217, v219\n"
        "v_add_f32 v216, v216, v218\n"
        "v_mfma_f32_16x16x32_bf16 v[100:103], a[236:239], v[92:95], v[100:103]\n"
        "v_add_f32 v217, v217, v219\n"
        "v_mul_f32 v218, 0x3acccccd, v216\n"
        "v_mfma_f32_16x16x32_bf16 v[104:107], a[240:243], v[92:95], v[104:107]\n"
        "ds_read_b128 v[88:91], v2 offset:16384\n"
        "s_waitcnt lgkmcnt(4)\n"
        "v_mfma_f32_16x16x32_bf16 v[96:99], a[244:247], v[72:75], v[96:99]\n"
        "v_mul_f32 v217, 0x3acccccd, v217\n"
        "v_fma_f32 v217, -v218, v218, v217\n"
        "ds_write_b128 v13, v[168:171] offset:23552\n"
        "v_mfma_f32_16x16x32_bf16 v[100:103], a[248:251], v[72:75], v[100:103]\n"
        "v_max_f32 v217, 0, v217\n"
        "v_add_f32 v217, 0x3727c5ac, v217\n"
        "v_mfma_f32_16x16x32_bf16 v[104:107], a[252:255], v[72:75], v[104:107]\n"
        "v_rsq_f32 v216, v217\n"
        "ds_read_b128 v[92:95], v2 offset:17408\n"
        "v_mfma_f32_16x16x32_bf16 v[96:99], a[128:131], v[76:79], v[96:99]\n"
        "v_mul_f32 v216, v69, v216\n"
        "v_mul_f32_e64 v217, -v218, v216\n"
        "v_mfma_f32_16x16x32_bf16 v[100:103], a[132:135], v[76:79], v[100:103]\n"
        "ds_write_b64 v19, v[216:217]\n"
        "ds_write2st64_b64 v14, v[172:173], v[174:175] offset0:40 offset1:41\n"
        "v_mfma_f32_16x16x32_bf16 v[104:107], a[136:139], v[76:79], v[104:107]\n"
        "ds_read_b128 v[72:75], v2 offset:18432\n"
        "s_waitcnt lgkmcnt(6)\n"
        "v_mfma_f32_16x16x32_bf16 v[96:99], a[140:143], v[80:83], v[96:99]\n"
        "buffer_load_dwordx4 v[184:187], v2, s[24:27], s6 offen offset:2048 sc1\n"
        "v_mfma_f32_16x16x32_bf16 v[100:103], a[144:147], v[80:83], v[100:103]\n"
        "buffer_load_dwordx4 v[188:191], v2, s[24:27], s6 offen offset:3072 sc1\n"
        "v_mfma_f32_16x16x32_bf16 v[104:107], a[148:151], v[80:83], v[104:107]\n"
        "ds_read_b128 v[76:79], v2 offset:19456\n"
        "s_waitcnt lgkmcnt(2)\n"
        "s_barrier\n"
        "ds_read_b64 v[120:121], v18\n"
        "ds_read_b64 v[122:123], v18 offset:128\n"
        "s_waitcnt vmcnt(12)\n"
        "v_mfma_f32_16x16x32_bf16 v[20:23], a[176:179], v[56:59], v[20:23]\n"
        "buffer_load_dwordx4 v[192:195], v2, s[24:27], s10 offen sc1\n"
        "s_waitcnt vmcnt(12)\n"
        "v_mfma_f32_16x16x32_bf16 v[24:27], a[180:183], v[56:59], v[24:27]\n"
        "s_waitcnt vmcnt(11)\n"
        "v_mfma_f32_16x16x32_bf16 v[28:31], a[184:187], v[56:59], v[28:31]\n"
        "buffer_load_dwordx4 v[196:199], v2, s[24:27], s7 offen sc1\n"
        "v_mfma_f32_16x16x32_bf16 v[96:99], a[152:155], v[84:87], v[96:99]\n"
        "v_mfma_f32_16x16x32_bf16 v[100:103], a[156:159], v[84:87], v[100:103]\n"
        "v_mfma_f32_16x16x32_bf16 v[104:107], a[160:163], v[84:87], v[104:107]\n"
        "ds_read_b128 v[80:83], v2 offset:20480\n"
        "v_mfma_f32_16x16x32_bf16 v[96:99], a[164:167], v[88:91], v[96:99]\n"
        "buffer_load_dwordx4 v[200:203], v2, s[24:27], s7 offen offset:1024 sc1\n"
        "v_mfma_f32_16x16x32_bf16 v[100:103], a[168:171], v[88:91], v[100:103]\n"
        "v_mfma_f32_16x16x32_bf16 v[104:107], a[172:175], v[88:91], v[104:107]\n"
        "ds_read_b128 v[84:87], v2 offset:21504\n"
        "s_waitcnt vmcnt(12)\n"
        "v_mfma_f32_16x16x32_bf16 v[20:23], a[188:191], v[60:63], v[20:23]\n"
        "s_waitcnt vmcnt(11)\n"
        "v_mfma_f32_16x16x32_bf16 v[24:27], a[192:195], v[60:63], v[24:27]\n"
        "buffer_load_dwordx4 v[204:207], v2, s[24:27], s7 offen offset:2048 sc1\n"
        "s_waitcnt vmcnt(11)\n"
        "v_mfma_f32_16x16x32_bf16 v[28:31], a[196:199], v[60:63], v[28:31]\n"
        "s_waitcnt vmcnt(10)\n"
        "v_mfma_f32_16x16x32_bf16 v[20:23], a[200:203], v[64:67], v[20:23]\n"
        "buffer_load_dwordx4 v[208:211], v2, s[24:27], s7 offen offset:3072 sc1\n"
        "s_waitcnt vmcnt(10)\n"
        "v_mfma_f32_16x16x32_bf16 v[24:27], a[204:207], v[64:67], v[24:27]\n"
        "s_waitcnt vmcnt(9)\n"
        "v_mfma_f32_16x16x32_bf16 v[28:31], a[208:211], v[64:67], v[28:31]\n"
        "v_mfma_f32_16x16x32_bf16 v[96:99], a[176:179], v[92:95], v[96:99]\n"
        "buffer_load_dwordx4 v[212:215], v2, s[24:27], s11 offen sc1\n"
        "v_mfma_f32_16x16x32_bf16 v[100:103], a[180:183], v[92:95], v[100:103]\n"
        "v_mfma_f32_16x16x32_bf16 v[104:107], a[184:187], v[92:95], v[104:107]\n"
        "ds_read_b128 v[88:91], v2 offset:22528\n"
        "s_waitcnt lgkmcnt(5)\n"
        "v_mfma_f32_16x16x32_bf16 v[96:99], a[188:191], v[72:75], v[96:99]\n"
        "v_mfma_f32_16x16x32_bf16 v[100:103], a[192:195], v[72:75], v[100:103]\n"
        "buffer_load_dwordx4 v[124:127], v2, s[24:27], s61 offen sc1\n"
        "v_mfma_f32_16x16x32_bf16 v[104:107], a[196:199], v[72:75], v[104:107]\n"
        "ds_read_b128 v[92:95], v2 offset:23552\n"
        "v_mfma_f32_16x16x32_bf16 v[96:99], a[200:203], v[76:79], v[96:99]\n"
        "v_mfma_f32_16x16x32_bf16 v[100:103], a[204:207], v[76:79], v[100:103]\n"
        "v_mfma_f32_16x16x32_bf16 v[104:107], a[208:211], v[76:79], v[104:107]\n"
        "ds_read_b128 v[72:75], v2 offset:24576\n"
        "s_waitcnt lgkmcnt(3)\n"
        "v_mov_b32_dpp v32, v20 quad_perm:[0,0,2,2] row_mask:0xf bank_mask:0xf\n"
        "v_mul_f32_dpp v44, v20, v69 quad_perm:[1,1,3,3] row_mask:0xf bank_mask:0xf\n"
        "v_mov_b32_dpp v33, v21 quad_perm:[0,0,2,2] row_mask:0xf bank_mask:0xf\n"
        "v_mul_f32_dpp v45, v21, v69 quad_perm:[1,1,3,3] row_mask:0xf bank_mask:0xf\n"
        "v_mov_b32_dpp v34, v22 quad_perm:[0,0,2,2] row_mask:0xf bank_mask:0xf\n"
        "v_mul_f32_dpp v46, v22, v69 quad_perm:[1,1,3,3] row_mask:0xf bank_mask:0xf\n"
        "v_mov_b32_dpp v35, v23 quad_perm:[0,0,2,2] row_mask:0xf bank_mask:0xf\n"
        "v_mul_f32_dpp v47, v23, v69 quad_perm:[1,1,3,3] row_mask:0xf bank_mask:0xf\n"
        "buffer_load_dwordx4 v[128:131], v2, s[24:27], s61 offen offset:1024 sc1\n"
        "v_mov_b32_dpp v36, v24 quad_perm:[0,0,2,2] row_mask:0xf bank_mask:0xf\n"
        "v_mul_f32_dpp v48, v24, v69 quad_perm:[1,1,3,3] row_mask:0xf bank_mask:0xf\n"
        "v_mov_b32_dpp v37, v25 quad_perm:[0,0,2,2] row_mask:0xf bank_mask:0xf\n"
        "v_mul_f32_dpp v49, v25, v69 quad_perm:[1,1,3,3] row_mask:0xf bank_mask:0xf\n"
        "v_mov_b32_dpp v38, v26 quad_perm:[0,0,2,2] row_mask:0xf bank_mask:0xf\n"
        "v_mul_f32_dpp v50, v26, v69 quad_perm:[1,1,3,3] row_mask:0xf bank_mask:0xf\n"
        "v_mov_b32_dpp v39, v27 quad_perm:[0,0,2,2] row_mask:0xf bank_mask:0xf\n"
        "v_mul_f32_dpp v51, v27, v69 quad_perm:[1,1,3,3] row_mask:0xf bank_mask:0xf\n"
        "buffer_load_dwordx4 v[132:135], v2, s[24:27], s61 offen offset:2048 sc1\n"
        "v_mov_b32_dpp v40, v28 quad_perm:[0,0,2,2] row_mask:0xf bank_mask:0xf\n"
        "v_mul_f32_dpp v52, v28, v69 quad_perm:[1,1,3,3] row_mask:0xf bank_mask:0xf\n"
        "v_mov_b32_dpp v41, v29 quad_perm:[0,0,2,2] row_mask:0xf bank_mask:0xf\n"
        "v_mul_f32_dpp v53, v29, v69 quad_perm:[1,1,3,3] row_mask:0xf bank_mask:0xf\n"
        "v_mov_b32_dpp v42, v30 quad_perm:[0,0,2,2] row_mask:0xf bank_mask:0xf\n"
        "v_mul_f32_dpp v54, v30, v69 quad_perm:[1,1,3,3] row_mask:0xf bank_mask:0xf\n"
        "v_mov_b32_dpp v43, v31 quad_perm:[0,0,2,2] row_mask:0xf bank_mask:0xf\n"
        "v_mul_f32_dpp v55, v31, v69 quad_perm:[1,1,3,3] row_mask:0xf bank_mask:0xf\n"
        "buffer_load_dwordx4 v[136:139], v2, s[24:27], s61 offen offset:3072 sc1\n"
        "v_mfma_f32_16x16x32_bf16 v[108:111], a[12:15], v[80:83], 0\n"
        "v_fma_f32 v20, v121, v32, v44\n"
        "v_fma_f32 v21, v121, v33, v45\n"
        "v_fma_f32 v22, v121, v34, v46\n"
        "v_fma_f32 v23, v121, v35, v47\n"
        "v_mfma_f32_16x16x32_bf16 v[112:115], a[16:19], v[80:83], 0\n"
        "v_fmac_f32 v20, v96, v120\n"
        "v_fmac_f32 v21, v97, v120\n"
        "v_fmac_f32 v22, v98, v120\n"
        "v_fmac_f32 v23, v99, v120\n"
        "v_mfma_f32_16x16x32_bf16 v[116:119], a[20:23], v[80:83], 0\n"
        "v_exp_f32 v96, v20\n"
        "ds_read_b128 v[76:79], v2 offset:25600\n"
        "v_mfma_f32_16x16x32_bf16 v[108:111], a[24:27], v[84:87], v[108:111]\n"
        "v_exp_f32 v97, v21\n"
        "v_exp_f32 v98, v22\n"
        "v_mfma_f32_16x16x32_bf16 v[112:115], a[28:31], v[84:87], v[112:115]\n"
        "v_exp_f32 v99, v23\n"
        "buffer_load_dwordx4 v[140:143], v3, s[24:27], s61 offen sc1\n"
        "v_fma_f32 v96, v96, v69, v69\n"
        "v_mfma_f32_16x16x32_bf16 v[116:119], a[32:35], v[84:87], v[116:119]\n"
        "ds_read_b128 v[80:83], v2 offset:26624\n"
        "s_waitcnt lgkmcnt(3)\n"
        "v_mfma_f32_16x16x32_bf16 v[108:111], a[36:39], v[88:91], v[108:111]\n"
        "v_fma_f32 v97, v97, v69, v69\n"
        "v_fma_f32 v98, v98, v69, v69\n"
        "v_fma_f32 v99, v99, v69, v69\n"
        "v_mfma_f32_16x16x32_bf16 v[112:115], a[40:43], v[88:91], v[112:115]\n"
        "s_waitcnt vmcnt(14)\n"
        "v_max_u32 v68, v176, v177\n"
        "v_max3_u32 v68, v68, v178, v179\n"
        "s_waitcnt vmcnt(13)\n"
        "v_max3_u32 v68, v68, v180, v181\n"
        "v_max3_u32 v68, v68, v182, v183\n"
        "v_mfma_f32_16x16x32_bf16 v[116:119], a[44:47], v[88:91], v[116:119]\n"
        "s_waitcnt vmcnt(12)\n"
        "v_max3_u32 v68, v68, v184, v185\n"
        "v_max3_u32 v68, v68, v186, v187\n"
        "ds_read_b128 v[84:87], v2 offset:27648\n"
        "v_mfma_f32_16x16x32_bf16 v[108:111], a[48:51], v[92:95], v[108:111]\n"
        "s_waitcnt vmcnt(11)\n"
        "v_max3_u32 v68, v68, v188, v189\n"
        "buffer_load_dwordx4 v[144:147], v3, s[24:27], s61 offen offset:1024 sc1\n"
        "v_max3_u32 v68, v68, v190, v191\n"
        "s_waitcnt vmcnt(11)\n"
        "v_max3_u32 v68, v68, v192, v193\n"
        "v_mfma_f32_16x16x32_bf16 v[112:115], a[52:55], v[92:95], v[112:115]\n"
        "v_max3_u32 v68, v68, v194, v195\n"
        "v_cmp_le_u32 vcc, s47, v68\n"
        "s_cbranch_vccnz .Ls23\n"
        ".Lb23:\n"
        "v_mfma_f32_16x16x32_bf16 v[116:119], a[56:59], v[92:95], v[116:119]\n"
        "v_rcp_f32 v96, v96\n"
        "ds_read_b128 v[88:91], v2 offset:28672\n"
        "s_waitcnt lgkmcnt(3)\n"
        "v_mfma_f32_16x16x32_bf16 v[108:111], a[60:63], v[72:75], v[108:111]\n"
        "v_rcp_f32 v97, v97\n"
        "v_mfma_f32_16x16x32_bf16 v[112:115], a[64:67], v[72:75], v[112:115]\n"
        "v_rcp_f32 v98, v98\n"
        "v_rcp_f32 v99, v99\n"
        "v_mfma_f32_16x16x32_bf16 v[116:119], a[68:71], v[72:75], v[116:119]\n"
        "ds_write_b128 v13, v[176:179] offset:40960\n"
        "v_mul_f32 v96, v96, v20\n"
        "ds_read_b128 v[92:95], v2 offset:29696\n"
        "v_mfma_f32_16x16x32_bf16 v[108:111], a[72:75], v[76:79], v[108:111]\n"
        "v_mul_f32 v97, v97, v21\n"
        "buffer_load_dwordx4 v[148:151], v3, s[24:27], s61 offen offset:2048 sc1\n"
        "v_mul_f32 v98, v98, v22\n"
        "v_mul_f32 v99, v99, v23\n"
        "v_mfma_f32_16x16x32_bf16 v[112:115], a[76:79], v[76:79], v[112:115]\n"
        "ds_write_b128 v13, v[180:183] offset:41984\n"
        "v_cvt_pk_bf16_f32 v56, v96, v97\n"
        "v_cvt_pk_bf16_f32 v57, v98, v99\n"
        "v_fma_f32 v20, v121, v36, v48\n"
        "v_mfma_f32_16x16x32_bf16 v[116:119], a[80:83], v[76:79], v[116:119]\n"
        "ds_read_b128 v[72:75], v2 offset:30720\n"
        "s_waitcnt lgkmcnt(5)\n"
        "v_mfma_f32_16x16x32_bf16 v[108:111], a[84:87], v[80:83], v[108:111]\n"
        "v_fma_f32 v21, v121, v37, v49\n"
        "ds_write_b128 v13, v[184:187] offset:43008\n"
        "v_fma_f32 v22, v121, v38, v50\n"
        "v_fma_f32 v23, v121, v39, v51\n"
        "v_mfma_f32_16x16x32_bf16 v[112:115], a[88:91], v[80:83], v[112:115]\n"
        "v_fmac_f32 v20, v100, v120\n"
        "buffer_load_dwordx4 v[152:155], v3, s[24:27], s61 offen offset:3072 sc1\n"
        "v_fmac_f32 v21, v101, v120\n"
        "v_mfma_f32_16x16x32_bf16 v[116:119], a[92:95], v[80:83], v[116:119]\n"
        "ds_write_b128 v13, v[188:191] offset:44032\n"
        "v_fmac_f32 v22, v102, v120\n"
        "ds_read_b128 v[76:79], v2 offset:31744\n"
        "v_mfma_f32_16x16x32_bf16 v[108:111], a[96:99], v[84:87], v[108:111]\n"
        "v_fmac_f32 v23, v103, v120\n"
        "v_exp_f32 v100, v20\n"
        "v_mfma_f32_16x16x32_bf16 v[112:115], a[100:103], v[84:87], v[112:115]\n"
        "v_exp_f32 v101, v21\n"
        "ds_write2st64_b64 v14, v[192:193], v[194:195] offset0:80 offset1:81\n"
        "s_waitcnt vmcnt(12)\n"
        "v_max_u32 v68, v196, v197\n"
        "v_mfma_f32_16x16x32_bf16 v[116:119], a[104:107], v[84:87], v[116:119]\n"
        "v_max3_u32 v68, v68, v198, v199\n"
        "ds_read_b128 v[80:83], v2 offset:32768\n"
        "s_waitcnt lgkmcnt(7)\n"
        "v_mfma_f32_16x16x32_bf16 v[108:111], a[108:111], v[88:91], v[108:111]\n"
        "s_waitcnt vmcnt(11)\n"
        "v_max3_u32 v68, v68, v200, v201\n"
        "v_max3_u32 v68, v68, v202, v203\n"
        "buffer_load_dwordx4 v[156:159], v4, s[24:27], s61 offen sc1\n"
        "s_waitcnt vmcnt(11)\n"
        "v_max3_u32 v68, v68, v204, v205\n"
        "v_mfma_f32_16x16x32_bf16 v[112:115], a[112:115], v[88:91], v[112:115]\n"
        "v_max3_u32 v68, v68, v206, v207\n"
        "s_waitcnt vmcnt(10)\n"
        "v_max3_u32 v68, v68, v208, v209\n"
        "v_max3_u32 v68, v68, v210, v211\n"
        "v_mfma_f32_16x16x32_bf16 v[116:119], a[116:119], v[88:91], v[116:119]\n"
        "s_waitcnt vmcnt(9)\n"
        "v_max3_u32 v68, v68, v212, v213\n"
        "v_max3_u32 v68, v68, v214, v215\n"
        "ds_read_b128 v[84:87], v2 offset:33792\n"
        "v_mfma_f32_16x16x32_bf16 v[108:111], a[120:123], v[92:95], v[108:111]\n"
        "v_cmp_le_u32 vcc, s47, v68\n"
        "s_cbranch_vccnz .Ls24\n"
        ".Lb24:\n"
        "v_exp_f32 v102, v22\n"
        "v_mfma_f32_16x16x32_bf16 v[112:115], a[124:127], v[92:95], v[112:115]\n"
        "v_exp_f32 v103, v23\n"
        "v_fma_f32 v100, v100, v69, v69\n"
        "v_fma_f32 v101, v101, v69, v69\n"
        "v_mfma_f32_16x16x32_bf16 v[116:119], a[216:219], v[92:95], v[116:119]\n"
        "ds_read_b128 v[88:91], v2 offset:34816\n"
        "s_waitcnt lgkmcnt(4)\n"
        "v_mfma_f32_16x16x32_bf16 v[108:111], a[220:223], v[72:75], v[108:111]\n"
        "buffer_load_dwordx4 v[160:163], v4, s[24:27], s61 offen offset:1024 sc1\n"
        "ds_write_b128 v13, v[196:199] offset:61440\n"
        "v_fma_f32 v102, v102, v69, v69\n"
        "v_fma_f32 v103, v103, v69, v69\n"
        "v_mfma_f32_16x16x32_bf16 v[112:115], a[224:227], v[72:75], v[112:115]\n"
        "v_rcp_f32 v100, v100\n"
        "v_rcp_f32 v101, v101\n"
        "v_mfma_f32_16x16x32_bf16 v[116:119], a[228:231], v[72:75], v[116:119]\n"
        "ds_write_b128 v13, v[200:203] offset:62464\n"
        "ds_read_b128 v[92:95], v2 offset:35840\n"
        "v_mfma_f32_16x16x32_bf16 v[108:111], a[232:235], v[76:79], v[108:111]\n"
        "v_rcp_f32 v102, v102\n"
        "v_rcp_f32 v103, v103\n"
        "v_mfma_f32_16x16x32_bf16 v[112:115], a[236:239], v[76:79], v[112:115]\n"
        "v_mul_f32 v100, v100, v20\n"
        "v_mul_f32 v101, v101, v21\n"
        "ds_write_b128 v13, v[204:207] offset:63488\n"
        "buffer_load_dwordx4 v[164:167], v4, s[24:27], s61 offen offset:2048 sc1\n"
        "v_mfma_f32_16x16x32_bf16 v[116:119], a[240:243], v[76:79], v[116:119]\n"
        "ds_read_b128 v[72:75], v2 offset:36864\n"
        "s_waitcnt lgkmcnt(6)\n"
        "v_mfma_f32_16x16x32_bf16 v[108:111], a[244:247], v[80:83], v[108:111]\n"
        "v_mul_f32 v102, v102, v22\n"
        "v_mul_f32 v103, v103, v23\n"
        "v_cvt_pk_bf16_f32 v58, v100, v101\n"
        "v_cvt_pk_bf16_f32 v59, v102, v103\n"
        "v_mfma_f32_16x16x32_bf16 v[112:115], a[248:251], v[80:83], v[112:115]\n"
        "ds_write_b128 v13, v[208:211] offset:64512\n"
        "buffer_store_dwordx4 v[56:59], v2, s[24:27], s87 offen sc1\n"
        "s_add_u32 s87, s87, 6144\n"
        "v_fma_f32 v20, v121, v40, v52\n"
        "v_mfma_f32_16x16x32_bf16 v[116:119], a[252:255], v[80:83], v[116:119]\n"
        "v_fma_f32 v21, v121, v41, v53\n"
        "ds_read_b128 v[76:79], v2 offset:37888\n"
        "v_mfma_f32_16x16x32_bf16 v[108:111], a[128:131], v[84:87], v[108:111]\n"
        "ds_write2st64_b64 v14, v[212:213], v[214:215] offset0:120 offset1:121\n"
        "v_fma_f32 v22, v121, v42, v54\n"
        "buffer_load_dwordx4 v[168:171], v4, s[24:27], s61 offen offset:3072 sc1\n"
        "v_fma_f32 v23, v121, v43, v55\n"
        "v_mfma_f32_16x16x32_bf16 v[112:115], a[132:135], v[84:87], v[112:115]\n"
        "v_fmac_f32 v20, v104, v120\n"
        "v_fmac_f32 v21, v105, v120\n"
        "v_fmac_f32 v22, v106, v120\n"
        "v_fmac_f32 v23, v107, v120\n"
        "v_mfma_f32_16x16x32_bf16 v[116:119], a[136:139], v[84:87], v[116:119]\n"
        "ds_read_b128 v[80:83], v2 offset:38912\n"
        "s_waitcnt lgkmcnt(6)\n"
        "v_mfma_f32_16x16x32_bf16 v[108:111], a[140:143], v[88:91], v[108:111]\n"
        "v_exp_f32 v104, v20\n"
        "v_exp_f32 v105, v21\n"
        "v_mfma_f32_16x16x32_bf16 v[112:115], a[144:147], v[88:91], v[112:115]\n"
        "v_exp_f32 v106, v22\n"
        "v_mfma_f32_16x16x32_bf16 v[116:119], a[148:151], v[88:91], v[116:119]\n"
        "v_exp_f32 v107, v23\n"
        "buffer_load_dwordx4 v[172:175], v5, s[24:27], s61 offen sc1\n"
        "ds_read_b128 v[84:87], v2 offset:39936\n"
        "s_waitcnt lgkmcnt(2)\n"
        "s_barrier\n"
        "v_mfma_f32_16x16x32_bf16 v[108:111], a[152:155], v[92:95], v[108:111]\n"
        "v_fma_f32 v104, v104, v69, v69\n"
        "v_fma_f32 v105, v105, v69, v69\n"
        "v_fma_f32 v106, v106, v69, v69\n"
        "v_mfma_f32_16x16x32_bf16 v[112:115], a[156:159], v[92:95], v[112:115]\n"
        "v_fma_f32 v107, v107, v69, v69\n"
        "buffer_load_dwordx4 v[176:179], v5, s[24:27], s61 offen offset:1024 sc1\n"
        "v_mfma_f32_16x16x32_bf16 v[116:119], a[160:163], v[92:95], v[116:119]\n"
        "ds_read_b128 v[88:91], v16\n"
        "v_mfma_f32_16x16x32_bf16 v[108:111], a[164:167], v[72:75], v[108:111]\n"
        "v_rcp_f32 v104, v104\n"
        "v_mfma_f32_16x16x32_bf16 v[112:115], a[168:171], v[72:75], v[112:115]\n"
        "v_rcp_f32 v105, v105\n"
        "v_mfma_f32_16x16x32_bf16 v[116:119], a[172:175], v[72:75], v[116:119]\n"
        "v_rcp_f32 v106, v106\n"
        "ds_read_b128 v[92:95], v16 offset:1024\n"
        "v_mfma_f32_16x16x32_bf16 v[108:111], a[176:179], v[76:79], v[108:111]\n"
        "v_rcp_f32 v107, v107\n"
        "buffer_load_dwordx4 v[180:183], v5, s[24:27], s61 offen offset:2048 sc1\n"
        "v_mfma_f32_16x16x32_bf16 v[112:115], a[180:183], v[76:79], v[112:115]\n"
        "v_mul_f32 v104, v104, v20\n"
        "v_mul_f32 v105, v105, v21\n"
        "v_mul_f32 v106, v106, v22\n"
        "v_mfma_f32_16x16x32_bf16 v[116:119], a[184:187], v[76:79], v[116:119]\n"
        "buffer_load_dwordx4 v[184:187], v5, s[24:27], s61 offen offset:3072 sc1\n"
        "ds_read_b128 v[72:75], v16 offset:2048\n"
        "s_waitcnt lgkmcnt(3)\n"
        "v_mfma_f32_16x16x32_bf16 v[108:111], a[188:191], v[80:83], v[108:111]\n"
        "v_mul_f32 v107, v107, v23\n"
        "v_cvt_pk_bf16_f32 v64, v104, v105\n"
        "v_mfma_f32_16x16x32_bf16 v[112:115], a[192:195], v[80:83], v[112:115]\n"
        "v_cvt_pk_bf16_f32 v65, v106, v107\n"
        "v_permlane32_swap_b32 v64, v66\n"
        "v_mfma_f32_16x16x32_bf16 v[116:119], a[196:199], v[80:83], v[116:119]\n"
        "s_add_u32 s86, s61, 16384\n"
        "ds_read_b128 v[76:79], v16 offset:3072\n"
        "v_mfma_f32_16x16x32_bf16 v[108:111], a[200:203], v[84:87], v[108:111]\n"
        "v_permlane32_swap_b32 v65, v67\n"
        "buffer_store_dwordx4 v[64:67], v9, s[24:27], s88 offen sc1\n"
        "v_mfma_f32_16x16x32_bf16 v[112:115], a[204:207], v[84:87], v[112:115]\n"
        "s_add_u32 s88, s88, 6144\n"
        "buffer_load_dwordx4 v[188:191], v2, s[24:27], s86 offen sc1\n"
        "v_mfma_f32_16x16x32_bf16 v[116:119], a[208:211], v[84:87], v[116:119]\n"
        "ds_read_b128 v[80:83], v16 offset:4096\n"
        "s_waitcnt lgkmcnt(3)\n"
        "v_mfma_f32_16x16x32_bf16 v[96:99], a[12:15], v[88:91], 0\n"
        "v_mfma_f32_16x16x32_bf16 v[100:103], a[16:19], v[88:91], 0\n"
        "v_mfma_f32_16x16x32_bf16 v[104:107], a[20:23], v[88:91], 0\n"
        "v_fma_f32 v20, v123, v32, v44\n"
        "v_fma_f32 v21, v123, v33, v45\n"
        "ds_read_b128 v[84:87], v16 offset:5120\n"
        "v_mfma_f32_16x16x32_bf16 v[96:99], a[24:27], v[92:95], v[96:99]\n"
        "v_fma_f32 v22, v123, v34, v46\n"
        "v_fma_f32 v23, v123, v35, v47\n"
        "v_fmac_f32 v20, v108, v122\n"
        "v_mfma_f32_16x16x32_bf16 v[100:103], a[28:31], v[92:95], v[100:103]\n"
        "v_fmac_f32 v21, v109, v122\n"
        "v_fmac_f32 v22, v110, v122\n"
        "buffer_load_dwordx4 v[192:195], v2, s[24:27], s86 offen offset:1024 sc1\n"
        "v_mfma_f32_16x16x32_bf16 v[104:107], a[32:35], v[92:95], v[104:107]\n"
        "ds_read_b128 v[88:91], v16 offset:6144\n"
        "s_waitcnt lgkmcnt(3)\n"
        "v_mfma_f32_16x16x32_bf16 v[96:99], a[36:39], v[72:75], v[96:99]\n"
        "v_fmac_f32 v23, v111, v122\n"
        "v_exp_f32 v108, v20\n"
        "v_mfma_f32_16x16x32_bf16 v[100:103], a[40:43], v[72:75], v[100:103]\n"
        "v_exp_f32 v109, v21\n"
        "v_mfma_f32_16x16x32_bf16 v[104:107], a[44:47], v[72:75], v[104:107]\n"
        "v_exp_f32 v110, v22\n"
        "ds_read_b128 v[92:95], v16 offset:7168\n"
        "v_mfma_f32_16x16x32_bf16 v[96:99], a[48:51], v[76:79], v[96:99]\n"
        "v_exp_f32 v111, v23\n"
        "v_fma_f32 v108, v108, v69, v69\n"
        "v_mfma_f32_16x16x32_bf16 v[100:103], a[52:55], v[76:79], v[100:103]\n"
        "buffer_load_dwordx4 v[196:199], v2, s[24:27], s86 offen offset:2048 sc1\n"
        "v_fma_f32 v109, v109, v69, v69\n"
        "v_fma_f32 v110, v110, v69, v69\n"
        "v_mfma_f32_16x16x32_bf16 v[104:107], a[56:59], v[76:79], v[104:107]\n"
        "ds_read_b128 v[72:75], v16 offset:8192\n"
        "s_waitcnt lgkmcnt(3)\n"
        "v_mfma_f32_16x16x32_bf16 v[96:99], a[60:63], v[80:83], v[96:99]\n"
        "v_fma_f32 v111, v111, v69, v69\n"
        "v_rcp_f32 v108, v108\n"
        "v_mfma_f32_16x16x32_bf16 v[100:103], a[64:67], v[80:83], v[100:103]\n"
        "v_rcp_f32 v109, v109\n"
        "v_mfma_f32_16x16x32_bf16 v[104:107], a[68:71], v[80:83], v[104:107]\n"
        "v_rcp_f32 v110, v110\n"
        "ds_read_b128 v[76:79], v16 offset:9216\n"
        "v_mfma_f32_16x16x32_bf16 v[96:99], a[72:75], v[84:87], v[96:99]\n"
        "buffer_load_dwordx4 v[200:203], v2, s[24:27], s86 offen offset:3072 sc1\n"
        "v_rcp_f32 v111, v111\n"
        "v_mfma_f32_16x16x32_bf16 v[100:103], a[76:79], v[84:87], v[100:103]\n"
        "v_mul_f32 v108, v108, v20\n"
        "v_mul_f32 v109, v109, v21\n"
        "v_mul_f32 v110, v110, v22\n"
        "v_mfma_f32_16x16x32_bf16 v[104:107], a[80:83], v[84:87], v[104:107]\n"
        "ds_read_b128 v[80:83], v16 offset:10240\n"
        "s_waitcnt lgkmcnt(3)\n"
        "v_mfma_f32_16x16x32_bf16 v[96:99], a[84:87], v[88:91], v[96:99]\n"
        "v_mul_f32 v111, v111, v23\n"
        "v_cvt_pk_bf16_f32 v60, v108, v109\n"
        "buffer_load_dwordx4 v[204:207], v3, s[24:27], s86 offen sc1\n"
        "v_mfma_f32_16x16x32_bf16 v[100:103], a[88:91], v[88:91], v[100:103]\n"
        "v_cvt_pk_bf16_f32 v61, v110, v111\n"
        "v_fma_f32 v20, v123, v36, v48\n"
        "v_mfma_f32_16x16x32_bf16 v[104:107], a[92:95], v[88:91], v[104:107]\n"
        "v_fma_f32 v21, v123, v37, v49\n"
        "v_fma_f32 v22, v123, v38, v50\n"
        "ds_read_b128 v[84:87], v16 offset:11264\n"
        "v_mfma_f32_16x16x32_bf16 v[96:99], a[96:99], v[92:95], v[96:99]\n"
        "v_fma_f32 v23, v123, v39, v51\n"
        "v_fmac_f32 v20, v112, v122\n"
        "buffer_load_dwordx4 v[208:211], v3, s[24:27], s86 offen offset:1024 sc1\n"
        "v_mfma_f32_16x16x32_bf16 v[100:103], a[100:103], v[92:95], v[100:103]\n"
        "v_fmac_f32 v21, v113, v122\n"
        "v_fmac_f32 v22, v114, v122\n"
        "v_fmac_f32 v23, v115, v122\n"
        "v_mfma_f32_16x16x32_bf16 v[104:107], a[104:107], v[92:95], v[104:107]\n"
        "ds_read_b128 v[88:91], v16 offset:12288\n"
        "s_waitcnt lgkmcnt(3)\n"
        "v_mfma_f32_16x16x32_bf16 v[96:99], a[108:111], v[72:75], v[96:99]\n"
        "v_exp_f32 v112, v20\n"
        "v_mfma_f32_16x16x32_bf16 v[100:103], a[112:115], v[72:75], v[100:103]\n"
        "v_exp_f32 v113, v21\n"
        "v_mfma_f32_16x16x32_bf16 v[104:107], a[116:119], v[72:75], v[104:107]\n"
        "v_exp_f32 v114, v22\n"
        "ds_read_b128 v[92:95], v16 offset:13312\n"
        "v_mfma_f32_16x16x32_bf16 v[96:99], a[120:123], v[76:79], v[96:99]\n"
        "buffer_load_dwordx4 v[212:215], v3, s[24:27], s86 offen offset:2048 sc1\n"
        "v_exp_f32 v115, v23\n"
        "v_mfma_f32_16x16x32_bf16 v[100:103], a[124:127], v[76:79], v[100:103]\n"
        "v_fma_f32 v112, v112, v69, v69\n"
        "v_fma_f32 v113, v113, v69, v69\n"
        "v_fma_f32 v114, v114, v69, v69\n"
        "v_mfma_f32_16x16x32_bf16 v[104:107], a[216:219], v[76:79], v[104:107]\n"
        "v_fma_f32 v115, v115, v69, v69\n"
        "ds_read_b128 v[72:75], v16 offset:14336\n"
        "s_waitcnt lgkmcnt(3)\n"
        "v_mfma_f32_16x16x32_bf16 v[96:99], a[220:223], v[80:83], v[96:99]\n"
        "v_rcp_f32 v112, v112\n"
        "v_mfma_f32_16x16x32_bf16 v[100:103], a[224:227], v[80:83], v[100:103]\n"
        "buffer_load_dwordx4 v[216:219], v3, s[24:27], s86 offen offset:3072 sc1\n"
        "v_rcp_f32 v113, v113\n"
        "v_mfma_f32_16x16x32_bf16 v[104:107], a[228:231], v[80:83], v[104:107]\n"
        "v_rcp_f32 v114, v114\n"
        "ds_read_b128 v[76:79], v16 offset:15360\n"
        "v_mfma_f32_16x16x32_bf16 v[96:99], a[232:235], v[84:87], v[96:99]\n"
        "v_rcp_f32 v115, v115\n"
        "v_mul_f32 v112, v112, v20\n"
        "v_mfma_f32_16x16x32_bf16 v[100:103], a[236:239], v[84:87], v[100:103]\n"
        "v_mul_f32 v113, v113, v21\n"
        "v_mul_f32 v114, v114, v22\n"
        "buffer_load_dwordx4 v[220:223], v4, s[24:27], s86 offen sc1\n"
        "v_mfma_f32_16x16x32_bf16 v[104:107], a[240:243], v[84:87], v[104:107]\n"
        "ds_read_b128 v[80:83], v16 offset:16384\n"
        "s_waitcnt lgkmcnt(3)\n"
        "v_mfma_f32_16x16x32_bf16 v[96:99], a[244:247], v[88:91], v[96:99]\n"
        "v_mul_f32 v115, v115, v23\n"
        "v_cvt_pk_bf16_f32 v62, v112, v113\n"
        "v_mfma_f32_16x16x32_bf16 v[100:103], a[248:251], v[88:91], v[100:103]\n"
        "v_cvt_pk_bf16_f32 v63, v114, v115\n"
        "buffer_store_dwordx4 v[60:63], v2, s[24:27], s87 offen sc1\n"
        "s_add_u32 s87, s87, 6144\n"
        "v_mfma_f32_16x16x32_bf16 v[104:107], a[252:255], v[88:91], v[104:107]\n"
        "v_fma_f32 v20, v123, v40, v52\n"
        "buffer_load_dwordx4 v[224:227], v4, s[24:27], s86 offen offset:1024 sc1\n"
        "ds_read_b128 v[84:87], v16 offset:17408\n"
        "v_mfma_f32_16x16x32_bf16 v[96:99], a[128:131], v[92:95], v[96:99]\n"
        "v_fma_f32 v21, v123, v41, v53\n"
        "v_fma_f32 v22, v123, v42, v54\n"
        "v_fma_f32 v23, v123, v43, v55\n"
        "v_mfma_f32_16x16x32_bf16 v[100:103], a[132:135], v[92:95], v[100:103]\n"
        "v_fmac_f32 v20, v116, v122\n"
        "v_fmac_f32 v21, v117, v122\n"
        "v_fmac_f32 v22, v118, v122\n"
        "v_mfma_f32_16x16x32_bf16 v[104:107], a[136:139], v[92:95], v[104:107]\n"
        "ds_read_b128 v[88:91], v16 offset:18432\n"
        "s_waitcnt lgkmcnt(3)\n"
        "v_mfma_f32_16x16x32_bf16 v[96:99], a[140:143], v[72:75], v[96:99]\n"
        "buffer_load_dwordx4 v[228:231], v4, s[24:27], s86 offen offset:2048 sc1\n"
        "v_fmac_f32 v23, v119, v122\n"
        "v_mfma_f32_16x16x32_bf16 v[100:103], a[144:147], v[72:75], v[100:103]\n"
        "v_exp_f32 v116, v20\n"
        "v_mfma_f32_16x16x32_bf16 v[104:107], a[148:151], v[72:75], v[104:107]\n"
        "v_exp_f32 v117, v21\n"
        "ds_read_b128 v[92:95], v16 offset:19456\n"
        "v_mfma_f32_16x16x32_bf16 v[96:99], a[152:155], v[76:79], v[96:99]\n"
        "v_exp_f32 v118, v22\n"
        "v_mfma_f32_16x16x32_bf16 v[100:103], a[156:159], v[76:79], v[100:103]\n"
        "v_exp_f32 v119, v23\n"
        "v_fma_f32 v116, v116, v69, v69\n"
        "v_mfma_f32_16x16x32_bf16 v[104:107], a[160:163], v[76:79], v[104:107]\n"
        "buffer_load_dwordx4 v[232:235], v4, s[24:27], s86 offen offset:3072 sc1\n"
        "ds_read_b128 v[72:75], v16 offset:20480\n"
        "s_waitcnt lgkmcnt(3)\n"
        "v_mfma_f32_16x16x32_bf16 v[96:99], a[164:167], v[80:83], v[96:99]\n"
        "v_fma_f32 v117, v117, v69, v69\n"
        "v_fma_f32 v118, v118, v69, v69\n"
        "v_fma_f32 v119, v119, v69, v69\n"
        "v_mfma_f32_16x16x32_bf16 v[100:103], a[168:171], v[80:83], v[100:103]\n"
        "v_rcp_f32 v116, v116\n"
        "v_mfma_f32_16x16x32_bf16 v[104:107], a[172:175], v[80:83], v[104:107]\n"
        "v_rcp_f32 v117, v117\n"
        "ds_read_b128 v[76:79], v16 offset:21504\n"
        "v_mfma_f32_16x16x32_bf16 v[96:99], a[176:179], v[84:87], v[96:99]\n"
        "v_rcp_f32 v118, v118\n"
        "buffer_load_dwordx4 v[236:239], v5, s[24:27], s86 offen sc1\n"
        "v_mfma_f32_16x16x32_bf16 v[100:103], a[180:183], v[84:87], v[100:103]\n"
        "v_rcp_f32 v119, v119\n"
        "v_mul_f32 v116, v116, v20\n"
        "v_mfma_f32_16x16x32_bf16 v[104:107], a[184:187], v[84:87], v[104:107]\n"
        "v_mul_f32 v117, v117, v21\n"
        "ds_read_b128 v[80:83], v16 offset:22528\n"
        "s_waitcnt lgkmcnt(3)\n"
        "v_mfma_f32_16x16x32_bf16 v[96:99], a[188:191], v[88:91], v[96:99]\n"
        "v_mul_f32 v118, v118, v22\n"
        "v_mul_f32 v119, v119, v23\n"
        "v_mfma_f32_16x16x32_bf16 v[100:103], a[192:195], v[88:91], v[100:103]\n"
        "v_cvt_pk_bf16_f32 v64, v116, v117\n"
        "buffer_load_dwordx4 v[240:243], v5, s[24:27], s86 offen offset:1024 sc1\n"
        "v_cvt_pk_bf16_f32 v65, v118, v119\n"
        "v_mfma_f32_16x16x32_bf16 v[104:107], a[196:199], v[88:91], v[104:107]\n"
        "v_permlane32_swap_b32 v64, v66\n"
        "ds_read_b128 v[84:87], v16 offset:23552\n"
        "v_mfma_f32_16x16x32_bf16 v[96:99], a[200:203], v[92:95], v[96:99]\n"
        "v_permlane32_swap_b32 v65, v67\n"
        "buffer_store_dwordx4 v[64:67], v9, s[24:27], s88 offen sc1\n"
        "v_mfma_f32_16x16x32_bf16 v[100:103], a[204:207], v[92:95], v[100:103]\n"
        "s_add_u32 s88, s88, 6144\n"
        "buffer_load_dwordx4 a[0:3], v5, s[24:27], s86 offen offset:2048 sc1\n"
        "v_mfma_f32_16x16x32_bf16 v[104:107], a[208:211], v[92:95], v[104:107]\n"
        "ds_read_b128 v[88:91], v16 offset:24576\n"
        "s_waitcnt lgkmcnt(3)\n"
        "ds_read_b64 v[120:121], v18 offset:256\n"
        "ds_read_b64 v[122:123], v18 offset:384\n"
        "v_mfma_f32_16x16x32_bf16 v[108:111], a[12:15], v[72:75], 0\n"
        "v_mfma_f32_16x16x32_bf16 v[112:115], a[16:19], v[72:75], 0\n"
        "v_mfma_f32_16x16x32_bf16 v[116:119], a[20:23], v[72:75], 0\n"
        "ds_read_b128 v[92:95], v16 offset:25600\n"
        "v_mfma_f32_16x16x32_bf16 v[108:111], a[24:27], v[76:79], v[108:111]\n"
        "s_waitcnt lgkmcnt(2)\n"
        "v_fma_f32 v20, v121, v32, v44\n"
        "v_fma_f32 v21, v121, v33, v45\n"
        "v_fma_f32 v22, v121, v34, v46\n"
        "v_mfma_f32_16x16x32_bf16 v[112:115], a[28:31], v[76:79], v[112:115]\n"
        "v_fma_f32 v23, v121, v35, v47\n"
        "v_fmac_f32 v20, v96, v120\n"
        "v_fmac_f32 v21, v97, v120\n"
        "v_mfma_f32_16x16x32_bf16 v[116:119], a[32:35], v[76:79], v[116:119]\n"
        "ds_read_b128 v[72:75], v16 offset:26624\n"
        "v_mfma_f32_16x16x32_bf16 v[108:111], a[36:39], v[80:83], v[108:111]\n"
        "buffer_load_dwordx4 a[4:7], v5, s[24:27], s86 offen offset:3072 sc1\n"
        "v_fmac_f32 v22, v98, v120\n"
        "v_fmac_f32 v23, v99, v120\n"
        "v_mfma_f32_16x16x32_bf16 v[112:115], a[40:43], v[80:83], v[112:115]\n"
        "v_exp_f32 v96, v20\n"
        "v_mfma_f32_16x16x32_bf16 v[116:119], a[44:47], v[80:83], v[116:119]\n"
        "v_exp_f32 v97, v21\n"
        "ds_read_b128 v[76:79], v16 offset:27648\n"
        "v_mfma_f32_16x16x32_bf16 v[108:111], a[48:51], v[84:87], v[108:111]\n"
        "v_exp_f32 v98, v22\n"
        "v_mfma_f32_16x16x32_bf16 v[112:115], a[52:55], v[84:87], v[112:115]\n"
        "v_exp_f32 v99, v23\n"
        "s_add_u32 s86, s61, 32768\n"
        "v_mfma_f32_16x16x32_bf16 v[116:119], a[56:59], v[84:87], v[116:119]\n"
        "ds_read_b128 v[80:83], v16 offset:28672\n"
        "s_waitcnt lgkmcnt(3)\n"
        "v_mfma_f32_16x16x32_bf16 v[108:111], a[60:63], v[88:91], v[108:111]\n"
        "v_fma_f32 v96, v96, v69, v69\n"
        "v_fma_f32 v97, v97, v69, v69\n"
        "v_fma_f32 v98, v98, v69, v69\n"
        "v_mfma_f32_16x16x32_bf16 v[112:115], a[64:67], v[88:91], v[112:115]\n"
        "v_fma_f32 v99, v99, v69, v69\n"
        "v_rcp_f32 v96, v96\n"
        "v_mfma_f32_16x16x32_bf16 v[116:119], a[68:71], v[88:91], v[116:119]\n"
        "buffer_load_dwordx4 a[8:11], v2, s[24:27], s86 offen sc1\n"
        "ds_read_b128 v[84:87], v16 offset:29696\n"
        "v_mfma_f32_16x16x32_bf16 v[108:111], a[72:75], v[92:95], v[108:111]\n"
        "v_rcp_f32 v97, v97\n"
        "v_mfma_f32_16x16x32_bf16 v[112:115], a[76:79], v[92:95], v[112:115]\n"
        "v_rcp_f32 v98, v98\n"
        "v_mfma_f32_16x16x32_bf16 v[116:119], a[80:83], v[92:95], v[116:119]\n"
        "ds_read_b128 v[88:91], v16 offset:30720\n"
        "s_waitcnt lgkmcnt(3)\n"
        "v_mfma_f32_16x16x32_bf16 v[108:111], a[84:87], v[72:75], v[108:111]\n"
        "v_rcp_f32 v99, v99\n"
        "v_mul_f32 v96, v96, v20\n"
        "v_mfma_f32_16x16x32_bf16 v[112:115], a[88:91], v[72:75], v[112:115]\n"
        "v_mul_f32 v97, v97, v21\n"
        "v_mul_f32 v98, v98, v22\n"
        "buffer_load_dwordx4 v[24:27], v2, s[24:27], s12 offen sc1\n"
        "v_mfma_f32_16x16x32_bf16 v[116:119], a[92:95], v[72:75], v[116:119]\n"
        "v_mul_f32 v99, v99, v23\n"
        "v_cvt_pk_bf16_f32 v56, v96, v97\n"
        "ds_read_b128 v[92:95], v16 offset:31744\n"
        "v_mfma_f32_16x16x32_bf16 v[108:111], a[96:99], v[76:79], v[108:111]\n"
        "v_cvt_pk_bf16_f32 v57, v98, v99\n"
        "v_fma_f32 v20, v121, v36, v48\n"
        "v_fma_f32 v21, v121, v37, v49\n"
        "v_mfma_f32_16x16x32_bf16 v[112:115], a[100:103], v[76:79], v[112:115]\n"
        "buffer_load_dwordx4 v[28:31], v2, s[24:27], s12 offen offset:1024 sc1\n"
        "v_fma_f32 v22, v121, v38, v50\n"
        "v_fma_f32 v23, v121, v39, v51\n"
        "v_mfma_f32_16x16x32_bf16 v[116:119], a[104:107], v[76:79], v[116:119]\n"
        "ds_read_b128 v[72:75], v16 offset:32768\n"
        "s_waitcnt lgkmcnt(3)\n"
        "v_mfma_f32_16x16x32_bf16 v[108:111], a[108:111], v[80:83], v[108:111]\n"
        "v_fmac_f32 v20, v100, v120\n"
        "v_fmac_f32 v21, v101, v120\n"
        "v_fmac_f32 v22, v102, v120\n"
        "v_mfma_f32_16x16x32_bf16 v[112:115], a[112:115], v[80:83], v[112:115]\n"
        "v_fmac_f32 v23, v103, v120\n"
        "buffer_load_dwordx4 a[212:215], v2, s[24:27], s12 offen offset:2048 sc1\n"
        "v_mfma_f32_16x16x32_bf16 v[116:119], a[116:119], v[80:83], v[116:119]\n"
        "v_exp_f32 v100, v20\n"
        "ds_read_b128 v[76:79], v16 offset:33792\n"
        "v_mfma_f32_16x16x32_bf16 v[108:111], a[120:123], v[84:87], v[108:111]\n"
        "v_exp_f32 v101, v21\n"
        "v_mfma_f32_16x16x32_bf16 v[112:115], a[124:127], v[84:87], v[112:115]\n"
        "v_exp_f32 v102, v22\n"
        "v_mfma_f32_16x16x32_bf16 v[116:119], a[216:219], v[84:87], v[116:119]\n"
        "ds_read_b128 v[80:83], v16 offset:34816\n"
        "s_waitcnt lgkmcnt(3)\n"
        "v_mfma_f32_16x16x32_bf16 v[108:111], a[220:223], v[88:91], v[108:111]\n"
        "v_exp_f32 v103, v23\n"
        "v_fma_f32 v100, v100, v69, v69\n"
        "v_mfma_f32_16x16x32_bf16 v[112:115], a[224:227], v[88:91], v[112:115]\n"
        "v_fma_f32 v101, v101, v69, v69\n"
        "buffer_load_dwordx4 a[12:15], v2, s[24:27], s12 offen offset:3072 sc1\n"
        "v_fma_f32 v102, v102, v69, v69\n"
        "v_mfma_f32_16x16x32_bf16 v[116:119], a[228:231], v[88:91], v[116:119]\n"
        "v_fma_f32 v103, v103, v69, v69\n"
        "ds_read_b128 v[84:87], v16 offset:35840\n"
        "v_mfma_f32_16x16x32_bf16 v[108:111], a[232:235], v[92:95], v[108:111]\n"
        "v_rcp_f32 v100, v100\n"
        "v_mfma_f32_16x16x32_bf16 v[112:115], a[236:239], v[92:95], v[112:115]\n"
        "v_rcp_f32 v101, v101\n"
        "v_mfma_f32_16x16x32_bf16 v[116:119], a[240:243], v[92:95], v[116:119]\n"
        "ds_read_b128 v[88:91], v16 offset:36864\n"
        "s_waitcnt lgkmcnt(3)\n"
        "v_mfma_f32_16x16x32_bf16 v[108:111], a[244:247], v[72:75], v[108:111]\n"
        "v_rcp_f32 v102, v102\n"
        "buffer_load_dwordx4 a[16:19], v3, s[24:27], s12 offen sc1\n"
        "v_mfma_f32_16x16x32_bf16 v[112:115], a[248:251], v[72:75], v[112:115]\n"
        "v_rcp_f32 v103, v103\n"
        "v_mul_f32 v100, v100, v20\n"
        "v_mfma_f32_16x16x32_bf16 v[116:119], a[252:255], v[72:75], v[116:119]\n"
        "v_mul_f32 v101, v101, v21\n"
        "v_mul_f32 v102, v102, v22\n"
        "ds_read_b128 v[92:95], v16 offset:37888\n"
        "v_mfma_f32_16x16x32_bf16 v[108:111], a[128:131], v[76:79], v[108:111]\n"
        "v_mul_f32 v103, v103, v23\n"
        "v_cvt_pk_bf16_f32 v58, v100, v101\n"
        "buffer_load_dwordx4 a[20:23], v3, s[24:27], s12 offen offset:1024 sc1\n"
        "v_mfma_f32_16x16x32_bf16 v[112:115], a[132:135], v[76:79], v[112:115]\n"
        "v_cvt_pk_bf16_f32 v59, v102, v103\n"
        "buffer_store_dwordx4 v[56:59], v2, s[24:27], s87 offen sc1\n"
        "s_add_u32 s87, s87, 6144\n"
        "v_mfma_f32_16x16x32_bf16 v[116:119], a[136:139], v[76:79], v[116:119]\n"
        "v_fma_f32 v20, v121, v40, v52\n"
        "ds_read_b128 v[72:75], v16 offset:38912\n"
        "s_waitcnt lgkmcnt(3)\n"
        "v_mfma_f32_16x16x32_bf16 v[108:111], a[140:143], v[80:83], v[108:111]\n"
        "v_fma_f32 v21, v121, v41, v53\n"
        "buffer_load_dwordx4 a[24:27], v3, s[24:27], s12 offen offset:2048 sc1\n"
        "v_fma_f32 v22, v121, v42, v54\n"
        "v_mfma_f32_16x16x32_bf16 v[112:115], a[144:147], v[80:83], v[112:115]\n"
        "v_fma_f32 v23, v121, v43, v55\n"
        "v_fmac_f32 v20, v104, v120\n"
        "v_fmac_f32 v21, v105, v120\n"
        "v_mfma_f32_16x16x32_bf16 v[116:119], a[148:151], v[80:83], v[116:119]\n"
        "v_fmac_f32 v22, v106, v120\n"
        "ds_read_b128 v[76:79], v16 offset:39936\n"
        "v_mfma_f32_16x16x32_bf16 v[108:111], a[152:155], v[84:87], v[108:111]\n"
        "v_fmac_f32 v23, v107, v120\n"
        "buffer_load_dwordx4 a[28:31], v3, s[24:27], s12 offen offset:3072 sc1\n"
        "v_mfma_f32_16x16x32_bf16 v[112:115], a[156:159], v[84:87], v[112:115]\n"
        "v_exp_f32 v104, v20\n"
        "v_mfma_f32_16x16x32_bf16 v[116:119], a[160:163], v[84:87], v[116:119]\n"
        "v_exp_f32 v105, v21\n"
        "s_waitcnt lgkmcnt(2)\n"
        "v_mfma_f32_16x16x32_bf16 v[108:111], a[164:167], v[88:91], v[108:111]\n"
        "v_exp_f32 v106, v22\n"
        "v_mfma_f32_16x16x32_bf16 v[112:115], a[168:171], v[88:91], v[112:115]\n"
        "v_exp_f32 v107, v23\n"
        "v_fma_f32 v104, v104, v69, v69\n"
        "v_mfma_f32_16x16x32_bf16 v[116:119], a[172:175], v[88:91], v[116:119]\n"
        "v_fma_f32 v105, v105, v69, v69\n"
        "buffer_load_dwordx4 a[32:35], v4, s[24:27], s12 offen sc1\n"
        "v_fma_f32 v106, v106, v69, v69\n"
        "v_mfma_f32_16x16x32_bf16 v[108:111], a[176:179], v[92:95], v[108:111]\n"
        "v_fma_f32 v107, v107, v69, v69\n"
        "v_rcp_f32 v104, v104\n"
        "v_mfma_f32_16x16x32_bf16 v[112:115], a[180:183], v[92:95], v[112:115]\n"
        "v_rcp_f32 v105, v105\n"
        "v_mfma_f32_16x16x32_bf16 v[116:119], a[184:187], v[92:95], v[116:119]\n"
        "v_rcp_f32 v106, v106\n"
        "s_waitcnt lgkmcnt(0)\n"
        "v_mfma_f32_16x16x32_bf16 v[108:111], a[188:191], v[72:75], v[108:111]\n"
        "buffer_load_dwordx4 a[36:39], v2, s[24:27], s14 offen sc1\n"
        "v_rcp_f32 v107, v107\n"
        "v_mfma_f32_16x16x32_bf16 v[112:115], a[192:195], v[72:75], v[112:115]\n"
        "v_mul_f32 v104, v104, v20\n"
        "v_mul_f32 v105, v105, v21\n"
        "v_mul_f32 v106, v106, v22\n"
        "v_mfma_f32_16x16x32_bf16 v[116:119], a[196:199], v[72:75], v[116:119]\n"
        "v_mul_f32 v107, v107, v23\n"
        "v_cvt_pk_bf16_f32 v64, v104, v105\n"
        "buffer_load_dwordx4 a[40:43], v2, s[24:27], s86 offen offset:1024 sc1\n"
        "v_mfma_f32_16x16x32_bf16 v[108:111], a[200:203], v[76:79], v[108:111]\n"
        "v_cvt_pk_bf16_f32 v65, v106, v107\n"
        "v_permlane32_swap_b32 v64, v66\n"
        "v_mfma_f32_16x16x32_bf16 v[112:115], a[204:207], v[76:79], v[112:115]\n"
        "v_permlane32_swap_b32 v65, v67\n"
        "buffer_store_dwordx4 v[64:67], v9, s[24:27], s88 offen sc1\n"
        "v_mfma_f32_16x16x32_bf16 v[116:119], a[208:211], v[76:79], v[116:119]\n"
        "s_add_u32 s88, s88, 6144\n"
        "buffer_load_dwordx4 a[44:47], v2, s[24:27], s86 offen offset:2048 sc1\n"
        "v_fma_f32 v20, v123, v32, v44\n"
        "v_fma_f32 v21, v123, v33, v45\n"
        "v_fma_f32 v22, v123, v34, v46\n"
        "v_fma_f32 v23, v123, v35, v47\n"
        "v_fmac_f32 v20, v108, v122\n"
        "v_fmac_f32 v21, v109, v122\n"
        "v_fmac_f32 v22, v110, v122\n"
        "v_fmac_f32 v23, v111, v122\n"
        "v_exp_f32 v108, v20\n"
        "v_exp_f32 v109, v21\n"
        "v_exp_f32 v110, v22\n"
        "v_exp_f32 v111, v23\n"
        "v_fma_f32 v108, v108, v69, v69\n"
        "v_fma_f32 v109, v109, v69, v69\n"
        "v_fma_f32 v110, v110, v69, v69\n"
        "v_fma_f32 v111, v111, v69, v69\n"
        "v_rcp_f32 v108, v108\n"
        "v_rcp_f32 v109, v109\n"
        "v_rcp_f32 v110, v110\n"
        "v_rcp_f32 v111, v111\n"
        "buffer_load_dwordx4 a[48:51], v4, s[24:27], s12 offen offset:1024 sc1\n"
        "s_waitcnt vmcnt(14)\n"
        "v_mfma_f32_16x16x32_bf16 v[84:87], v[124:127], v[24:27], 0\n"
        "v_mul_f32 v108, v108, v20\n"
        "v_mul_f32 v109, v109, v21\n"
        "v_mul_f32 v110, v110, v22\n"
        "v_mfma_f32_16x16x32_bf16 v[88:91], v[128:131], v[24:27], 0\n"
        "v_mul_f32 v111, v111, v23\n"
        "v_cvt_pk_bf16_f32 v60, v108, v109\n"
        "v_cvt_pk_bf16_f32 v61, v110, v111\n"
        "v_mfma_f32_16x16x32_bf16 v[92:95], v[132:135], v[24:27], 0\n"
        "v_fma_f32 v20, v123, v36, v48\n"
        "v_fma_f32 v21, v123, v37, v49\n"
        "v_fma_f32 v22, v123, v38, v50\n"
        "v_mfma_f32_16x16x32_bf16 v[96:99], v[136:139], v[24:27], 0\n"
        "v_fma_f32 v23, v123, v39, v51\n"
        "v_fmac_f32 v20, v112, v122\n"
        "v_fmac_f32 v21, v113, v122\n"
        "v_mfma_f32_16x16x32_bf16 v[100:103], v[140:143], v[24:27], 0\n"
        "v_fmac_f32 v22, v114, v122\n"
        "v_fmac_f32 v23, v115, v122\n"
        "buffer_load_dwordx4 a[52:55], v2, s[24:27], s14 offen offset:1024 sc1\n"
        "s_waitcnt vmcnt(14)\n"
        "v_mfma_f32_16x16x32_bf16 v[84:87], v[144:147], v[28:31], v[84:87]\n"
        "v_exp_f32 v112, v20\n"
        "v_mfma_f32_16x16x32_bf16 v[88:91], v[148:151], v[28:31], v[88:91]\n"
        "v_exp_f32 v113, v21\n"
        "v_mfma_f32_16x16x32_bf16 v[92:95], v[152:155], v[28:31], v[92:95]\n"
        "v_exp_f32 v114, v22\n"
        "v_mfma_f32_16x16x32_bf16 v[96:99], v[156:159], v[28:31], v[96:99]\n"
        "v_exp_f32 v115, v23\n"
        "v_fma_f32 v112, v112, v69, v69\n"
        "v_mfma_f32_16x16x32_bf16 v[100:103], v[160:163], v[28:31], v[100:103]\n"
        "v_fma_f32 v113, v113, v69, v69\n"
        "v_fma_f32 v114, v114, v69, v69\n"
        "buffer_load_dwordx4 a[56:59], v2, s[24:27], s86 offen offset:3072 sc1\n"
        "s_waitcnt vmcnt(14)\n"
        "v_mfma_f32_16x16x32_bf16 v[84:87], v[164:167], a[212:215], v[84:87]\n"
        "v_fma_f32 v115, v115, v69, v69\n"
        "v_rcp_f32 v112, v112\n"
        "v_mfma_f32_16x16x32_bf16 v[88:91], v[168:171], a[212:215], v[88:91]\n"
        "v_rcp_f32 v113, v113\n"
        "v_mfma_f32_16x16x32_bf16 v[92:95], v[172:175], a[212:215], v[92:95]\n"
        "v_rcp_f32 v114, v114\n"
        "v_rcp_f32 v115, v115\n"
        "v_mfma_f32_16x16x32_bf16 v[96:99], v[176:179], a[212:215], v[96:99]\n"
        "v_mul_f32 v112, v112, v20\n"
        "v_mul_f32 v113, v113, v21\n"
        "v_mul_f32 v114, v114, v22\n"
        "v_mfma_f32_16x16x32_bf16 v[100:103], v[180:183], a[212:215], v[100:103]\n"
        "v_mul_f32 v115, v115, v23\n"
        "v_cvt_pk_bf16_f32 v62, v112, v113\n"
        "buffer_load_dwordx4 a[60:63], v3, s[24:27], s86 offen sc1\n"
        "s_waitcnt vmcnt(14)\n"
        "v_mfma_f32_16x16x32_bf16 v[84:87], v[184:187], a[12:15], v[84:87]\n"
        "v_cvt_pk_bf16_f32 v63, v114, v115\n"
        "buffer_store_dwordx4 v[60:63], v2, s[24:27], s87 offen sc1\n"
        "s_add_u32 s87, s87, 6144\n"
        "v_mfma_f32_16x16x32_bf16 v[88:91], v[188:191], a[12:15], v[88:91]\n"
        "v_fma_f32 v20, v123, v40, v52\n"
        "v_fma_f32 v21, v123, v41, v53\n"
        "v_fma_f32 v22, v123, v42, v54\n"
        "v_mfma_f32_16x16x32_bf16 v[92:95], v[192:195], a[12:15], v[92:95]\n"
        "v_fma_f32 v23, v123, v43, v55\n"
        "v_fmac_f32 v20, v116, v122\n"
        "v_fmac_f32 v21, v117, v122\n"
        "v_mfma_f32_16x16x32_bf16 v[96:99], v[196:199], a[12:15], v[96:99]\n"
        "v_fmac_f32 v22, v118, v122\n"
        "v_fmac_f32 v23, v119, v122\n"
        "v_mfma_f32_16x16x32_bf16 v[100:103], v[200:203], a[12:15], v[100:103]\n"
        "v_exp_f32 v116, v20\n"
        "buffer_load_dwordx4 a[64:67], v3, s[24:27], s86 offen offset:1024 sc1\n"
        "s_waitcnt vmcnt(15)\n"
        "v_mfma_f32_16x16x32_bf16 v[84:87], v[204:207], a[16:19], v[84:87]\n"
        "v_exp_f32 v117, v21\n"
        "v_exp_f32 v118, v22\n"
        "v_mfma_f32_16x16x32_bf16 v[88:91], v[208:211], a[16:19], v[88:91]\n"
        "v_exp_f32 v119, v23\n"
        "v_fma_f32 v116, v116, v69, v69\n"
        "v_mfma_f32_16x16x32_bf16 v[92:95], v[212:215], a[16:19], v[92:95]\n"
        "v_fma_f32 v117, v117, v69, v69\n"
        "v_fma_f32 v118, v118, v69, v69\n"
        "v_fma_f32 v119, v119, v69, v69\n"
        "v_mfma_f32_16x16x32_bf16 v[96:99], v[216:219], a[16:19], v[96:99]\n"
        "v_rcp_f32 v116, v116\n"
        "v_mfma_f32_16x16x32_bf16 v[100:103], v[220:223], a[16:19], v[100:103]\n"
        "v_rcp_f32 v117, v117\n"
        "buffer_load_dwordx4 a[68:71], v3, s[24:27], s86 offen offset:2048 sc1\n"
        "s_waitcnt vmcnt(15)\n"
        "v_mfma_f32_16x16x32_bf16 v[84:87], v[224:227], a[20:23], v[84:87]\n"
        "v_rcp_f32 v118, v118\n"
        "v_rcp_f32 v119, v119\n"
        "v_mfma_f32_16x16x32_bf16 v[88:91], v[228:231], a[20:23], v[88:91]\n"
        "v_mul_f32 v116, v116, v20\n"
        "v_mul_f32 v117, v117, v21\n"
        "v_mul_f32 v118, v118, v22\n"
        "v_mfma_f32_16x16x32_bf16 v[92:95], v[232:235], a[20:23], v[92:95]\n"
        "v_mul_f32 v119, v119, v23\n"
        "v_cvt_pk_bf16_f32 v64, v116, v117\n"
        "v_cvt_pk_bf16_f32 v65, v118, v119\n"
        "v_mfma_f32_16x16x32_bf16 v[96:99], v[236:239], a[20:23], v[96:99]\n"
        "v_permlane32_swap_b32 v64, v66\n"
        "v_mfma_f32_16x16x32_bf16 v[100:103], v[240:243], a[20:23], v[100:103]\n"
        "v_permlane32_swap_b32 v65, v67\n"
        "buffer_store_dwordx4 v[64:67], v9, s[24:27], s88 offen sc1\n"
        "s_add_u32 s88, s88, 6144\n"
        "buffer_load_dwordx4 a[72:75], v3, s[24:27], s86 offen offset:3072 sc1\n"
        "s_waitcnt vmcnt(15)\n"
        "v_mfma_f32_16x16x32_bf16 v[104:107], v[124:127], a[24:27], 0\n"
        "v_mfma_f32_16x16x32_bf16 v[108:111], v[128:131], a[24:27], 0\n"
        "v_mfma_f32_16x16x32_bf16 v[112:115], v[132:135], a[24:27], 0\n"
        "v_mfma_f32_16x16x32_bf16 v[116:119], v[136:139], a[24:27], 0\n"
        "v_mfma_f32_16x16x32_bf16 v[120:123], v[140:143], a[24:27], 0\n"
        "buffer_load_dwordx4 a[76:79], v4, s[24:27], s12 offen offset:2048 sc1\n"
        "s_waitcnt vmcnt(15)\n"
        "v_mfma_f32_16x16x32_bf16 v[104:107], v[144:147], a[28:31], v[104:107]\n"
        "v_mfma_f32_16x16x32_bf16 v[108:111], v[148:151], a[28:31], v[108:111]\n"
        "v_mfma_f32_16x16x32_bf16 v[112:115], v[152:155], a[28:31], v[112:115]\n"
        "v_mfma_f32_16x16x32_bf16 v[116:119], v[156:159], a[28:31], v[116:119]\n"
        "v_mfma_f32_16x16x32_bf16 v[120:123], v[160:163], a[28:31], v[120:123]\n"
        "buffer_load_dwordx4 a[80:83], v2, s[24:27], s14 offen offset:2048 sc1\n"
        "s_waitcnt vmcnt(15)\n"
        "v_mfma_f32_16x16x32_bf16 v[104:107], v[164:167], a[32:35], v[104:107]\n"
        "v_mfma_f32_16x16x32_bf16 v[108:111], v[168:171], a[32:35], v[108:111]\n"
        "v_mfma_f32_16x16x32_bf16 v[112:115], v[172:175], a[32:35], v[112:115]\n"
        "v_mfma_f32_16x16x32_bf16 v[116:119], v[176:179], a[32:35], v[116:119]\n"
        "v_mfma_f32_16x16x32_bf16 v[120:123], v[180:183], a[32:35], v[120:123]\n"
        "buffer_load_dwordx4 a[84:87], v4, s[24:27], s86 offen sc1\n"
        "s_waitcnt vmcnt(15)\n"
        "v_mfma_f32_16x16x32_bf16 v[84:87], a[0:3], a[36:39], v[84:87]\n"
        "v_mfma_f32_16x16x32_bf16 v[88:91], a[4:7], a[36:39], v[88:91]\n"
        "v_mfma_f32_16x16x32_bf16 v[92:95], a[8:11], a[36:39], v[92:95]\n"
        "buffer_load_dwordx4 a[88:91], v4, s[24:27], s86 offen offset:1024 sc1\n"
        "s_waitcnt vmcnt(15)\n"
        "v_mfma_f32_16x16x32_bf16 v[96:99], a[40:43], a[36:39], v[96:99]\n"
        "buffer_load_dwordx4 a[92:95], v4, s[24:27], s86 offen offset:2048 sc1\n"
        "s_waitcnt vmcnt(14)\n"
        "v_mfma_f32_16x16x32_bf16 v[100:103], a[44:47], a[36:39], v[100:103]\n"
        "buffer_load_dwordx4 a[96:99], v4, s[24:27], s86 offen offset:3072 sc1\n"
        "s_waitcnt vmcnt(14)\n"
        "v_mfma_f32_16x16x32_bf16 v[104:107], v[184:187], a[48:51], v[104:107]\n"
        "v_mfma_f32_16x16x32_bf16 v[108:111], v[188:191], a[48:51], v[108:111]\n"
        "v_mfma_f32_16x16x32_bf16 v[112:115], v[192:195], a[48:51], v[112:115]\n"
        "v_mfma_f32_16x16x32_bf16 v[116:119], v[196:199], a[48:51], v[116:119]\n"
        "v_mfma_f32_16x16x32_bf16 v[120:123], v[200:203], a[48:51], v[120:123]\n"
        "buffer_load_dwordx4 a[100:103], v5, s[24:27], s86 offen sc1\n"
        "buffer_load_dwordx4 a[104:107], v4, s[24:27], s12 offen offset:3072 sc1\n"
        "s_waitcnt vmcnt(14)\n"
        "v_mfma_f32_16x16x32_bf16 v[84:87], a[56:59], a[52:55], v[84:87]\n"
        "buffer_load_dwordx4 a[108:111], v2, s[24:27], s14 offen offset:3072 sc1\n"
        "s_waitcnt vmcnt(14)\n"
        "v_mfma_f32_16x16x32_bf16 v[88:91], a[60:63], a[52:55], v[88:91]\n"
        "buffer_load_dwordx4 a[112:115], v5, s[24:27], s86 offen offset:1024 sc1\n"
        "s_waitcnt vmcnt(13)\n"
        "v_mfma_f32_16x16x32_bf16 v[92:95], a[64:67], a[52:55], v[92:95]\n"
        "buffer_load_dwordx4 a[116:119], v5, s[24:27], s86 offen offset:2048 sc1\n"
        "s_waitcnt vmcnt(13)\n"
        "v_mfma_f32_16x16x32_bf16 v[96:99], a[68:71], a[52:55], v[96:99]\n"
        "buffer_load_dwordx4 a[120:123], v5, s[24:27], s86 offen offset:3072 sc1\n"
        "s_waitcnt vmcnt(12)\n"
        "v_mfma_f32_16x16x32_bf16 v[100:103], a[72:75], a[52:55], v[100:103]\n"
        "s_add_u32 s86, s61, 49152\n"
        "buffer_load_dwordx4 a[124:127], v2, s[24:27], s86 offen sc1\n"
        "s_waitcnt vmcnt(12)\n"
        "v_mfma_f32_16x16x32_bf16 v[104:107], v[204:207], a[76:79], v[104:107]\n"
        "v_mfma_f32_16x16x32_bf16 v[108:111], v[208:211], a[76:79], v[108:111]\n"
        "v_mfma_f32_16x16x32_bf16 v[112:115], v[212:215], a[76:79], v[112:115]\n"
        "v_mfma_f32_16x16x32_bf16 v[116:119], v[216:219], a[76:79], v[116:119]\n"
        "v_mfma_f32_16x16x32_bf16 v[120:123], v[220:223], a[76:79], v[120:123]\n"
        "buffer_load_dwordx4 a[216:219], v2, s[24:27], s86 offen offset:1024 sc1\n"
        "buffer_load_dwordx4 a[220:223], v3, s[24:27], s14 offen offset:2048 sc1\n"
        "s_waitcnt vmcnt(12)\n"
        "v_mfma_f32_16x16x32_bf16 v[84:87], a[84:87], a[80:83], v[84:87]\n"
        "buffer_load_dwordx4 a[224:227], v3, s[24:27], s14 offen sc1\n"
        "s_waitcnt vmcnt(12)\n"
        "v_mfma_f32_16x16x32_bf16 v[88:91], a[88:91], a[80:83], v[88:91]\n"
        "buffer_load_dwordx4 a[228:231], v2, s[24:27], s86 offen offset:2048 sc1\n"
        "s_waitcnt vmcnt(12)\n"
        "v_mfma_f32_16x16x32_bf16 v[92:95], a[92:95], a[80:83], v[92:95]\n"
        "buffer_load_dwordx4 a[232:235], v2, s[24:27], s86 offen offset:3072 sc1\n"
        "s_waitcnt vmcnt(12)\n"
        "v_mfma_f32_16x16x32_bf16 v[96:99], a[96:99], a[80:83], v[96:99]\n"
        "buffer_load_dwordx4 a[236:239], v3, s[24:27], s86 offen sc1\n"
        "s_waitcnt vmcnt(12)\n"
        "v_mfma_f32_16x16x32_bf16 v[100:103], a[100:103], a[80:83], v[100:103]\n"
        "buffer_load_dwordx4 a[240:243], v3, s[24:27], s86 offen offset:1024 sc1\n"
        "s_waitcnt vmcnt(12)\n"
        "v_mfma_f32_16x16x32_bf16 v[104:107], v[224:227], a[104:107], v[104:107]\n"
        "v_mfma_f32_16x16x32_bf16 v[108:111], v[228:231], a[104:107], v[108:111]\n"
        "v_mfma_f32_16x16x32_bf16 v[112:115], v[232:235], a[104:107], v[112:115]\n"
        "v_mfma_f32_16x16x32_bf16 v[116:119], v[236:239], a[104:107], v[116:119]\n"
        "v_mfma_f32_16x16x32_bf16 v[120:123], v[240:243], a[104:107], v[120:123]\n"
        "buffer_load_dwordx4 a[244:247], v3, s[24:27], s86 offen offset:2048 sc1\n"
        "buffer_load_dwordx4 a[248:251], v3, s[24:27], s14 offen offset:3072 sc1\n"
        "s_waitcnt vmcnt(12)\n"
        "v_mfma_f32_16x16x32_bf16 v[84:87], a[112:115], a[108:111], v[84:87]\n"
        "buffer_load_dwordx4 a[252:255], v3, s[24:27], s14 offen offset:1024 sc1\n"
        "s_waitcnt vmcnt(12)\n"
        "v_mfma_f32_16x16x32_bf16 v[88:91], a[116:119], a[108:111], v[88:91]\n"
        "buffer_load_dwordx4 a[128:131], v3, s[24:27], s86 offen offset:3072 sc1\n"
        "s_waitcnt vmcnt(12)\n"
        "v_mfma_f32_16x16x32_bf16 v[92:95], a[120:123], a[108:111], v[92:95]\n"
        "buffer_load_dwordx4 a[132:135], v4, s[24:27], s86 offen sc1\n"
        "s_waitcnt vmcnt(12)\n"
        "v_mfma_f32_16x16x32_bf16 v[96:99], a[124:127], a[108:111], v[96:99]\n"
        "buffer_load_dwordx4 a[136:139], v4, s[24:27], s86 offen offset:1024 sc1\n"
        "s_waitcnt vmcnt(12)\n"
        "v_mfma_f32_16x16x32_bf16 v[100:103], a[216:219], a[108:111], v[100:103]\n"
        "buffer_load_dwordx4 a[140:143], v4, s[24:27], s86 offen offset:2048 sc1\n"
        "s_waitcnt vmcnt(12)\n"
        "v_mfma_f32_16x16x32_bf16 v[104:107], a[0:3], a[220:223], v[104:107]\n"
        "v_mfma_f32_16x16x32_bf16 v[108:111], a[4:7], a[220:223], v[108:111]\n"
        "v_mfma_f32_16x16x32_bf16 v[112:115], a[8:11], a[220:223], v[112:115]\n"
        "v_mfma_f32_16x16x32_bf16 v[116:119], a[40:43], a[220:223], v[116:119]\n"
        "v_mfma_f32_16x16x32_bf16 v[120:123], a[44:47], a[220:223], v[120:123]\n"
        "buffer_load_dwordx4 a[144:147], v4, s[24:27], s86 offen offset:3072 sc1\n"
        "buffer_load_dwordx4 a[148:151], v4, s[24:27], s14 offen sc1\n"
        "s_waitcnt vmcnt(12)\n"
        "v_mfma_f32_16x16x32_bf16 v[84:87], a[228:231], a[224:227], v[84:87]\n"
        "buffer_load_dwordx4 a[152:155], v4, s[24:27], s14 offen offset:1024 sc1\n"
        "s_waitcnt vmcnt(12)\n"
        "v_mfma_f32_16x16x32_bf16 v[88:91], a[232:235], a[224:227], v[88:91]\n"
        "buffer_load_dwordx4 a[156:159], v4, s[24:27], s14 offen offset:2048 sc1\n"
        "s_waitcnt vmcnt(12)\n"
        "v_mfma_f32_16x16x32_bf16 v[92:95], a[236:239], a[224:227], v[92:95]\n"
        "buffer_load_dwordx4 a[160:163], v4, s[24:27], s14 offen offset:3072 sc1\n"
        "s_waitcnt vmcnt(12)\n"
        "v_mfma_f32_16x16x32_bf16 v[96:99], a[240:243], a[224:227], v[96:99]\n"
        "buffer_load_dwordx4 a[164:167], v2, s[24:27], s13 offen sc1\n"
        "s_waitcnt vmcnt(12)\n"
        "v_mfma_f32_16x16x32_bf16 v[100:103], a[244:247], a[224:227], v[100:103]\n"
        "buffer_load_dwordx4 a[168:171], v2, s[24:27], s13 offen offset:1024 sc1\n"
        "s_waitcnt vmcnt(12)\n"
        "v_mfma_f32_16x16x32_bf16 v[104:107], a[56:59], a[248:251], v[104:107]\n"
        "v_mfma_f32_16x16x32_bf16 v[108:111], a[60:63], a[248:251], v[108:111]\n"
        "v_mfma_f32_16x16x32_bf16 v[112:115], a[64:67], a[248:251], v[112:115]\n"
        "v_mfma_f32_16x16x32_bf16 v[116:119], a[68:71], a[248:251], v[116:119]\n"
        "v_mfma_f32_16x16x32_bf16 v[120:123], a[72:75], a[248:251], v[120:123]\n"
        "buffer_load_dwordx4 a[172:175], v2, s[24:27], s13 offen offset:2048 sc1\n"
        "buffer_load_dwordx4 a[176:179], v2, s[24:27], s13 offen offset:3072 sc1\n"
        "s_waitcnt vmcnt(12)\n"
        "v_mfma_f32_16x16x32_bf16 v[84:87], a[128:131], a[252:255], v[84:87]\n"
        "buffer_load_dwordx4 a[180:183], v3, s[24:27], s13 offen sc1\n"
        "s_waitcnt vmcnt(12)\n"
        "v_mfma_f32_16x16x32_bf16 v[88:91], a[132:135], a[252:255], v[88:91]\n"
        "buffer_load_dwordx4 a[184:187], v3, s[24:27], s13 offen offset:1024 sc1\n"
        "s_waitcnt vmcnt(12)\n"
        "v_mfma_f32_16x16x32_bf16 v[92:95], a[136:139], a[252:255], v[92:95]\n"
        "buffer_load_dwordx4 a[188:191], v2, s[24:27], s15 offen sc1\n"
        "s_waitcnt vmcnt(12)\n"
        "v_mfma_f32_16x16x32_bf16 v[96:99], a[140:143], a[252:255], v[96:99]\n"
        "buffer_load_dwordx4 a[192:195], v2, s[24:27], s15 offen offset:1024 sc1\n"
        "s_waitcnt vmcnt(12)\n"
        "v_mfma_f32_16x16x32_bf16 v[100:103], a[144:147], a[252:255], v[100:103]\n"
        "buffer_load_dwordx4 a[196:199], v2, s[24:27], s15 offen offset:2048 sc1\n"
        "s_waitcnt vmcnt(12)\n"
        "v_mfma_f32_16x16x32_bf16 v[104:107], a[84:87], a[148:151], v[104:107]\n"
        "v_mfma_f32_16x16x32_bf16 v[108:111], a[88:91], a[148:151], v[108:111]\n"
        "v_mfma_f32_16x16x32_bf16 v[112:115], a[92:95], a[148:151], v[112:115]\n"
        "v_mfma_f32_16x16x32_bf16 v[116:119], a[96:99], a[148:151], v[116:119]\n"
        "v_cmp_u_f32 vcc, v84, v84\n"
        "v_mfma_f32_16x16x32_bf16 v[120:123], a[100:103], a[148:151], v[120:123]\n"
        "buffer_load_dwordx4 a[200:203], v2, s[24:27], s15 offen offset:3072 sc1\n"
        "s_waitcnt vmcnt(12)\n"
        "v_mfma_f32_16x16x32_bf16 v[104:107], a[112:115], a[152:155], v[104:107]\n"
        "s_cbranch_vccnz .Ls25\n"
        ".Lb25:\n"
        "v_mfma_f32_16x16x32_bf16 v[108:111], a[116:119], a[152:155], v[108:111]\n"
        "v_mfma_f32_16x16x32_bf16 v[112:115], a[120:123], a[152:155], v[112:115]\n"
        "v_add_u32 v13, s90, v2\n"
        "v_mfma_f32_16x16x32_bf16 v[116:119], a[124:127], a[152:155], v[116:119]\n"
        "v_mfma_f32_16x16x32_bf16 v[120:123], a[216:219], a[152:155], v[120:123]\n"
        "ds_write_b128 v13, v[84:87]\n"
        "buffer_load_dwordx4 a[204:207], v3, s[24:27], s15 offen sc1\n"
        "s_waitcnt vmcnt(12)\n"
        "v_mfma_f32_16x16x32_bf16 v[104:107], a[228:231], a[156:159], v[104:107]\n"
        "v_mfma_f32_16x16x32_bf16 v[108:111], a[232:235], a[156:159], v[108:111]\n"
        "ds_write_b128 v13, v[88:91] offset:1024\n"
        "v_mfma_f32_16x16x32_bf16 v[112:115], a[236:239], a[156:159], v[112:115]\n"
        "v_mfma_f32_16x16x32_bf16 v[116:119], a[240:243], a[156:159], v[116:119]\n"
        "ds_write_b128 v13, v[92:95] offset:2048\n"
        "v_mfma_f32_16x16x32_bf16 v[120:123], a[244:247], a[156:159], v[120:123]\n"
        "buffer_load_dwordx4 a[208:211], v3, s[24:27], s15 offen offset:1024 sc1\n"
        "s_waitcnt vmcnt(12)\n"
        "v_mfma_f32_16x16x32_bf16 v[104:107], a[128:131], a[160:163], v[104:107]\n"
        "ds_write_b128 v13, v[96:99] offset:3072\n"
        "v_mfma_f32_16x16x32_bf16 v[108:111], a[132:135], a[160:163], v[108:111]\n"
        "v_mfma_f32_16x16x32_bf16 v[112:115], a[136:139], a[160:163], v[112:115]\n"
        "ds_write_b128 v13, v[100:103] offset:4096\n"
        "v_mfma_f32_16x16x32_bf16 v[116:119], a[140:143], a[160:163], v[116:119]\n"
        "v_mfma_f32_16x16x32_bf16 v[120:123], a[144:147], a[160:163], v[120:123]\n"
        "buffer_load_dwordx4 a[212:215], v3, s[24:27], s13 offen offset:2048 sc1\n"
        "s_waitcnt vmcnt(12)\n"
        "v_mfma_f32_16x16x32_bf16 v[84:87], v[124:127], a[164:167], 0\n"
        "v_mfma_f32_16x16x32_bf16 v[88:91], v[128:131], a[164:167], 0\n"
        "v_mfma_f32_16x16x32_bf16 v[92:95], v[132:135], a[164:167], 0\n"
        "v_mfma_f32_16x16x32_bf16 v[96:99], v[136:139], a[164:167], 0\n"
        "v_cmp_u_f32 vcc, v104, v104\n"
        "v_mfma_f32_16x16x32_bf16 v[100:103], v[140:143], a[164:167], 0\n"
        "buffer_load_dwordx4 a[12:15], v3, s[24:27], s13 offen offset:3072 sc1\n"
        "s_waitcnt vmcnt(12)\n"
        "v_mfma_f32_16x16x32_bf16 v[84:87], v[144:147], a[168:171], v[84:87]\n"
        "v_mfma_f32_16x16x32_bf16 v[88:91], v[148:151], a[168:171], v[88:91]\n"
        "v_mfma_f32_16x16x32_bf16 v[92:95], v[152:155], a[168:171], v[92:95]\n"
        "s_cbranch_vccnz .Ls26\n"
        ".Lb26:\n"
        "v_mfma_f32_16x16x32_bf16 v[96:99], v[156:159], a[168:171], v[96:99]\n"
        "v_mfma_f32_16x16x32_bf16 v[100:103], v[160:163], a[168:171], v[100:103]\n"
        "buffer_load_dwordx4 a[16:19], v4, s[24:27], s13 offen sc1\n"
        "s_waitcnt vmcnt(12)\n"
        "v_mfma_f32_16x16x32_bf16 v[84:87], v[164:167], a[172:175], v[84:87]\n"
        "v_mfma_f32_16x16x32_bf16 v[88:91], v[168:171], a[172:175], v[88:91]\n"
        "v_add_u32 v13, s91, v2\n"
        "v_mfma_f32_16x16x32_bf16 v[92:95], v[172:175], a[172:175], v[92:95]\n"
        "v_mfma_f32_16x16x32_bf16 v[96:99], v[176:179], a[172:175], v[96:99]\n"
        "v_mfma_f32_16x16x32_bf16 v[100:103], v[180:183], a[172:175], v[100:103]\n"
        "buffer_load_dwordx4 a[20:23], v4, s[24:27], s13 offen offset:1024 sc1\n"
        "s_waitcnt vmcnt(12)\n"
        "v_mfma_f32_16x16x32_bf16 v[84:87], v[184:187], a[176:179], v[84:87]\n"
        "ds_write_b128 v13, v[104:107]\n"
        "v_mfma_f32_16x16x32_bf16 v[88:91], v[188:191], a[176:179], v[88:91]\n"
        "v_mfma_f32_16x16x32_bf16 v[92:95], v[192:195], a[176:179], v[92:95]\n"
        "v_mfma_f32_16x16x32_bf16 v[96:99], v[196:199], a[176:179], v[96:99]\n"
        "v_mfma_f32_16x16x32_bf16 v[100:103], v[200:203], a[176:179], v[100:103]\n"
        "ds_write_b128 v13, v[108:111] offset:1024\n"
        "buffer_load_dwordx4 a[36:39], v4, s[24:27], s13 offen offset:2048 sc1\n"
        "s_waitcnt vmcnt(12)\n"
        "v_mfma_f32_16x16x32_bf16 v[84:87], v[204:207], a[180:183], v[84:87]\n"
        "v_mfma_f32_16x16x32_bf16 v[88:91], v[208:211], a[180:183], v[88:91]\n"
        "v_mfma_f32_16x16x32_bf16 v[92:95], v[212:215], a[180:183], v[92:95]\n"
        "v_mfma_f32_16x16x32_bf16 v[96:99], v[216:219], a[180:183], v[96:99]\n"
        "ds_write_b128 v13, v[112:115] offset:2048\n"
        "v_mfma_f32_16x16x32_bf16 v[100:103], v[220:223], a[180:183], v[100:103]\n"
        "buffer_load_dwordx4 a[52:55], v4, s[24:27], s13 offen offset:3072 sc1\n"
        "s_waitcnt vmcnt(12)\n"
        "v_mfma_f32_16x16x32_bf16 v[84:87], v[224:227], a[184:187], v[84:87]\n"
        "v_mfma_f32_16x16x32_bf16 v[88:91], v[228:231], a[184:187], v[88:91]\n"
        "v_mfma_f32_16x16x32_bf16 v[92:95], v[232:235], a[184:187], v[92:95]\n"
        "ds_write_b128 v13, v[116:119] offset:3072\n"
        "v_mfma_f32_16x16x32_bf16 v[96:99], v[236:239], a[184:187], v[96:99]\n"
        "v_mfma_f32_16x16x32_bf16 v[100:103], v[240:243], a[184:187], v[100:103]\n"
        "buffer_load_dwordx4 a[80:83], v3, s[24:27], s15 offen offset:2048 sc1\n"
        "s_waitcnt vmcnt(12)\n"
        "v_mfma_f32_16x16x32_bf16 v[84:87], a[0:3], a[188:191], v[84:87]\n"
        "v_mfma_f32_16x16x32_bf16 v[88:91], a[4:7], a[188:191], v[88:91]\n"
        "ds_write_b128 v13, v[120:123] offset:4096\n"
        "v_mfma_f32_16x16x32_bf16 v[92:95], a[8:11], a[188:191], v[92:95]\n"
        "v_mfma_f32_16x16x32_bf16 v[96:99], a[40:43], a[188:191], v[96:99]\n"
        "v_mfma_f32_16x16x32_bf16 v[100:103], a[44:47], a[188:191], v[100:103]\n"
        "buffer_load_dwordx4 a[108:111], v3, s[24:27], s15 offen offset:3072 sc1\n"
        "s_waitcnt vmcnt(12)\n"
        "v_mfma_f32_16x16x32_bf16 v[84:87], a[56:59], a[192:195], v[84:87]\n"
        "v_mfma_f32_16x16x32_bf16 v[88:91], a[60:63], a[192:195], v[88:91]\n"
        "v_mfma_f32_16x16x32_bf16 v[92:95], a[64:67], a[192:195], v[92:95]\n"
        "v_mfma_f32_16x16x32_bf16 v[96:99], a[68:71], a[192:195], v[96:99]\n"
        "v_mfma_f32_16x16x32_bf16 v[100:103], a[72:75], a[192:195], v[100:103]\n"
        "buffer_load_dwordx4 a[224:227], v4, s[24:27], s15 offen sc1\n"
        "s_waitcnt vmcnt(12)\n"
        "v_mfma_f32_16x16x32_bf16 v[84:87], a[84:87], a[196:199], v[84:87]\n"
        "v_mfma_f32_16x16x32_bf16 v[88:91], a[88:91], a[196:199], v[88:91]\n"
        "v_mfma_f32_16x16x32_bf16 v[92:95], a[92:95], a[196:199], v[92:95]\n"
        "v_mfma_f32_16x16x32_bf16 v[96:99], a[96:99], a[196:199], v[96:99]\n"
        "v_mfma_f32_16x16x32_bf16 v[100:103], a[100:103], a[196:199], v[100:103]\n"
        "buffer_load_dwordx4 a[252:255], v4, s[24:27], s15 offen offset:1024 sc1\n"
        "s_waitcnt vmcnt(12)\n"
        "v_mfma_f32_16x16x32_bf16 v[84:87], a[112:115], a[200:203], v[84:87]\n"
        "v_mfma_f32_16x16x32_bf16 v[88:91], a[116:119], a[200:203], v[88:91]\n"
        "v_mfma_f32_16x16x32_bf16 v[92:95], a[120:123], a[200:203], v[92:95]\n"
        "v_mfma_f32_16x16x32_bf16 v[96:99], a[124:127], a[200:203], v[96:99]\n"
        "v_mfma_f32_16x16x32_bf16 v[100:103], a[216:219], a[200:203], v[100:103]\n"
        "buffer_load_dwordx4 a[24:27], v4, s[24:27], s15 offen offset:2048 sc1\n"
        "s_waitcnt vmcnt(12)\n"
        "v_mfma_f32_16x16x32_bf16 v[84:87], a[228:231], a[204:207], v[84:87]\n"
        "v_mfma_f32_16x16x32_bf16 v[88:91], a[232:235], a[204:207], v[88:91]\n"
        "v_mfma_f32_16x16x32_bf16 v[92:95], a[236:239], a[204:207], v[92:95]\n"
        "v_mfma_f32_16x16x32_bf16 v[96:99], a[240:243], a[204:207], v[96:99]\n"
        "v_mfma_f32_16x16x32_bf16 v[100:103], a[244:247], a[204:207], v[100:103]\n"
        "buffer_load_dwordx4 a[28:31], v4, s[24:27], s15 offen offset:3072 sc1\n"
        "s_waitcnt vmcnt(12)\n"
        "v_mfma_f32_16x16x32_bf16 v[84:87], a[128:131], a[208:211], v[84:87]\n"
        "v_mfma_f32_16x16x32_bf16 v[88:91], a[132:135], a[208:211], v[88:91]\n"
        "v_mfma_f32_16x16x32_bf16 v[92:95], a[136:139], a[208:211], v[92:95]\n"
        "v_mfma_f32_16x16x32_bf16 v[96:99], a[140:143], a[208:211], v[96:99]\n"
        "v_mfma_f32_16x16x32_bf16 v[100:103], a[144:147], a[208:211], v[100:103]\n"
        "s_waitcnt vmcnt(11)\n"
        "v_mfma_f32_16x16x32_bf16 v[104:107], v[124:127], a[212:215], 0\n"
        "v_mfma_f32_16x16x32_bf16 v[108:111], v[128:131], a[212:215], 0\n"
        "v_mfma_f32_16x16x32_bf16 v[112:115], v[132:135], a[212:215], 0\n"
        "v_mfma_f32_16x16x32_bf16 v[116:119], v[136:139], a[212:215], 0\n"
        "v_cmp_u_f32 vcc, v84, v84\n"
        "s_cbranch_vccnz .Ls27\n"
        ".Lb27:\n"
        "v_mfma_f32_16x16x32_bf16 v[120:123], v[140:143], a[212:215], 0\n"
        "ds_read_b128 v[64:67], v15 offset:512\n"
        "ds_read_b128 v[68:71], v15 offset:528\n"
        "s_waitcnt vmcnt(10)\n"
        "v_mfma_f32_16x16x32_bf16 v[104:107], v[144:147], a[12:15], v[104:107]\n"
        "ds_read_b128 v[72:75], v15 offset:544\n"
        "ds_read_b128 v[76:79], v15 offset:560\n"
        "v_mfma_f32_16x16x32_bf16 v[108:111], v[148:151], a[12:15], v[108:111]\n"
        "ds_read_b128 v[80:83], v15 offset:576\n"
        "v_add_u32 v13, s92, v2\n"
        "v_mfma_f32_16x16x32_bf16 v[112:115], v[152:155], a[12:15], v[112:115]\n"
        "v_lshlrev_b32 v44, 16, v244\n"
        "v_and_b32 v45, s56, v244\n"
        "v_mfma_f32_16x16x32_bf16 v[116:119], v[156:159], a[12:15], v[116:119]\n"
        "v_lshlrev_b32 v46, 16, v245\n"
        "v_and_b32 v47, s56, v245\n"
        "v_mfma_f32_16x16x32_bf16 v[120:123], v[160:163], a[12:15], v[120:123]\n"
        "ds_write_b128 v13, v[84:87]\n"
        "v_lshlrev_b32 v48, 16, v246\n"
        "s_waitcnt vmcnt(9)\n"
        "v_mfma_f32_16x16x32_bf16 v[104:107], v[164:167], a[16:19], v[104:107]\n"
        "v_and_b32 v49, s56, v246\n"
        "v_lshlrev_b32 v50, 16, v247\n"
        "v_mfma_f32_16x16x32_bf16 v[108:111], v[168:171], a[16:19], v[108:111]\n"
        "v_and_b32 v51, s56, v247\n"
        "ds_write_b128 v13, v[88:91] offset:1024\n"
        "v_mfma_f32_16x16x32_bf16 v[112:115], v[172:175], a[16:19], v[112:115]\n"
        "v_lshlrev_b32 v52, 16, v248\n"
        "v_and_b32 v53, s56, v248\n"
        "v_mfma_f32_16x16x32_bf16 v[116:119], v[176:179], a[16:19], v[116:119]\n"
        "v_lshlrev_b32 v54, 16, v249\n"
        "v_and_b32 v55, s56, v249\n"
        "v_mfma_f32_16x16x32_bf16 v[120:123], v[180:183], a[16:19], v[120:123]\n"
        "ds_write_b128 v13, v[92:95] offset:2048\n"
        "v_lshlrev_b32 v56, 16, v250\n"
        "s_waitcnt vmcnt(8)\n"
        "v_mfma_f32_16x16x32_bf16 v[104:107], v[184:187], a[20:23], v[104:107]\n"
        "v_and_b32 v57, s56, v250\n"
        "v_lshlrev_b32 v58, 16, v251\n"
        "v_mfma_f32_16x16x32_bf16 v[108:111], v[188:191], a[20:23], v[108:111]\n"
        "v_and_b32 v59, s56, v251\n"
        "ds_write_b128 v13, v[96:99] offset:3072\n"
        "v_mfma_f32_16x16x32_bf16 v[112:115], v[192:195], a[20:23], v[112:115]\n"
        "v_lshlrev_b32 v60, 16, v252\n"
        "v_and_b32 v61, s56, v252\n"
        "v_mfma_f32_16x16x32_bf16 v[116:119], v[196:199], a[20:23], v[116:119]\n"
        "v_lshlrev_b32 v62, 16, v253\n"
        "v_and_b32 v63, s56, v253\n"
        "v_mfma_f32_16x16x32_bf16 v[120:123], v[200:203], a[20:23], v[120:123]\n"
        "ds_write_b128 v13, v[100:103] offset:4096\n"
        "s_waitcnt vmcnt(7)\n"
        "v_mfma_f32_16x16x32_bf16 v[104:107], v[204:207], a[36:39], v[104:107]\n"
        "s_waitcnt lgkmcnt(0)\n"
        "s_barrier\n"
        "ds_read_b128 v[84:87], v12\n"
        "v_mfma_f32_16x16x32_bf16 v[108:111], v[208:211], a[36:39], v[108:111]\n"
        "ds_read_b128 v[88:91], v12 offset:1024\n"
        "ds_read_b128 v[92:95], v12 offset:2048\n"
        "v_mfma_f32_16x16x32_bf16 v[112:115], v[212:215], a[36:39], v[112:115]\n"
        "ds_read_b128 v[96:99], v12 offset:3072\n"
        "ds_read_b128 v[100:103], v12 offset:4096\n"
        "v_mfma_f32_16x16x32_bf16 v[116:119], v[216:219], a[36:39], v[116:119]\n"
        "ds_read_b128 v[20:23], v12 offset:5120\n"
        "ds_read_b128 v[24:27], v12 offset:6144\n"
        "v_mfma_f32_16x16x32_bf16 v[120:123], v[220:223], a[36:39], v[120:123]\n"
        "ds_read_b128 v[28:31], v12 offset:7168\n"
        "ds_read_b128 v[32:35], v12 offset:8192\n"
        "s_waitcnt vmcnt(6)\n"
        "v_mfma_f32_16x16x32_bf16 v[104:107], v[224:227], a[52:55], v[104:107]\n"
        "ds_read_b128 v[36:39], v12 offset:9216\n"
        "s_waitcnt lgkmcnt(9)\n"
        "v_fmac_f32 v44, v64, v84\n"
        "v_mfma_f32_16x16x32_bf16 v[108:111], v[228:231], a[52:55], v[108:111]\n"
        "v_fmac_f32 v45, v65, v85\n"
        "v_fmac_f32 v46, v66, v86\n"
        "v_mfma_f32_16x16x32_bf16 v[112:115], v[232:235], a[52:55], v[112:115]\n"
        "v_fmac_f32 v47, v67, v87\n"
        "s_waitcnt lgkmcnt(8)\n"
        "v_fmac_f32 v48, v68, v88\n"
        "v_mfma_f32_16x16x32_bf16 v[116:119], v[236:239], a[52:55], v[116:119]\n"
        "v_fmac_f32 v49, v69, v89\n"
        "v_fmac_f32 v50, v70, v90\n"
        "v_mfma_f32_16x16x32_bf16 v[120:123], v[240:243], a[52:55], v[120:123]\n"
        "v_fmac_f32 v51, v71, v91\n"
        "s_waitcnt vmcnt(5)\n"
        "v_mfma_f32_16x16x32_bf16 v[104:107], a[0:3], a[80:83], v[104:107]\n"
        "s_waitcnt lgkmcnt(7)\n"
        "v_fmac_f32 v52, v72, v92\n"
        "v_fmac_f32 v53, v73, v93\n"
        "v_mfma_f32_16x16x32_bf16 v[108:111], a[4:7], a[80:83], v[108:111]\n"
        "v_fmac_f32 v54, v74, v94\n"
        "v_fmac_f32 v55, v75, v95\n"
        "v_mfma_f32_16x16x32_bf16 v[112:115], a[8:11], a[80:83], v[112:115]\n"
        "s_waitcnt lgkmcnt(6)\n"
        "v_fmac_f32 v56, v76, v96\n"
        "v_fmac_f32 v57, v77, v97\n"
        "v_mfma_f32_16x16x32_bf16 v[116:119], a[40:43], a[80:83], v[116:119]\n"
        "v_fmac_f32 v58, v78, v98\n"
        "v_fmac_f32 v59, v79, v99\n"
        "v_mfma_f32_16x16x32_bf16 v[120:123], a[44:47], a[80:83], v[120:123]\n"
        "s_waitcnt lgkmcnt(5)\n"
        "v_fmac_f32 v60, v80, v100\n"
        "v_fmac_f32 v61, v81, v101\n"
        "s_waitcnt vmcnt(4)\n"
        "v_mfma_f32_16x16x32_bf16 v[104:107], a[56:59], a[108:111], v[104:107]\n"
        "v_fmac_f32 v62, v82, v102\n"
        "v_fmac_f32 v63, v83, v103\n"
        "v_mfma_f32_16x16x32_bf16 v[108:111], a[60:63], a[108:111], v[108:111]\n"
        "ds_read_b128 v[84:87], v12 offset:10240\n"
        "ds_read_b128 v[88:91], v12 offset:11264\n"
        "v_mfma_f32_16x16x32_bf16 v[112:115], a[64:67], a[108:111], v[112:115]\n"
        "ds_read_b128 v[92:95], v12 offset:12288\n"
        "ds_read_b128 v[96:99], v12 offset:13312\n"
        "v_mfma_f32_16x16x32_bf16 v[116:119], a[68:71], a[108:111], v[116:119]\n"
        "ds_read_b128 v[100:103], v12 offset:14336\n"
        "s_waitcnt lgkmcnt(9)\n"
        "v_fmac_f32 v44, v64, v20\n"
        "v_mfma_f32_16x16x32_bf16 v[120:123], a[72:75], a[108:111], v[120:123]\n"
        "v_fmac_f32 v45, v65, v21\n"
        "s_waitcnt vmcnt(3)\n"
        "v_mfma_f32_16x16x32_bf16 v[104:107], a[84:87], a[224:227], v[104:107]\n"
        "v_fmac_f32 v46, v66, v22\n"
        "v_fmac_f32 v47, v67, v23\n"
        "v_mfma_f32_16x16x32_bf16 v[108:111], a[88:91], a[224:227], v[108:111]\n"
        "s_waitcnt lgkmcnt(8)\n"
        "v_fmac_f32 v48, v68, v24\n"
        "v_fmac_f32 v49, v69, v25\n"
        "v_mfma_f32_16x16x32_bf16 v[112:115], a[92:95], a[224:227], v[112:115]\n"
        "v_fmac_f32 v50, v70, v26\n"
        "v_fmac_f32 v51, v71, v27\n"
        "v_mfma_f32_16x16x32_bf16 v[116:119], a[96:99], a[224:227], v[116:119]\n"
        "s_waitcnt lgkmcnt(7)\n"
        "v_fmac_f32 v52, v72, v28\n"
        "v_fmac_f32 v53, v73, v29\n"
        "v_mfma_f32_16x16x32_bf16 v[120:123], a[100:103], a[224:227], v[120:123]\n"
        "v_fmac_f32 v54, v74, v30\n"
        "v_fmac_f32 v55, v75, v31\n"
        "s_waitcnt vmcnt(2)\n"
        "v_mfma_f32_16x16x32_bf16 v[104:107], a[112:115], a[252:255], v[104:107]\n"
        "s_waitcnt lgkmcnt(6)\n"
        "v_fmac_f32 v56, v76, v32\n"
        "v_fmac_f32 v57, v77, v33\n"
        "v_mfma_f32_16x16x32_bf16 v[108:111], a[116:119], a[252:255], v[108:111]\n"
        "v_fmac_f32 v58, v78, v34\n"
        "v_fmac_f32 v59, v79, v35\n"
        "v_mfma_f32_16x16x32_bf16 v[112:115], a[120:123], a[252:255], v[112:115]\n"
        "s_waitcnt lgkmcnt(5)\n"
        "v_fmac_f32 v60, v80, v36\n"
        "v_fmac_f32 v61, v81, v37\n"
        "v_mfma_f32_16x16x32_bf16 v[116:119], a[124:127], a[252:255], v[116:119]\n"
        "v_fmac_f32 v62, v82, v38\n"
        "v_fmac_f32 v63, v83, v39\n"
        "v_mfma_f32_16x16x32_bf16 v[120:123], a[216:219], a[252:255], v[120:123]\n"
        "s_waitcnt lgkmcnt(4)\n"
        "v_fmac_f32 v44, v64, v84\n"
        "s_waitcnt vmcnt(1)\n"
        "v_mfma_f32_16x16x32_bf16 v[104:107], a[228:231], a[24:27], v[104:107]\n"
        "v_fmac_f32 v45, v65, v85\n"
        "v_fmac_f32 v46, v66, v86\n"
        "v_mfma_f32_16x16x32_bf16 v[108:111], a[232:235], a[24:27], v[108:111]\n"
        "v_fmac_f32 v47, v67, v87\n"
        "s_waitcnt lgkmcnt(3)\n"
        "v_fmac_f32 v48, v68, v88\n"
        "v_mfma_f32_16x16x32_bf16 v[112:115], a[236:239], a[24:27], v[112:115]\n"
        "v_fmac_f32 v49, v69, v89\n"
        "v_fmac_f32 v50, v70, v90\n"
        "v_mfma_f32_16x16x32_bf16 v[116:119], a[240:243], a[24:27], v[116:119]\n"
        "v_fmac_f32 v51, v71, v91\n"
        "s_waitcnt lgkmcnt(2)\n"
        "v_fmac_f32 v52, v72, v92\n"
        "v_mfma_f32_16x16x32_bf16 v[120:123], a[244:247], a[24:27], v[120:123]\n"
        "v_fmac_f32 v53, v73, v93\n"
        "v_fmac_f32 v54, v74, v94\n"
        "s_waitcnt vmcnt(0)\n"
        "v_mfma_f32_16x16x32_bf16 v[104:107], a[128:131], a[28:31], v[104:107]\n"
        "v_fmac_f32 v55, v75, v95\n"
        "s_waitcnt lgkmcnt(1)\n"
        "v_fmac_f32 v56, v76, v96\n"
        "v_mfma_f32_16x16x32_bf16 v[108:111], a[132:135], a[28:31], v[108:111]\n"
        "v_fmac_f32 v57, v77, v97\n"
        "v_fmac_f32 v58, v78, v98\n"
        "v_mfma_f32_16x16x32_bf16 v[112:115], a[136:139], a[28:31], v[112:115]\n"
        "v_fmac_f32 v59, v79, v99\n"
        "s_waitcnt lgkmcnt(0)\n"
        "v_fmac_f32 v60, v80, v100\n"
        "v_mfma_f32_16x16x32_bf16 v[116:119], a[140:143], a[28:31], v[116:119]\n"
        "v_fmac_f32 v61, v81, v101\n"
        "v_fmac_f32 v62, v82, v102\n"
        "v_mfma_f32_16x16x32_bf16 v[120:123], a[144:147], a[28:31], v[120:123]\n"
        "v_fmac_f32 v63, v83, v103\n"
        "v_cmp_u_f32 vcc, v104, v104\n"
        "s_cbranch_vccnz .Ls28\n"
        ".Lb28:\n"
        "v_pk_fma_f32 v[44:45], v[64:65], v[104:105], v[44:45]\n"
        "v_pk_fma_f32 v[46:47], v[66:67], v[106:107], v[46:47]\n"
        "v_pk_fma_f32 v[48:49], v[68:69], v[108:109], v[48:49]\n"
        "v_pk_fma_f32 v[50:51], v[70:71], v[110:111], v[50:51]\n"
        "v_pk_fma_f32 v[52:53], v[72:73], v[112:113], v[52:53]\n"
        "v_pk_fma_f32 v[54:55], v[74:75], v[114:115], v[54:55]\n"
        "v_pk_fma_f32 v[56:57], v[76:77], v[116:117], v[56:57]\n"
        "v_pk_fma_f32 v[58:59], v[78:79], v[118:119], v[58:59]\n"
        "v_pk_fma_f32 v[60:61], v[80:81], v[120:121], v[60:61]\n"
        "v_pk_fma_f32 v[62:63], v[82:83], v[122:123], v[62:63]\n"
        "v_cvt_pk_bf16_f32 v20, v44, v45\n"
        "v_cvt_pk_bf16_f32 v21, v46, v47\n"
        "v_cvt_pk_bf16_f32 v22, v48, v49\n"
        "v_cvt_pk_bf16_f32 v23, v50, v51\n"
        "buffer_store_dwordx4 v[20:23], v8, s[48:51], 0 offen\n"
        "v_cvt_pk_bf16_f32 v24, v52, v53\n"
        "v_cvt_pk_bf16_f32 v25, v54, v55\n"
        "v_cvt_pk_bf16_f32 v26, v56, v57\n"
        "v_cvt_pk_bf16_f32 v27, v58, v59\n"
        "buffer_store_dwordx4 v[24:27], v8, s[48:51], 0 offen offset:16\n"
        "v_cvt_pk_bf16_f32 v28, v60, v61\n"
        "v_cvt_pk_bf16_f32 v29, v62, v63\n"
        "buffer_store_dwordx2 v[28:29], v8, s[48:51], 0 offen offset:32\n"
        "s_branch .Lend\n"
        ".Lhot:\n"
        ".Ls15:\n"
        "s_waitcnt vmcnt(0) lgkmcnt(0)\n"
        "s_nop 7\n"
        "s_nop 7\n"
        ".Lp2:\n"
        "s_mov_b32 s89, 8192\n"
        "buffer_load_dwordx4 v[136:139], v2, s[24:27], s89 offen sc1\n"
        "buffer_load_dwordx4 v[140:143], v2, s[24:27], s89 offen offset:1024 sc1\n"
        "s_waitcnt vmcnt(0)\n"
        "v_cmp_ne_u64_e64 s[66:67], v[136:137], v[6:7]\n"
        "v_cmp_ne_u64_e64 s[68:69], v[138:139], v[6:7]\n"
        "v_cmp_ne_u64_e64 s[70:71], v[140:141], v[6:7]\n"
        "v_cmp_ne_u64_e64 s[72:73], v[142:143], v[6:7]\n"
        "s_or_b64 s[66:67], s[66:67], s[68:69]\n"
        "s_or_b64 s[66:67], s[66:67], s[70:71]\n"
        "s_or_b64 s[66:67], s[66:67], s[72:73]\n"
        "s_cmp_eq_u64 s[66:67], 0\n"
        "s_cbranch_scc1 .Lq2\n"
        "s_sub_u32 s55, s55, 1\n"
        "s_cbranch_scc0 .Lp2\n"
        "s_mov_b32 s55, 0\n"
        ".Lq2:\n"
        "s_waitcnt vmcnt(0) lgkmcnt(0)\n"
        "s_nop 7\n"
        "s_nop 7\n"
        "s_branch .Lb15\n"
        ".Ls16:\n"
        "s_waitcnt lgkmcnt(0)\n"
        "s_nop 7\n"
        "s_nop 7\n"
        ".Lp3:\n"
        "buffer_load_dwordx4 v[56:59], v2, s[24:27], s4 offen sc1\n"
        "buffer_load_dwordx4 v[60:63], v2, s[24:27], s4 offen offset:1024 sc1\n"
        "buffer_load_dwordx4 v[64:67], v2, s[24:27], s4 offen offset:2048 sc1\n"
        "buffer_load_dwordx4 v[68:71], v2, s[24:27], s4 offen offset:3072 sc1\n"
        "buffer_load_dwordx4 v[72:75], v2, s[24:27], s8 offen sc1\n"
        "buffer_load_dwordx4 v[76:79], v2, s[24:27], s5 offen sc1\n"
        "buffer_load_dwordx4 v[80:83], v2, s[24:27], s5 offen offset:1024 sc1\n"
        "buffer_load_dwordx4 v[84:87], v2, s[24:27], s5 offen offset:2048 sc1\n"
        "buffer_load_dwordx4 v[88:91], v2, s[24:27], s5 offen offset:3072 sc1\n"
        "buffer_load_dwordx4 v[92:95], v2, s[24:27], s9 offen sc1\n"
        "buffer_load_dwordx4 v[96:99], v2, s[24:27], s6 offen sc1\n"
        "buffer_load_dwordx4 v[100:103], v2, s[24:27], s6 offen offset:1024 sc1\n"
        "buffer_load_dwordx4 v[104:107], v2, s[24:27], s6 offen offset:2048 sc1\n"
        "buffer_load_dwordx4 v[108:111], v2, s[24:27], s6 offen offset:3072 sc1\n"
        "buffer_load_dwordx4 v[112:115], v2, s[24:27], s10 offen sc1\n"
        "buffer_load_dwordx4 v[116:119], v2, s[24:27], s7 offen sc1\n"
        "buffer_load_dwordx4 v[120:123], v2, s[24:27], s7 offen offset:1024 sc1\n"
        "buffer_load_dwordx4 v[124:127], v2, s[24:27], s7 offen offset:2048 sc1\n"
        "buffer_load_dwordx4 v[128:131], v2, s[24:27], s7 offen offset:3072 sc1\n"
        "buffer_load_dwordx4 v[132:135], v2, s[24:27], s11 offen sc1\n"
        "s_waitcnt vmcnt(0)\n"
        ".Lc3:\n"
        "v_cmpx_ne_u32 vcc, s47, v56\n"
        "v_cmpx_ne_u32 vcc, s47, v57\n"
        "v_cmpx_ne_u32 vcc, s47, v58\n"
        "v_cmpx_ne_u32 vcc, s47, v59\n"
        "v_cmpx_ne_u32 vcc, s47, v60\n"
        "v_cmpx_ne_u32 vcc, s47, v61\n"
        "v_cmpx_ne_u32 vcc, s47, v62\n"
        "v_cmpx_ne_u32 vcc, s47, v63\n"
        "v_cmpx_ne_u32 vcc, s47, v64\n"
        "v_cmpx_ne_u32 vcc, s47, v65\n"
        "v_cmpx_ne_u32 vcc, s47, v66\n"
        "v_cmpx_ne_u32 vcc, s47, v67\n"
        "v_cmpx_ne_u32 vcc, s47, v68\n"
        "v_cmpx_ne_u32 vcc, s47, v69\n"
        "v_cmpx_ne_u32 vcc, s47, v70\n"
        "v_cmpx_ne_u32 vcc, s47, v71\n"
        "v_cmpx_ne_u32 vcc, s47, v72\n"
        "v_cmpx_ne_u32 vcc, s47, v73\n"
        "v_cmpx_ne_u32 vcc, s47, v74\n"
        "v_cmpx_ne_u32 vcc, s47, v75\n"
        "s_nop 4\n"
        "s_cmp_eq_u64 exec, -1\n"
        "s_mov_b64 exec, -1\n"
        "s_cbranch_scc1 .Lq3\n"
        "s_sub_u32 s55, s55, 1\n"
        "s_cbranch_scc0 .Lp3\n"
        "s_mov_b32 s55, 0\n"
        ".Lq3:\n"
        "v_mfma_f32_16x16x32_bf16 v[136:139], a[192:195], v[56:59], 0\n"
        "v_mfma_f32_16x16x32_bf16 v[140:143], a[196:199], v[56:59], 0\n"
        "v_mfma_f32_16x16x32_bf16 v[144:147], a[200:203], v[56:59], 0\n"
        "v_mfma_f32_16x16x32_bf16 v[148:151], a[204:207], v[56:59], 0\n"
        "v_mfma_f32_16x16x32_bf16 v[152:155], a[208:211], v[56:59], 0\n"
        "v_mfma_f32_16x16x32_bf16 v[136:139], a[212:215], v[60:63], v[136:139]\n"
        "v_mfma_f32_16x16x32_bf16 v[140:143], a[128:131], v[60:63], v[140:143]\n"
        "v_mfma_f32_16x16x32_bf16 v[144:147], a[132:135], v[60:63], v[144:147]\n"
        "v_mfma_f32_16x16x32_bf16 v[148:151], a[136:139], v[60:63], v[148:151]\n"
        "v_mfma_f32_16x16x32_bf16 v[152:155], a[140:143], v[60:63], v[152:155]\n"
        "v_mfma_f32_16x16x32_bf16 v[136:139], a[144:147], v[64:67], v[136:139]\n"
        "v_mfma_f32_16x16x32_bf16 v[140:143], a[148:151], v[64:67], v[140:143]\n"
        "v_mfma_f32_16x16x32_bf16 v[144:147], a[152:155], v[64:67], v[144:147]\n"
        "v_mfma_f32_16x16x32_bf16 v[148:151], a[156:159], v[64:67], v[148:151]\n"
        "v_mfma_f32_16x16x32_bf16 v[152:155], a[160:163], v[64:67], v[152:155]\n"
        "v_mfma_f32_16x16x32_bf16 v[136:139], a[164:167], v[68:71], v[136:139]\n"
        "v_mfma_f32_16x16x32_bf16 v[140:143], a[168:171], v[68:71], v[140:143]\n"
        "v_mfma_f32_16x16x32_bf16 v[144:147], a[172:175], v[68:71], v[144:147]\n"
        "v_mfma_f32_16x16x32_bf16 v[148:151], a[176:179], v[68:71], v[148:151]\n"
        "v_mfma_f32_16x16x32_bf16 v[152:155], a[180:183], v[68:71], v[152:155]\n"
        "v_mfma_f32_16x16x32_bf16 v[136:139], a[184:187], v[72:75], v[136:139]\n"
        "v_mfma_f32_16x16x32_bf16 v[140:143], a[188:191], v[72:75], v[140:143]\n"
        "v_mfma_f32_16x16x32_bf16 v[144:147], a[0:3], v[72:75], v[144:147]\n"
        "v_mfma_f32_16x16x32_bf16 v[148:151], a[4:7], v[72:75], v[148:151]\n"
        "v_mfma_f32_16x16x32_bf16 v[152:155], a[8:11], v[72:75], v[152:155]\n"
        "s_waitcnt lgkmcnt(0)\n"
        "s_nop 7\n"
        "s_nop 7\n"
        "s_branch .Lb16\n"
        ".Ls17:\n"
        "s_waitcnt lgkmcnt(0)\n"
        "s_nop 7\n"
        "s_nop 7\n"
        "s_branch .Lc4\n"
        ".Lp4:\n"
        "buffer_load_dwordx4 v[76:79], v2, s[24:27], s5 offen sc1\n"
        "buffer_load_dwordx4 v[80:83], v2, s[24:27], s5 offen offset:1024 sc1\n"
        "buffer_load_dwordx4 v[84:87], v2, s[24:27], s5 offen offset:2048 sc1\n"
        "buffer_load_dwordx4 v[88:91], v2, s[24:27], s5 offen offset:3072 sc1\n"
        "buffer_load_dwordx4 v[92:95], v2, s[24:27], s9 offen sc1\n"
        "buffer_load_dwordx4 v[96:99], v2, s[24:27], s6 offen sc1\n"
        "buffer_load_dwordx4 v[100:103], v2, s[24:27], s6 offen offset:1024 sc1\n"
        "buffer_load_dwordx4 v[104:107], v2, s[24:27], s6 offen offset:2048 sc1\n"
        "buffer_load_dwordx4 v[108:111], v2, s[24:27], s6 offen offset:3072 sc1\n"
        "buffer_load_dwordx4 v[112:115], v2, s[24:27], s10 offen sc1\n"
        "buffer_load_dwordx4 v[116:119], v2, s[24:27], s7 offen sc1\n"
        "buffer_load_dwordx4 v[120:123], v2, s[24:27], s7 offen offset:1024 sc1\n"
        "buffer_load_dwordx4 v[124:127], v2, s[24:27], s7 offen offset:2048 sc1\n"
        "buffer_load_dwordx4 v[128:131], v2, s[24:27], s7 offen offset:3072 sc1\n"
        "buffer_load_dwordx4 v[132:135], v2, s[24:27], s11 offen sc1\n"
        "s_waitcnt vmcnt(0)\n"
        ".Lc4:\n"
        "v_cmpx_ne_u32 vcc, s47, v76\n"
        "v_cmpx_ne_u32 vcc, s47, v77\n"
        "v_cmpx_ne_u32 vcc, s47, v78\n"
        "v_cmpx_ne_u32 vcc, s47, v79\n"
        "v_cmpx_ne_u32 vcc, s47, v80\n"
        "v_cmpx_ne_u32 vcc, s47, v81\n"
        "v_cmpx_ne_u32 vcc, s47, v82\n"
        "v_cmpx_ne_u32 vcc, s47, v83\n"
        "v_cmpx_ne_u32 vcc, s47, v84\n"
        "v_cmpx_ne_u32 vcc, s47, v85\n"
        "v_cmpx_ne_u32 vcc, s47, v86\n"
        "v_cmpx_ne_u32 vcc, s47, v87\n"
        "v_cmpx_ne_u32 vcc, s47, v88\n"
        "v_cmpx_ne_u32 vcc, s47, v89\n"
        "v_cmpx_ne_u32 vcc, s47, v90\n"
        "v_cmpx_ne_u32 vcc, s47, v91\n"
        "v_cmpx_ne_u32 vcc, s47, v92\n"
        "v_cmpx_ne_u32 vcc, s47, v93\n"
        "v_cmpx_ne_u32 vcc, s47, v94\n"
        "v_cmpx_ne_u32 vcc, s47, v95\n"
        "s_nop 4\n"
        "s_cmp_eq_u64 exec, -1\n"
        "s_mov_b64 exec, -1\n"
        "s_cbranch_scc1 .Lq4\n"
        "s_sub_u32 s55, s55, 1\n"
        "s_cbranch_scc0 .Lp4\n"
        "s_mov_b32 s55, 0\n"
        ".Lq4:\n"
        "v_mfma_f32_16x16x32_bf16 v[156:159], a[192:195], v[76:79], 0\n"
        "v_mfma_f32_16x16x32_bf16 v[160:163], a[196:199], v[76:79], 0\n"
        "v_mfma_f32_16x16x32_bf16 v[164:167], a[200:203], v[76:79], 0\n"
        "v_mfma_f32_16x16x32_bf16 v[168:171], a[204:207], v[76:79], 0\n"
        "v_mfma_f32_16x16x32_bf16 v[172:175], a[208:211], v[76:79], 0\n"
        "v_mfma_f32_16x16x32_bf16 v[156:159], a[212:215], v[80:83], v[156:159]\n"
        "v_mfma_f32_16x16x32_bf16 v[160:163], a[128:131], v[80:83], v[160:163]\n"
        "v_mfma_f32_16x16x32_bf16 v[164:167], a[132:135], v[80:83], v[164:167]\n"
        "v_mfma_f32_16x16x32_bf16 v[168:171], a[136:139], v[80:83], v[168:171]\n"
        "v_mfma_f32_16x16x32_bf16 v[172:175], a[140:143], v[80:83], v[172:175]\n"
        "v_mfma_f32_16x16x32_bf16 v[156:159], a[144:147], v[84:87], v[156:159]\n"
        "v_mfma_f32_16x16x32_bf16 v[160:163], a[148:151], v[84:87], v[160:163]\n"
        "v_mfma_f32_16x16x32_bf16 v[164:167], a[152:155], v[84:87], v[164:167]\n"
        "v_mfma_f32_16x16x32_bf16 v[168:171], a[156:159], v[84:87], v[168:171]\n"
        "v_mfma_f32_16x16x32_bf16 v[172:175], a[160:163], v[84:87], v[172:175]\n"
        "v_mfma_f32_16x16x32_bf16 v[156:159], a[164:167], v[88:91], v[156:159]\n"
        "v_mfma_f32_16x16x32_bf16 v[160:163], a[168:171], v[88:91], v[160:163]\n"
        "v_mfma_f32_16x16x32_bf16 v[164:167], a[172:175], v[88:91], v[164:167]\n"
        "v_mfma_f32_16x16x32_bf16 v[168:171], a[176:179], v[88:91], v[168:171]\n"
        "v_mfma_f32_16x16x32_bf16 v[172:175], a[180:183], v[88:91], v[172:175]\n"
        "v_mfma_f32_16x16x32_bf16 v[156:159], a[184:187], v[92:95], v[156:159]\n"
        "v_mfma_f32_16x16x32_bf16 v[160:163], a[188:191], v[92:95], v[160:163]\n"
        "v_mfma_f32_16x16x32_bf16 v[164:167], a[0:3], v[92:95], v[164:167]\n"
        "v_mfma_f32_16x16x32_bf16 v[168:171], a[4:7], v[92:95], v[168:171]\n"
        "v_mfma_f32_16x16x32_bf16 v[172:175], a[8:11], v[92:95], v[172:175]\n"
        "s_waitcnt lgkmcnt(0)\n"
        "s_nop 7\n"
        "s_nop 7\n"
        "s_branch .Lb17\n"
        ".Ls18:\n"
        "s_waitcnt lgkmcnt(0)\n"
        "s_nop 7\n"
        "s_nop 7\n"
        "s_branch .Lc5\n"
        ".Lp5:\n"
        "buffer_load_dwordx4 v[96:99], v2, s[24:27], s6 offen sc1\n"
        "buffer_load_dwordx4 v[100:103], v2, s[24:27], s6 offen offset:1024 sc1\n"
        "buffer_load_dwordx4 v[104:107], v2, s[24:27], s6 offen offset:2048 sc1\n"
        "buffer_load_dwordx4 v[108:111], v2, s[24:27], s6 offen offset:3072 sc1\n"
        "buffer_load_dwordx4 v[112:115], v2, s[24:27], s10 offen sc1\n"
        "buffer_load_dwordx4 v[116:119], v2, s[24:27], s7 offen sc1\n"
        "buffer_load_dwordx4 v[120:123], v2, s[24:27], s7 offen offset:1024 sc1\n"
        "buffer_load_dwordx4 v[124:127], v2, s[24:27], s7 offen offset:2048 sc1\n"
        "buffer_load_dwordx4 v[128:131], v2, s[24:27], s7 offen offset:3072 sc1\n"
        "buffer_load_dwordx4 v[132:135], v2, s[24:27], s11 offen sc1\n"
        "s_waitcnt vmcnt(0)\n"
        ".Lc5:\n"
        "v_cmpx_ne_u32 vcc, s47, v96\n"
        "v_cmpx_ne_u32 vcc, s47, v97\n"
        "v_cmpx_ne_u32 vcc, s47, v98\n"
        "v_cmpx_ne_u32 vcc, s47, v99\n"
        "v_cmpx_ne_u32 vcc, s47, v100\n"
        "v_cmpx_ne_u32 vcc, s47, v101\n"
        "v_cmpx_ne_u32 vcc, s47, v102\n"
        "v_cmpx_ne_u32 vcc, s47, v103\n"
        "v_cmpx_ne_u32 vcc, s47, v104\n"
        "v_cmpx_ne_u32 vcc, s47, v105\n"
        "v_cmpx_ne_u32 vcc, s47, v106\n"
        "v_cmpx_ne_u32 vcc, s47, v107\n"
        "v_cmpx_ne_u32 vcc, s47, v108\n"
        "v_cmpx_ne_u32 vcc, s47, v109\n"
        "v_cmpx_ne_u32 vcc, s47, v110\n"
        "v_cmpx_ne_u32 vcc, s47, v111\n"
        "v_cmpx_ne_u32 vcc, s47, v112\n"
        "v_cmpx_ne_u32 vcc, s47, v113\n"
        "v_cmpx_ne_u32 vcc, s47, v114\n"
        "v_cmpx_ne_u32 vcc, s47, v115\n"
        "s_nop 4\n"
        "s_cmp_eq_u64 exec, -1\n"
        "s_mov_b64 exec, -1\n"
        "s_cbranch_scc1 .Lq5\n"
        "s_sub_u32 s55, s55, 1\n"
        "s_cbranch_scc0 .Lp5\n"
        "s_mov_b32 s55, 0\n"
        ".Lq5:\n"
        "v_mfma_f32_16x16x32_bf16 v[136:139], a[192:195], v[96:99], 0\n"
        "v_mfma_f32_16x16x32_bf16 v[140:143], a[196:199], v[96:99], 0\n"
        "v_mfma_f32_16x16x32_bf16 v[144:147], a[200:203], v[96:99], 0\n"
        "v_mfma_f32_16x16x32_bf16 v[148:151], a[204:207], v[96:99], 0\n"
        "v_mfma_f32_16x16x32_bf16 v[152:155], a[208:211], v[96:99], 0\n"
        "v_mfma_f32_16x16x32_bf16 v[136:139], a[212:215], v[100:103], v[136:139]\n"
        "v_mfma_f32_16x16x32_bf16 v[140:143], a[128:131], v[100:103], v[140:143]\n"
        "v_mfma_f32_16x16x32_bf16 v[144:147], a[132:135], v[100:103], v[144:147]\n"
        "v_mfma_f32_16x16x32_bf16 v[148:151], a[136:139], v[100:103], v[148:151]\n"
        "v_mfma_f32_16x16x32_bf16 v[152:155], a[140:143], v[100:103], v[152:155]\n"
        "v_mfma_f32_16x16x32_bf16 v[136:139], a[144:147], v[104:107], v[136:139]\n"
        "v_mfma_f32_16x16x32_bf16 v[140:143], a[148:151], v[104:107], v[140:143]\n"
        "v_mfma_f32_16x16x32_bf16 v[144:147], a[152:155], v[104:107], v[144:147]\n"
        "v_mfma_f32_16x16x32_bf16 v[148:151], a[156:159], v[104:107], v[148:151]\n"
        "v_mfma_f32_16x16x32_bf16 v[152:155], a[160:163], v[104:107], v[152:155]\n"
        "v_mfma_f32_16x16x32_bf16 v[136:139], a[164:167], v[108:111], v[136:139]\n"
        "v_mfma_f32_16x16x32_bf16 v[140:143], a[168:171], v[108:111], v[140:143]\n"
        "v_mfma_f32_16x16x32_bf16 v[144:147], a[172:175], v[108:111], v[144:147]\n"
        "v_mfma_f32_16x16x32_bf16 v[148:151], a[176:179], v[108:111], v[148:151]\n"
        "v_mfma_f32_16x16x32_bf16 v[152:155], a[180:183], v[108:111], v[152:155]\n"
        "v_mfma_f32_16x16x32_bf16 v[136:139], a[184:187], v[112:115], v[136:139]\n"
        "v_mfma_f32_16x16x32_bf16 v[140:143], a[188:191], v[112:115], v[140:143]\n"
        "v_mfma_f32_16x16x32_bf16 v[144:147], a[0:3], v[112:115], v[144:147]\n"
        "v_mfma_f32_16x16x32_bf16 v[148:151], a[4:7], v[112:115], v[148:151]\n"
        "v_mfma_f32_16x16x32_bf16 v[152:155], a[8:11], v[112:115], v[152:155]\n"
        "s_waitcnt lgkmcnt(0)\n"
        "s_nop 7\n"
        "s_nop 7\n"
        "s_branch .Lb18\n"
        ".Ls19:\n"
        "s_waitcnt lgkmcnt(0)\n"
        "s_nop 7\n"
        "s_nop 7\n"
        "s_branch .Lc6\n"
        ".Lp6:\n"
        "buffer_load_dwordx4 v[116:119], v2, s[24:27], s7 offen sc1\n"
        "buffer_load_dwordx4 v[120:123], v2, s[24:27], s7 offen offset:1024 sc1\n"
        "buffer_load_dwordx4 v[124:127], v2, s[24:27], s7 offen offset:2048 sc1\n"
        "buffer_load_dwordx4 v[128:131], v2, s[24:27], s7 offen offset:3072 sc1\n"
        "buffer_load_dwordx4 v[132:135], v2, s[24:27], s11 offen sc1\n"
        "s_waitcnt vmcnt(0)\n"
        ".Lc6:\n"
        "v_cmpx_ne_u32 vcc, s47, v116\n"
        "v_cmpx_ne_u32 vcc, s47, v117\n"
        "v_cmpx_ne_u32 vcc, s47, v118\n"
        "v_cmpx_ne_u32 vcc, s47, v119\n"
        "v_cmpx_ne_u32 vcc, s47, v120\n"
        "v_cmpx_ne_u32 vcc, s47, v121\n"
        "v_cmpx_ne_u32 vcc, s47, v122\n"
        "v_cmpx_ne_u32 vcc, s47, v123\n"
        "v_cmpx_ne_u32 vcc, s47, v124\n"
        "v_cmpx_ne_u32 vcc, s47, v125\n"
        "v_cmpx_ne_u32 vcc, s47, v126\n"
        "v_cmpx_ne_u32 vcc, s47, v127\n"
        "v_cmpx_ne_u32 vcc, s47, v128\n"
        "v_cmpx_ne_u32 vcc, s47, v129\n"
        "v_cmpx_ne_u32 vcc, s47, v130\n"
        "v_cmpx_ne_u32 vcc, s47, v131\n"
        "v_cmpx_ne_u32 vcc, s47, v132\n"
        "v_cmpx_ne_u32 vcc, s47, v133\n"
        "v_cmpx_ne_u32 vcc, s47, v134\n"
        "v_cmpx_ne_u32 vcc, s47, v135\n"
        "s_nop 4\n"
        "s_cmp_eq_u64 exec, -1\n"
        "s_mov_b64 exec, -1\n"
        "s_cbranch_scc1 .Lq6\n"
        "s_sub_u32 s55, s55, 1\n"
        "s_cbranch_scc0 .Lp6\n"
        "s_mov_b32 s55, 0\n"
        ".Lq6:\n"
        "v_mfma_f32_16x16x32_bf16 v[156:159], a[192:195], v[116:119], 0\n"
        "v_mfma_f32_16x16x32_bf16 v[160:163], a[196:199], v[116:119], 0\n"
        "v_mfma_f32_16x16x32_bf16 v[164:167], a[200:203], v[116:119], 0\n"
        "v_mfma_f32_16x16x32_bf16 v[168:171], a[204:207], v[116:119], 0\n"
        "v_mfma_f32_16x16x32_bf16 v[172:175], a[208:211], v[116:119], 0\n"
        "v_mfma_f32_16x16x32_bf16 v[156:159], a[212:215], v[120:123], v[156:159]\n"
        "v_mfma_f32_16x16x32_bf16 v[160:163], a[128:131], v[120:123], v[160:163]\n"
        "v_mfma_f32_16x16x32_bf16 v[164:167], a[132:135], v[120:123], v[164:167]\n"
        "v_mfma_f32_16x16x32_bf16 v[168:171], a[136:139], v[120:123], v[168:171]\n"
        "v_mfma_f32_16x16x32_bf16 v[172:175], a[140:143], v[120:123], v[172:175]\n"
        "v_mfma_f32_16x16x32_bf16 v[156:159], a[144:147], v[124:127], v[156:159]\n"
        "v_mfma_f32_16x16x32_bf16 v[160:163], a[148:151], v[124:127], v[160:163]\n"
        "v_mfma_f32_16x16x32_bf16 v[164:167], a[152:155], v[124:127], v[164:167]\n"
        "v_mfma_f32_16x16x32_bf16 v[168:171], a[156:159], v[124:127], v[168:171]\n"
        "v_mfma_f32_16x16x32_bf16 v[172:175], a[160:163], v[124:127], v[172:175]\n"
        "v_mfma_f32_16x16x32_bf16 v[156:159], a[164:167], v[128:131], v[156:159]\n"
        "v_mfma_f32_16x16x32_bf16 v[160:163], a[168:171], v[128:131], v[160:163]\n"
        "v_mfma_f32_16x16x32_bf16 v[164:167], a[172:175], v[128:131], v[164:167]\n"
        "v_mfma_f32_16x16x32_bf16 v[168:171], a[176:179], v[128:131], v[168:171]\n"
        "v_mfma_f32_16x16x32_bf16 v[172:175], a[180:183], v[128:131], v[172:175]\n"
        "v_mfma_f32_16x16x32_bf16 v[156:159], a[184:187], v[132:135], v[156:159]\n"
        "v_mfma_f32_16x16x32_bf16 v[160:163], a[188:191], v[132:135], v[160:163]\n"
        "v_mfma_f32_16x16x32_bf16 v[164:167], a[0:3], v[132:135], v[164:167]\n"
        "v_mfma_f32_16x16x32_bf16 v[168:171], a[4:7], v[132:135], v[168:171]\n"
        "v_mfma_f32_16x16x32_bf16 v[172:175], a[8:11], v[132:135], v[172:175]\n"
        "s_waitcnt lgkmcnt(0)\n"
        "s_nop 7\n"
        "s_nop 7\n"
        "s_branch .Lb19\n"
        ".Ls20:\n"
        "s_waitcnt lgkmcnt(0)\n"
        "s_nop 7\n"
        "s_nop 7\n"
        ".Lp7:\n"
        "buffer_load_dwordx4 v[136:139], v2, s[24:27], s4 offen sc1\n"
        "buffer_load_dwordx4 v[140:143], v2, s[24:27], s4 offen offset:1024 sc1\n"
        "buffer_load_dwordx4 v[144:147], v2, s[24:27], s4 offen offset:2048 sc1\n"
        "buffer_load_dwordx4 v[148:151], v2, s[24:27], s4 offen offset:3072 sc1\n"
        "buffer_load_dwordx4 v[152:155], v2, s[24:27], s8 offen sc1\n"
        "buffer_load_dwordx4 v[156:159], v2, s[24:27], s5 offen sc1\n"
        "buffer_load_dwordx4 v[160:163], v2, s[24:27], s5 offen offset:1024 sc1\n"
        "buffer_load_dwordx4 v[164:167], v2, s[24:27], s5 offen offset:2048 sc1\n"
        "buffer_load_dwordx4 v[168:171], v2, s[24:27], s5 offen offset:3072 sc1\n"
        "buffer_load_dwordx4 v[172:175], v2, s[24:27], s9 offen sc1\n"
        "buffer_load_dwordx4 v[216:219], v2, s[24:27], s72 offen sc1\n"
        "buffer_load_dwordx4 v[220:223], v2, s[24:27], s72 offen offset:1024 sc1\n"
        "buffer_load_dwordx4 v[224:227], v2, s[24:27], s72 offen offset:2048 sc1\n"
        "buffer_load_dwordx4 v[228:231], v2, s[24:27], s72 offen offset:3072 sc1\n"
        "s_waitcnt vmcnt(0)\n"
        ".Lc7:\n"
        "v_cmpx_ne_u32 vcc, s47, v136\n"
        "v_cmpx_ne_u32 vcc, s47, v137\n"
        "v_cmpx_ne_u32 vcc, s47, v138\n"
        "v_cmpx_ne_u32 vcc, s47, v139\n"
        "v_cmpx_ne_u32 vcc, s47, v140\n"
        "v_cmpx_ne_u32 vcc, s47, v141\n"
        "v_cmpx_ne_u32 vcc, s47, v142\n"
        "v_cmpx_ne_u32 vcc, s47, v143\n"
        "v_cmpx_ne_u32 vcc, s47, v144\n"
        "v_cmpx_ne_u32 vcc, s47, v145\n"
        "v_cmpx_ne_u32 vcc, s47, v146\n"
        "v_cmpx_ne_u32 vcc, s47, v147\n"
        "v_cmpx_ne_u32 vcc, s47, v148\n"
        "v_cmpx_ne_u32 vcc, s47, v149\n"
        "v_cmpx_ne_u32 vcc, s47, v150\n"
        "v_cmpx_ne_u32 vcc, s47, v151\n"
        "v_cmpx_ne_u32 vcc, s47, v152\n"
        "v_cmpx_ne_u32 vcc, s47, v153\n"
        "v_cmpx_ne_u32 vcc, s47, v154\n"
        "v_cmpx_ne_u32 vcc, s47, v155\n"
        "s_nop 4\n"
        "s_cmp_eq_u64 exec, -1\n"
        "s_mov_b64 exec, -1\n"
        "s_cbranch_scc1 .Lq7\n"
        "s_sub_u32 s55, s55, 1\n"
        "s_cbranch_scc0 .Lp7\n"
        "s_mov_b32 s55, 0\n"
        ".Lq7:\n"
        "s_waitcnt lgkmcnt(0)\n"
        "s_nop 7\n"
        "s_nop 7\n"
        "s_branch .Lb20\n"
        ".Ls21:\n"
        "s_waitcnt lgkmcnt(0)\n"
        "s_nop 7\n"
        "s_nop 7\n"
        ".Lp8:\n"
        "buffer_load_dwordx4 v[156:159], v2, s[24:27], s5 offen sc1\n"
        "buffer_load_dwordx4 v[160:163], v2, s[24:27], s5 offen offset:1024 sc1\n"
        "buffer_load_dwordx4 v[164:167], v2, s[24:27], s5 offen offset:2048 sc1\n"
        "buffer_load_dwordx4 v[168:171], v2, s[24:27], s5 offen offset:3072 sc1\n"
        "buffer_load_dwordx4 v[172:175], v2, s[24:27], s9 offen sc1\n"
        "buffer_load_dwordx4 v[216:219], v2, s[24:27], s72 offen sc1\n"
        "buffer_load_dwordx4 v[220:223], v2, s[24:27], s72 offen offset:1024 sc1\n"
        "buffer_load_dwordx4 v[224:227], v2, s[24:27], s72 offen offset:2048 sc1\n"
        "buffer_load_dwordx4 v[228:231], v2, s[24:27], s72 offen offset:3072 sc1\n"
        "s_waitcnt vmcnt(0)\n"
        ".Lc8:\n"
        "v_cmpx_ne_u32 vcc, s47, v156\n"
        "v_cmpx_ne_u32 vcc, s47, v157\n"
        "v_cmpx_ne_u32 vcc, s47, v158\n"
        "v_cmpx_ne_u32 vcc, s47, v159\n"
        "v_cmpx_ne_u32 vcc, s47, v160\n"
        "v_cmpx_ne_u32 vcc, s47, v161\n"
        "v_cmpx_ne_u32 vcc, s47, v162\n"
        "v_cmpx_ne_u32 vcc, s47, v163\n"
        "v_cmpx_ne_u32 vcc, s47, v164\n"
        "v_cmpx_ne_u32 vcc, s47, v165\n"
        "v_cmpx_ne_u32 vcc, s47, v166\n"
        "v_cmpx_ne_u32 vcc, s47, v167\n"
        "v_cmpx_ne_u32 vcc, s47, v168\n"
        "v_cmpx_ne_u32 vcc, s47, v169\n"
        "v_cmpx_ne_u32 vcc, s47, v170\n"
        "v_cmpx_ne_u32 vcc, s47, v171\n"
        "v_cmpx_ne_u32 vcc, s47, v172\n"
        "v_cmpx_ne_u32 vcc, s47, v173\n"
        "v_cmpx_ne_u32 vcc, s47, v174\n"
        "v_cmpx_ne_u32 vcc, s47, v175\n"
        "s_nop 4\n"
        "s_cmp_eq_u64 exec, -1\n"
        "s_mov_b64 exec, -1\n"
        "s_cbranch_scc1 .Lq8\n"
        "s_sub_u32 s55, s55, 1\n"
        "s_cbranch_scc0 .Lp8\n"
        "s_mov_b32 s55, 0\n"
        ".Lq8:\n"
        "s_waitcnt lgkmcnt(0)\n"
        "s_nop 7\n"
        "s_nop 7\n"
        "s_branch .Lb21\n"
        ".Ls22:\n"
        "s_waitcnt lgkmcnt(0)\n"
        "s_nop 7\n"
        "s_nop 7\n"
        ".Lp9:\n"
        "buffer_load_dwordx4 v[216:219], v2, s[24:27], s72 offen sc1\n"
        "buffer_load_dwordx4 v[220:223], v2, s[24:27], s72 offen offset:1024 sc1\n"
        "buffer_load_dwordx4 v[224:227], v2, s[24:27], s72 offen offset:2048 sc1\n"
        "buffer_load_dwordx4 v[228:231], v2, s[24:27], s72 offen offset:3072 sc1\n"
        "s_waitcnt vmcnt(0)\n"
        ".Lc9:\n"
        "v_cmpx_ne_u32 vcc, s47, v216\n"
        "v_cmpx_ne_u32 vcc, s47, v217\n"
        "v_cmpx_ne_u32 vcc, s47, v218\n"
        "v_cmpx_ne_u32 vcc, s47, v219\n"
        "v_cmpx_ne_u32 vcc, s47, v220\n"
        "v_cmpx_ne_u32 vcc, s47, v221\n"
        "v_cmpx_ne_u32 vcc, s47, v222\n"
        "v_cmpx_ne_u32 vcc, s47, v223\n"
        "v_cmpx_ne_u32 vcc, s47, v224\n"
        "v_cmpx_ne_u32 vcc, s47, v225\n"
        "v_cmpx_ne_u32 vcc, s47, v226\n"
        "v_cmpx_ne_u32 vcc, s47, v227\n"
        "v_cmpx_ne_u32 vcc, s47, v228\n"
        "v_cmpx_ne_u32 vcc, s47, v229\n"
        "v_cmpx_ne_u32 vcc, s47, v230\n"
        "v_cmpx_ne_u32 vcc, s47, v231\n"
        "s_nop 4\n"
        "s_cmp_eq_u64 exec, -1\n"
        "s_mov_b64 exec, -1\n"
        "s_cbranch_scc1 .Lq9\n"
        "s_sub_u32 s55, s55, 1\n"
        "s_cbranch_scc0 .Lp9\n"
        "s_mov_b32 s55, 0\n"
        ".Lq9:\n"
        "v_add_f32 v216, v216, v218\n"
        "v_add_f32 v217, v217, v219\n"
        "v_add_f32 v220, v220, v222\n"
        "v_add_f32 v221, v221, v223\n"
        "v_add_f32 v224, v224, v226\n"
        "v_add_f32 v225, v225, v227\n"
        "v_add_f32 v228, v228, v230\n"
        "v_add_f32 v229, v229, v231\n"
        "v_add_f32 v216, v216, v220\n"
        "v_add_f32 v217, v217, v221\n"
        "v_add_f32 v224, v224, v228\n"
        "v_add_f32 v225, v225, v229\n"
        "v_add_f32 v216, v216, v224\n"
        "v_add_f32 v217, v217, v225\n"
        "s_waitcnt lgkmcnt(0)\n"
        "s_nop 7\n"
        "s_nop 7\n"
        "s_branch .Lb22\n"
        ".Ls23:\n"
        "s_waitcnt lgkmcnt(0)\n"
        "s_nop 7\n"
        "s_nop 7\n"
        ".Lp11:\n"
        "buffer_load_dwordx4 v[176:179], v2, s[24:27], s6 offen sc1\n"
        "buffer_load_dwordx4 v[180:183], v2, s[24:27], s6 offen offset:1024 sc1\n"
        "buffer_load_dwordx4 v[184:187], v2, s[24:27], s6 offen offset:2048 sc1\n"
        "buffer_load_dwordx4 v[188:191], v2, s[24:27], s6 offen offset:3072 sc1\n"
        "buffer_load_dwordx4 v[192:195], v2, s[24:27], s10 offen sc1\n"
        "buffer_load_dwordx4 v[196:199], v2, s[24:27], s7 offen sc1\n"
        "buffer_load_dwordx4 v[200:203], v2, s[24:27], s7 offen offset:1024 sc1\n"
        "buffer_load_dwordx4 v[204:207], v2, s[24:27], s7 offen offset:2048 sc1\n"
        "buffer_load_dwordx4 v[208:211], v2, s[24:27], s7 offen offset:3072 sc1\n"
        "buffer_load_dwordx4 v[212:215], v2, s[24:27], s11 offen sc1\n"
        "s_waitcnt vmcnt(0)\n"
        ".Lc11:\n"
        "v_cmpx_ne_u32 vcc, s47, v176\n"
        "v_cmpx_ne_u32 vcc, s47, v177\n"
        "v_cmpx_ne_u32 vcc, s47, v178\n"
        "v_cmpx_ne_u32 vcc, s47, v179\n"
        "v_cmpx_ne_u32 vcc, s47, v180\n"
        "v_cmpx_ne_u32 vcc, s47, v181\n"
        "v_cmpx_ne_u32 vcc, s47, v182\n"
        "v_cmpx_ne_u32 vcc, s47, v183\n"
        "v_cmpx_ne_u32 vcc, s47, v184\n"
        "v_cmpx_ne_u32 vcc, s47, v185\n"
        "v_cmpx_ne_u32 vcc, s47, v186\n"
        "v_cmpx_ne_u32 vcc, s47, v187\n"
        "v_cmpx_ne_u32 vcc, s47, v188\n"
        "v_cmpx_ne_u32 vcc, s47, v189\n"
        "v_cmpx_ne_u32 vcc, s47, v190\n"
        "v_cmpx_ne_u32 vcc, s47, v191\n"
        "v_cmpx_ne_u32 vcc, s47, v192\n"
        "v_cmpx_ne_u32 vcc, s47, v193\n"
        "v_cmpx_ne_u32 vcc, s47, v194\n"
        "v_cmpx_ne_u32 vcc, s47, v195\n"
        "s_nop 4\n"
        "s_cmp_eq_u64 exec, -1\n"
        "s_mov_b64 exec, -1\n"
        "s_cbranch_scc1 .Lq11\n"
        "s_sub_u32 s55, s55, 1\n"
        "s_cbranch_scc0 .Lp11\n"
        "s_mov_b32 s55, 0\n"
        ".Lq11:\n"
        "s_waitcnt lgkmcnt(0)\n"
        "s_nop 7\n"
        "s_nop 7\n"
        "s_branch .Lb23\n"
        ".Ls24:\n"
        "s_waitcnt lgkmcnt(0)\n"
        "s_nop 7\n"
        "s_nop 7\n"
        ".Lp12:\n"
        "buffer_load_dwordx4 v[196:199], v2, s[24:27], s7 offen sc1\n"
        "buffer_load_dwordx4 v[200:203], v2, s[24:27], s7 offen offset:1024 sc1\n"
        "buffer_load_dwordx4 v[204:207], v2, s[24:27], s7 offen offset:2048 sc1\n"
        "buffer_load_dwordx4 v[208:211], v2, s[24:27], s7 offen offset:3072 sc1\n"
        "buffer_load_dwordx4 v[212:215], v2, s[24:27], s11 offen sc1\n"
        "s_waitcnt vmcnt(0)\n"
        ".Lc12:\n"
        "v_cmpx_ne_u32 vcc, s47, v196\n"
        "v_cmpx_ne_u32 vcc, s47, v197\n"
        "v_cmpx_ne_u32 vcc, s47, v198\n"
        "v_cmpx_ne_u32 vcc, s47, v199\n"
        "v_cmpx_ne_u32 vcc, s47, v200\n"
        "v_cmpx_ne_u32 vcc, s47, v201\n"
        "v_cmpx_ne_u32 vcc, s47, v202\n"
        "v_cmpx_ne_u32 vcc, s47, v203\n"
        "v_cmpx_ne_u32 vcc, s47, v204\n"
        "v_cmpx_ne_u32 vcc, s47, v205\n"
        "v_cmpx_ne_u32 vcc, s47, v206\n"
        "v_cmpx_ne_u32 vcc, s47, v207\n"
        "v_cmpx_ne_u32 vcc, s47, v208\n"
        "v_cmpx_ne_u32 vcc, s47, v209\n"
        "v_cmpx_ne_u32 vcc, s47, v210\n"
        "v_cmpx_ne_u32 vcc, s47, v211\n"
        "v_cmpx_ne_u32 vcc, s47, v212\n"
        "v_cmpx_ne_u32 vcc, s47, v213\n"
        "v_cmpx_ne_u32 vcc, s47, v214\n"
        "v_cmpx_ne_u32 vcc, s47, v215\n"
        "s_nop 4\n"
        "s_cmp_eq_u64 exec, -1\n"
        "s_mov_b64 exec, -1\n"
        "s_cbranch_scc1 .Lq12\n"
        "s_sub_u32 s55, s55, 1\n"
        "s_cbranch_scc0 .Lp12\n"
        "s_mov_b32 s55, 0\n"
        ".Lq12:\n"
        "s_waitcnt lgkmcnt(0)\n"
        "s_nop 7\n"
        "s_nop 7\n"
        "s_branch .Lb24\n"
        ".Ls25:\n"
        "s_waitcnt lgkmcnt(0)\n"
        "s_nop 7\n"
        "s_nop 7\n"
        ".Lp13:\n"
        "buffer_load_dwordx4 v[24:27], v2, s[24:27], s12 offen sc1\n"
        "buffer_load_dwordx4 v[28:31], v2, s[24:27], s12 offen offset:1024 sc1\n"
        "buffer_load_dwordx4 a[212:215], v2, s[24:27], s12 offen offset:2048 sc1\n"
        "buffer_load_dwordx4 a[12:15], v2, s[24:27], s12 offen offset:3072 sc1\n"
        "buffer_load_dwordx4 a[16:19], v3, s[24:27], s12 offen sc1\n"
        "buffer_load_dwordx4 a[20:23], v3, s[24:27], s12 offen offset:1024 sc1\n"
        "buffer_load_dwordx4 a[36:39], v2, s[24:27], s14 offen sc1\n"
        "buffer_load_dwordx4 a[52:55], v2, s[24:27], s14 offen offset:1024 sc1\n"
        "buffer_load_dwordx4 a[80:83], v2, s[24:27], s14 offen offset:2048 sc1\n"
        "buffer_load_dwordx4 a[108:111], v2, s[24:27], s14 offen offset:3072 sc1\n"
        "buffer_load_dwordx4 a[224:227], v3, s[24:27], s14 offen sc1\n"
        "buffer_load_dwordx4 a[252:255], v3, s[24:27], s14 offen offset:1024 sc1\n"
        "buffer_load_dwordx4 a[24:27], v3, s[24:27], s12 offen offset:2048 sc1\n"
        "buffer_load_dwordx4 a[28:31], v3, s[24:27], s12 offen offset:3072 sc1\n"
        "buffer_load_dwordx4 a[32:35], v4, s[24:27], s12 offen sc1\n"
        "buffer_load_dwordx4 a[48:51], v4, s[24:27], s12 offen offset:1024 sc1\n"
        "buffer_load_dwordx4 a[76:79], v4, s[24:27], s12 offen offset:2048 sc1\n"
        "buffer_load_dwordx4 a[104:107], v4, s[24:27], s12 offen offset:3072 sc1\n"
        "buffer_load_dwordx4 a[220:223], v3, s[24:27], s14 offen offset:2048 sc1\n"
        "buffer_load_dwordx4 a[248:251], v3, s[24:27], s14 offen offset:3072 sc1\n"
        "buffer_load_dwordx4 a[148:151], v4, s[24:27], s14 offen sc1\n"
        "buffer_load_dwordx4 a[152:155], v4, s[24:27], s14 offen offset:1024 sc1\n"
        "buffer_load_dwordx4 a[156:159], v4, s[24:27], s14 offen offset:2048 sc1\n"
        "buffer_load_dwordx4 a[160:163], v4, s[24:27], s14 offen offset:3072 sc1\n"
        "s_waitcnt vmcnt(0)\n"
        ".Lc13:\n"
        "v_cmpx_ne_u32 vcc, s47, v24\n"
        "v_cmpx_ne_u32 vcc, s47, v25\n"
        "v_cmpx_ne_u32 vcc, s47, v26\n"
        "v_cmpx_ne_u32 vcc, s47, v27\n"
        "v_cmpx_ne_u32 vcc, s47, v28\n"
        "v_cmpx_ne_u32 vcc, s47, v29\n"
        "v_cmpx_ne_u32 vcc, s47, v30\n"
        "v_cmpx_ne_u32 vcc, s47, v31\n"
        "v_accvgpr_read_b32 v17, a212\n"
        "v_cmpx_ne_u32 vcc, s47, v17\n"
        "v_accvgpr_read_b32 v17, a213\n"
        "v_cmpx_ne_u32 vcc, s47, v17\n"
        "v_accvgpr_read_b32 v17, a214\n"
        "v_cmpx_ne_u32 vcc, s47, v17\n"
        "v_accvgpr_read_b32 v17, a215\n"
        "v_cmpx_ne_u32 vcc, s47, v17\n"
        "v_accvgpr_read_b32 v17, a12\n"
        "v_cmpx_ne_u32 vcc, s47, v17\n"
        "v_accvgpr_read_b32 v17, a13\n"
        "v_cmpx_ne_u32 vcc, s47, v17\n"
        "v_accvgpr_read_b32 v17, a14\n"
        "v_cmpx_ne_u32 vcc, s47, v17\n"
        "v_accvgpr_read_b32 v17, a15\n"
        "v_cmpx_ne_u32 vcc, s47, v17\n"
        "v_accvgpr_read_b32 v17, a16\n"
        "v_cmpx_ne_u32 vcc, s47, v17\n"
        "v_accvgpr_read_b32 v17, a17\n"
        "v_cmpx_ne_u32 vcc, s47, v17\n"
        "v_accvgpr_read_b32 v17, a18\n"
        "v_cmpx_ne_u32 vcc, s47, v17\n"
        "v_accvgpr_read_b32 v17, a19\n"
        "v_cmpx_ne_u32 vcc, s47, v17\n"
        "v_accvgpr_read_b32 v17, a20\n"
        "v_cmpx_ne_u32 vcc, s47, v17\n"
        "v_accvgpr_read_b32 v17, a21\n"
        "v_cmpx_ne_u32 vcc, s47, v17\n"
        "v_accvgpr_read_b32 v17, a22\n"
        "v_cmpx_ne_u32 vcc, s47, v17\n"
        "v_accvgpr_read_b32 v17, a23\n"
        "v_cmpx_ne_u32 vcc, s47, v17\n"
        "v_accvgpr_read_b32 v17, a36\n"
        "v_cmpx_ne_u32 vcc, s47, v17\n"
        "v_accvgpr_read_b32 v17, a37\n"
        "v_cmpx_ne_u32 vcc, s47, v17\n"
        "v_accvgpr_read_b32 v17, a38\n"
        "v_cmpx_ne_u32 vcc, s47, v17\n"
        "v_accvgpr_read_b32 v17, a39\n"
        "v_cmpx_ne_u32 vcc, s47, v17\n"
        "v_accvgpr_read_b32 v17, a52\n"
        "v_cmpx_ne_u32 vcc, s47, v17\n"
        "v_accvgpr_read_b32 v17, a53\n"
        "v_cmpx_ne_u32 vcc, s47, v17\n"
        "v_accvgpr_read_b32 v17, a54\n"
        "v_cmpx_ne_u32 vcc, s47, v17\n"
        "v_accvgpr_read_b32 v17, a55\n"
        "v_cmpx_ne_u32 vcc, s47, v17\n"
        "v_accvgpr_read_b32 v17, a80\n"
        "v_cmpx_ne_u32 vcc, s47, v17\n"
        "v_accvgpr_read_b32 v17, a81\n"
        "v_cmpx_ne_u32 vcc, s47, v17\n"
        "v_accvgpr_read_b32 v17, a82\n"
        "v_cmpx_ne_u32 vcc, s47, v17\n"
        "v_accvgpr_read_b32 v17, a83\n"
        "v_cmpx_ne_u32 vcc, s47, v17\n"
        "v_accvgpr_read_b32 v17, a108\n"
        "v_cmpx_ne_u32 vcc, s47, v17\n"
        "v_accvgpr_read_b32 v17, a109\n"
        "v_cmpx_ne_u32 vcc, s47, v17\n"
        "v_accvgpr_read_b32 v17, a110\n"
        "v_cmpx_ne_u32 vcc, s47, v17\n"
        "v_accvgpr_read_b32 v17, a111\n"
        "v_cmpx_ne_u32 vcc, s47, v17\n"
        "v_accvgpr_read_b32 v17, a224\n"
        "v_cmpx_ne_u32 vcc, s47, v17\n"
        "v_accvgpr_read_b32 v17, a225\n"
        "v_cmpx_ne_u32 vcc, s47, v17\n"
        "v_accvgpr_read_b32 v17, a226\n"
        "v_cmpx_ne_u32 vcc, s47, v17\n"
        "v_accvgpr_read_b32 v17, a227\n"
        "v_cmpx_ne_u32 vcc, s47, v17\n"
        "v_accvgpr_read_b32 v17, a252\n"
        "v_cmpx_ne_u32 vcc, s47, v17\n"
        "v_accvgpr_read_b32 v17, a253\n"
        "v_cmpx_ne_u32 vcc, s47, v17\n"
        "v_accvgpr_read_b32 v17, a254\n"
        "v_cmpx_ne_u32 vcc, s47, v17\n"
        "v_accvgpr_read_b32 v17, a255\n"
        "v_cmpx_ne_u32 vcc, s47, v17\n"
        "s_nop 4\n"
        "s_cmp_eq_u64 exec, -1\n"
        "s_mov_b64 exec, -1\n"
        "s_cbranch_scc1 .Lq13\n"
        "s_sub_u32 s55, s55, 1\n"
        "s_cbranch_scc0 .Lp13\n"
        "s_mov_b32 s55, 0\n"
        ".Lq13:\n"
        "v_mfma_f32_16x16x32_bf16 v[84:87], v[124:127], v[24:27], 0\n"
        "v_mfma_f32_16x16x32_bf16 v[88:91], v[128:131], v[24:27], 0\n"
        "v_mfma_f32_16x16x32_bf16 v[92:95], v[132:135], v[24:27], 0\n"
        "v_mfma_f32_16x16x32_bf16 v[96:99], v[136:139], v[24:27], 0\n"
        "v_mfma_f32_16x16x32_bf16 v[100:103], v[140:143], v[24:27], 0\n"
        "v_mfma_f32_16x16x32_bf16 v[84:87], v[144:147], v[28:31], v[84:87]\n"
        "v_mfma_f32_16x16x32_bf16 v[88:91], v[148:151], v[28:31], v[88:91]\n"
        "v_mfma_f32_16x16x32_bf16 v[92:95], v[152:155], v[28:31], v[92:95]\n"
        "v_mfma_f32_16x16x32_bf16 v[96:99], v[156:159], v[28:31], v[96:99]\n"
        "v_mfma_f32_16x16x32_bf16 v[100:103], v[160:163], v[28:31], v[100:103]\n"
        "v_mfma_f32_16x16x32_bf16 v[84:87], v[164:167], a[212:215], v[84:87]\n"
        "v_mfma_f32_16x16x32_bf16 v[88:91], v[168:171], a[212:215], v[88:91]\n"
        "v_mfma_f32_16x16x32_bf16 v[92:95], v[172:175], a[212:215], v[92:95]\n"
        "v_mfma_f32_16x16x32_bf16 v[96:99], v[176:179], a[212:215], v[96:99]\n"
        "v_mfma_f32_16x16x32_bf16 v[100:103], v[180:183], a[212:215], v[100:103]\n"
        "v_mfma_f32_16x16x32_bf16 v[84:87], v[184:187], a[12:15], v[84:87]\n"
        "v_mfma_f32_16x16x32_bf16 v[88:91], v[188:191], a[12:15], v[88:91]\n"
        "v_mfma_f32_16x16x32_bf16 v[92:95], v[192:195], a[12:15], v[92:95]\n"
        "v_mfma_f32_16x16x32_bf16 v[96:99], v[196:199], a[12:15], v[96:99]\n"
        "v_mfma_f32_16x16x32_bf16 v[100:103], v[200:203], a[12:15], v[100:103]\n"
        "v_mfma_f32_16x16x32_bf16 v[84:87], v[204:207], a[16:19], v[84:87]\n"
        "v_mfma_f32_16x16x32_bf16 v[88:91], v[208:211], a[16:19], v[88:91]\n"
        "v_mfma_f32_16x16x32_bf16 v[92:95], v[212:215], a[16:19], v[92:95]\n"
        "v_mfma_f32_16x16x32_bf16 v[96:99], v[216:219], a[16:19], v[96:99]\n"
        "v_mfma_f32_16x16x32_bf16 v[100:103], v[220:223], a[16:19], v[100:103]\n"
        "v_mfma_f32_16x16x32_bf16 v[84:87], v[224:227], a[20:23], v[84:87]\n"
        "v_mfma_f32_16x16x32_bf16 v[88:91], v[228:231], a[20:23], v[88:91]\n"
        "v_mfma_f32_16x16x32_bf16 v[92:95], v[232:235], a[20:23], v[92:95]\n"
        "v_mfma_f32_16x16x32_bf16 v[96:99], v[236:239], a[20:23], v[96:99]\n"
        "v_mfma_f32_16x16x32_bf16 v[100:103], v[240:243], a[20:23], v[100:103]\n"
        "v_mfma_f32_16x16x32_bf16 v[84:87], a[0:3], a[36:39], v[84:87]\n"
        "v_mfma_f32_16x16x32_bf16 v[88:91], a[4:7], a[36:39], v[88:91]\n"
        "v_mfma_f32_16x16x32_bf16 v[92:95], a[8:11], a[36:39], v[92:95]\n"
        "v_mfma_f32_16x16x32_bf16 v[96:99], a[40:43], a[36:39], v[96:99]\n"
        "v_mfma_f32_16x16x32_bf16 v[100:103], a[44:47], a[36:39], v[100:103]\n"
        "v_mfma_f32_16x16x32_bf16 v[84:87], a[56:59], a[52:55], v[84:87]\n"
        "v_mfma_f32_16x16x32_bf16 v[88:91], a[60:63], a[52:55], v[88:91]\n"
        "v_mfma_f32_16x16x32_bf16 v[92:95], a[64:67], a[52:55], v[92:95]\n"
        "v_mfma_f32_16x16x32_bf16 v[96:99], a[68:71], a[52:55], v[96:99]\n"
        "v_mfma_f32_16x16x32_bf16 v[100:103], a[72:75], a[52:55], v[100:103]\n"
        "v_mfma_f32_16x16x32_bf16 v[84:87], a[84:87], a[80:83], v[84:87]\n"
        "v_mfma_f32_16x16x32_bf16 v[88:91], a[88:91], a[80:83], v[88:91]\n"
        "v_mfma_f32_16x16x32_bf16 v[92:95], a[92:95], a[80:83], v[92:95]\n"
        "v_mfma_f32_16x16x32_bf16 v[96:99], a[96:99], a[80:83], v[96:99]\n"
        "v_mfma_f32_16x16x32_bf16 v[100:103], a[100:103], a[80:83], v[100:103]\n"
        "v_mfma_f32_16x16x32_bf16 v[84:87], a[112:115], a[108:111], v[84:87]\n"
        "v_mfma_f32_16x16x32_bf16 v[88:91], a[116:119], a[108:111], v[88:91]\n"
        "v_mfma_f32_16x16x32_bf16 v[92:95], a[120:123], a[108:111], v[92:95]\n"
        "v_mfma_f32_16x16x32_bf16 v[96:99], a[124:127], a[108:111], v[96:99]\n"
        "v_mfma_f32_16x16x32_bf16 v[100:103], a[216:219], a[108:111], v[100:103]\n"
        "v_mfma_f32_16x16x32_bf16 v[84:87], a[228:231], a[224:227], v[84:87]\n"
        "v_mfma_f32_16x16x32_bf16 v[88:91], a[232:235], a[224:227], v[88:91]\n"
        "v_mfma_f32_16x16x32_bf16 v[92:95], a[236:239], a[224:227], v[92:95]\n"
        "v_mfma_f32_16x16x32_bf16 v[96:99], a[240:243], a[224:227], v[96:99]\n"
        "v_mfma_f32_16x16x32_bf16 v[100:103], a[244:247], a[224:227], v[100:103]\n"
        "v_mfma_f32_16x16x32_bf16 v[84:87], a[128:131], a[252:255], v[84:87]\n"
        "v_mfma_f32_16x16x32_bf16 v[88:91], a[132:135], a[252:255], v[88:91]\n"
        "v_mfma_f32_16x16x32_bf16 v[92:95], a[136:139], a[252:255], v[92:95]\n"
        "v_mfma_f32_16x16x32_bf16 v[96:99], a[140:143], a[252:255], v[96:99]\n"
        "v_mfma_f32_16x16x32_bf16 v[100:103], a[144:147], a[252:255], v[100:103]\n"
        "s_waitcnt lgkmcnt(0)\n"
        "s_nop 7\n"
        "s_nop 7\n"
        "s_branch .Lb25\n"
        ".Ls26:\n"
        "s_waitcnt lgkmcnt(0)\n"
        "s_nop 7\n"
        "s_nop 7\n"
        "s_branch .Lc14\n"
        ".Lp14:\n"
        "buffer_load_dwordx4 a[24:27], v3, s[24:27], s12 offen offset:2048 sc1\n"
        "buffer_load_dwordx4 a[28:31], v3, s[24:27], s12 offen offset:3072 sc1\n"
        "buffer_load_dwordx4 a[32:35], v4, s[24:27], s12 offen sc1\n"
        "buffer_load_dwordx4 a[48:51], v4, s[24:27], s12 offen offset:1024 sc1\n"
        "buffer_load_dwordx4 a[76:79], v4, s[24:27], s12 offen offset:2048 sc1\n"
        "buffer_load_dwordx4 a[104:107], v4, s[24:27], s12 offen offset:3072 sc1\n"
        "buffer_load_dwordx4 a[220:223], v3, s[24:27], s14 offen offset:2048 sc1\n"
        "buffer_load_dwordx4 a[248:251], v3, s[24:27], s14 offen offset:3072 sc1\n"
        "buffer_load_dwordx4 a[148:151], v4, s[24:27], s14 offen sc1\n"
        "buffer_load_dwordx4 a[152:155], v4, s[24:27], s14 offen offset:1024 sc1\n"
        "buffer_load_dwordx4 a[156:159], v4, s[24:27], s14 offen offset:2048 sc1\n"
        "buffer_load_dwordx4 a[160:163], v4, s[24:27], s14 offen offset:3072 sc1\n"
        "buffer_load_dwordx4 a[164:167], v2, s[24:27], s13 offen sc1\n"
        "buffer_load_dwordx4 a[168:171], v2, s[24:27], s13 offen offset:1024 sc1\n"
        "buffer_load_dwordx4 a[172:175], v2, s[24:27], s13 offen offset:2048 sc1\n"
        "buffer_load_dwordx4 a[176:179], v2, s[24:27], s13 offen offset:3072 sc1\n"
        "buffer_load_dwordx4 a[180:183], v3, s[24:27], s13 offen sc1\n"
        "buffer_load_dwordx4 a[184:187], v3, s[24:27], s13 offen offset:1024 sc1\n"
        "buffer_load_dwordx4 a[188:191], v2, s[24:27], s15 offen sc1\n"
        "buffer_load_dwordx4 a[192:195], v2, s[24:27], s15 offen offset:1024 sc1\n"
        "buffer_load_dwordx4 a[196:199], v2, s[24:27], s15 offen offset:2048 sc1\n"
        "buffer_load_dwordx4 a[200:203], v2, s[24:27], s15 offen offset:3072 sc1\n"
        "buffer_load_dwordx4 a[204:207], v3, s[24:27], s15 offen sc1\n"
        "buffer_load_dwordx4 a[208:211], v3, s[24:27], s15 offen offset:1024 sc1\n"
        "s_waitcnt vmcnt(0)\n"
        ".Lc14:\n"
        "v_accvgpr_read_b32 v17, a24\n"
        "v_cmpx_ne_u32 vcc, s47, v17\n"
        "v_accvgpr_read_b32 v17, a25\n"
        "v_cmpx_ne_u32 vcc, s47, v17\n"
        "v_accvgpr_read_b32 v17, a26\n"
        "v_cmpx_ne_u32 vcc, s47, v17\n"
        "v_accvgpr_read_b32 v17, a27\n"
        "v_cmpx_ne_u32 vcc, s47, v17\n"
        "v_accvgpr_read_b32 v17, a28\n"
        "v_cmpx_ne_u32 vcc, s47, v17\n"
        "v_accvgpr_read_b32 v17, a29\n"
        "v_cmpx_ne_u32 vcc, s47, v17\n"
        "v_accvgpr_read_b32 v17, a30\n"
        "v_cmpx_ne_u32 vcc, s47, v17\n"
        "v_accvgpr_read_b32 v17, a31\n"
        "v_cmpx_ne_u32 vcc, s47, v17\n"
        "v_accvgpr_read_b32 v17, a32\n"
        "v_cmpx_ne_u32 vcc, s47, v17\n"
        "v_accvgpr_read_b32 v17, a33\n"
        "v_cmpx_ne_u32 vcc, s47, v17\n"
        "v_accvgpr_read_b32 v17, a34\n"
        "v_cmpx_ne_u32 vcc, s47, v17\n"
        "v_accvgpr_read_b32 v17, a35\n"
        "v_cmpx_ne_u32 vcc, s47, v17\n"
        "v_accvgpr_read_b32 v17, a48\n"
        "v_cmpx_ne_u32 vcc, s47, v17\n"
        "v_accvgpr_read_b32 v17, a49\n"
        "v_cmpx_ne_u32 vcc, s47, v17\n"
        "v_accvgpr_read_b32 v17, a50\n"
        "v_cmpx_ne_u32 vcc, s47, v17\n"
        "v_accvgpr_read_b32 v17, a51\n"
        "v_cmpx_ne_u32 vcc, s47, v17\n"
        "v_accvgpr_read_b32 v17, a76\n"
        "v_cmpx_ne_u32 vcc, s47, v17\n"
        "v_accvgpr_read_b32 v17, a77\n"
        "v_cmpx_ne_u32 vcc, s47, v17\n"
        "v_accvgpr_read_b32 v17, a78\n"
        "v_cmpx_ne_u32 vcc, s47, v17\n"
        "v_accvgpr_read_b32 v17, a79\n"
        "v_cmpx_ne_u32 vcc, s47, v17\n"
        "v_accvgpr_read_b32 v17, a104\n"
        "v_cmpx_ne_u32 vcc, s47, v17\n"
        "v_accvgpr_read_b32 v17, a105\n"
        "v_cmpx_ne_u32 vcc, s47, v17\n"
        "v_accvgpr_read_b32 v17, a106\n"
        "v_cmpx_ne_u32 vcc, s47, v17\n"
        "v_accvgpr_read_b32 v17, a107\n"
        "v_cmpx_ne_u32 vcc, s47, v17\n"
        "v_accvgpr_read_b32 v17, a220\n"
        "v_cmpx_ne_u32 vcc, s47, v17\n"
        "v_accvgpr_read_b32 v17, a221\n"
        "v_cmpx_ne_u32 vcc, s47, v17\n"
        "v_accvgpr_read_b32 v17, a222\n"
        "v_cmpx_ne_u32 vcc, s47, v17\n"
        "v_accvgpr_read_b32 v17, a223\n"
        "v_cmpx_ne_u32 vcc, s47, v17\n"
        "v_accvgpr_read_b32 v17, a248\n"
        "v_cmpx_ne_u32 vcc, s47, v17\n"
        "v_accvgpr_read_b32 v17, a249\n"
        "v_cmpx_ne_u32 vcc, s47, v17\n"
        "v_accvgpr_read_b32 v17, a250\n"
        "v_cmpx_ne_u32 vcc, s47, v17\n"
        "v_accvgpr_read_b32 v17, a251\n"
        "v_cmpx_ne_u32 vcc, s47, v17\n"
        "v_accvgpr_read_b32 v17, a148\n"
        "v_cmpx_ne_u32 vcc, s47, v17\n"
        "v_accvgpr_read_b32 v17, a149\n"
        "v_cmpx_ne_u32 vcc, s47, v17\n"
        "v_accvgpr_read_b32 v17, a150\n"
        "v_cmpx_ne_u32 vcc, s47, v17\n"
        "v_accvgpr_read_b32 v17, a151\n"
        "v_cmpx_ne_u32 vcc, s47, v17\n"
        "v_accvgpr_read_b32 v17, a152\n"
        "v_cmpx_ne_u32 vcc, s47, v17\n"
        "v_accvgpr_read_b32 v17, a153\n"
        "v_cmpx_ne_u32 vcc, s47, v17\n"
        "v_accvgpr_read_b32 v17, a154\n"
        "v_cmpx_ne_u32 vcc, s47, v17\n"
        "v_accvgpr_read_b32 v17, a155\n"
        "v_cmpx_ne_u32 vcc, s47, v17\n"
        "v_accvgpr_read_b32 v17, a156\n"
        "v_cmpx_ne_u32 vcc, s47, v17\n"
        "v_accvgpr_read_b32 v17, a157\n"
        "v_cmpx_ne_u32 vcc, s47, v17\n"
        "v_accvgpr_read_b32 v17, a158\n"
        "v_cmpx_ne_u32 vcc, s47, v17\n"
        "v_accvgpr_read_b32 v17, a159\n"
        "v_cmpx_ne_u32 vcc, s47, v17\n"
        "v_accvgpr_read_b32 v17, a160\n"
        "v_cmpx_ne_u32 vcc, s47, v17\n"
        "v_accvgpr_read_b32 v17, a161\n"
        "v_cmpx_ne_u32 vcc, s47, v17\n"
        "v_accvgpr_read_b32 v17, a162\n"
        "v_cmpx_ne_u32 vcc, s47, v17\n"
        "v_accvgpr_read_b32 v17, a163\n"
        "v_cmpx_ne_u32 vcc, s47, v17\n"
        "s_nop 4\n"
        "s_cmp_eq_u64 exec, -1\n"
        "s_mov_b64 exec, -1\n"
        "s_cbranch_scc1 .Lq14\n"
        "s_sub_u32 s55, s55, 1\n"
        "s_cbranch_scc0 .Lp14\n"
        "s_mov_b32 s55, 0\n"
        ".Lq14:\n"
        "v_mfma_f32_16x16x32_bf16 v[104:107], v[124:127], a[24:27], 0\n"
        "v_mfma_f32_16x16x32_bf16 v[108:111], v[128:131], a[24:27], 0\n"
        "v_mfma_f32_16x16x32_bf16 v[112:115], v[132:135], a[24:27], 0\n"
        "v_mfma_f32_16x16x32_bf16 v[116:119], v[136:139], a[24:27], 0\n"
        "v_mfma_f32_16x16x32_bf16 v[120:123], v[140:143], a[24:27], 0\n"
        "v_mfma_f32_16x16x32_bf16 v[104:107], v[144:147], a[28:31], v[104:107]\n"
        "v_mfma_f32_16x16x32_bf16 v[108:111], v[148:151], a[28:31], v[108:111]\n"
        "v_mfma_f32_16x16x32_bf16 v[112:115], v[152:155], a[28:31], v[112:115]\n"
        "v_mfma_f32_16x16x32_bf16 v[116:119], v[156:159], a[28:31], v[116:119]\n"
        "v_mfma_f32_16x16x32_bf16 v[120:123], v[160:163], a[28:31], v[120:123]\n"
        "v_mfma_f32_16x16x32_bf16 v[104:107], v[164:167], a[32:35], v[104:107]\n"
        "v_mfma_f32_16x16x32_bf16 v[108:111], v[168:171], a[32:35], v[108:111]\n"
        "v_mfma_f32_16x16x32_bf16 v[112:115], v[172:175], a[32:35], v[112:115]\n"
        "v_mfma_f32_16x16x32_bf16 v[116:119], v[176:179], a[32:35], v[116:119]\n"
        "v_mfma_f32_16x16x32_bf16 v[120:123], v[180:183], a[32:35], v[120:123]\n"
        "v_mfma_f32_16x16x32_bf16 v[104:107], v[184:187], a[48:51], v[104:107]\n"
        "v_mfma_f32_16x16x32_bf16 v[108:111], v[188:191], a[48:51], v[108:111]\n"
        "v_mfma_f32_16x16x32_bf16 v[112:115], v[192:195], a[48:51], v[112:115]\n"
        "v_mfma_f32_16x16x32_bf16 v[116:119], v[196:199], a[48:51], v[116:119]\n"
        "v_mfma_f32_16x16x32_bf16 v[120:123], v[200:203], a[48:51], v[120:123]\n"
        "v_mfma_f32_16x16x32_bf16 v[104:107], v[204:207], a[76:79], v[104:107]\n"
        "v_mfma_f32_16x16x32_bf16 v[108:111], v[208:211], a[76:79], v[108:111]\n"
        "v_mfma_f32_16x16x32_bf16 v[112:115], v[212:215], a[76:79], v[112:115]\n"
        "v_mfma_f32_16x16x32_bf16 v[116:119], v[216:219], a[76:79], v[116:119]\n"
        "v_mfma_f32_16x16x32_bf16 v[120:123], v[220:223], a[76:79], v[120:123]\n"
        "v_mfma_f32_16x16x32_bf16 v[104:107], v[224:227], a[104:107], v[104:107]\n"
        "v_mfma_f32_16x16x32_bf16 v[108:111], v[228:231], a[104:107], v[108:111]\n"
        "v_mfma_f32_16x16x32_bf16 v[112:115], v[232:235], a[104:107], v[112:115]\n"
        "v_mfma_f32_16x16x32_bf16 v[116:119], v[236:239], a[104:107], v[116:119]\n"
        "v_mfma_f32_16x16x32_bf16 v[120:123], v[240:243], a[104:107], v[120:123]\n"
        "v_mfma_f32_16x16x32_bf16 v[104:107], a[0:3], a[220:223], v[104:107]\n"
        "v_mfma_f32_16x16x32_bf16 v[108:111], a[4:7], a[220:223], v[108:111]\n"
        "v_mfma_f32_16x16x32_bf16 v[112:115], a[8:11], a[220:223], v[112:115]\n"
        "v_mfma_f32_16x16x32_bf16 v[116:119], a[40:43], a[220:223], v[116:119]\n"
        "v_mfma_f32_16x16x32_bf16 v[120:123], a[44:47], a[220:223], v[120:123]\n"
        "v_mfma_f32_16x16x32_bf16 v[104:107], a[56:59], a[248:251], v[104:107]\n"
        "v_mfma_f32_16x16x32_bf16 v[108:111], a[60:63], a[248:251], v[108:111]\n"
        "v_mfma_f32_16x16x32_bf16 v[112:115], a[64:67], a[248:251], v[112:115]\n"
        "v_mfma_f32_16x16x32_bf16 v[116:119], a[68:71], a[248:251], v[116:119]\n"
        "v_mfma_f32_16x16x32_bf16 v[120:123], a[72:75], a[248:251], v[120:123]\n"
        "v_mfma_f32_16x16x32_bf16 v[104:107], a[84:87], a[148:151], v[104:107]\n"
        "v_mfma_f32_16x16x32_bf16 v[108:111], a[88:91], a[148:151], v[108:111]\n"
        "v_mfma_f32_16x16x32_bf16 v[112:115], a[92:95], a[148:151], v[112:115]\n"
        "v_mfma_f32_16x16x32_bf16 v[116:119], a[96:99], a[148:151], v[116:119]\n"
        "v_mfma_f32_16x16x32_bf16 v[120:123], a[100:103], a[148:151], v[120:123]\n"
        "v_mfma_f32_16x16x32_bf16 v[104:107], a[112:115], a[152:155], v[104:107]\n"
        "v_mfma_f32_16x16x32_bf16 v[108:111], a[116:119], a[152:155], v[108:111]\n"
        "v_mfma_f32_16x16x32_bf16 v[112:115], a[120:123], a[152:155], v[112:115]\n"
        "v_mfma_f32_16x16x32_bf16 v[116:119], a[124:127], a[152:155], v[116:119]\n"
        "v_mfma_f32_16x16x32_bf16 v[120:123], a[216:219], a[152:155], v[120:123]\n"
        "v_mfma_f32_16x16x32_bf16 v[104:107], a[228:231], a[156:159], v[104:107]\n"
        "v_mfma_f32_16x16x32_bf16 v[108:111], a[232:235], a[156:159], v[108:111]\n"
        "v_mfma_f32_16x16x32_bf16 v[112:115], a[236:239], a[156:159], v[112:115]\n"
        "v_mfma_f32_16x16x32_bf16 v[116:119], a[240:243], a[156:159], v[116:119]\n"
        "v_mfma_f32_16x16x32_bf16 v[120:123], a[244:247], a[156:159], v[120:123]\n"
        "v_mfma_f32_16x16x32_bf16 v[104:107], a[128:131], a[160:163], v[104:107]\n"
        "v_mfma_f32_16x16x32_bf16 v[108:111], a[132:135], a[160:163], v[108:111]\n"
        "v_mfma_f32_16x16x32_bf16 v[112:115], a[136:139], a[160:163], v[112:115]\n"
        "v_mfma_f32_16x16x32_bf16 v[116:119], a[140:143], a[160:163], v[116:119]\n"
        "v_mfma_f32_16x16x32_bf16 v[120:123], a[144:147], a[160:163], v[120:123]\n"
        "s_waitcnt lgkmcnt(0)\n"
        "s_nop 7\n"
        "s_nop 7\n"
        "s_branch .Lb26\n"
        ".Ls27:\n"
        "s_waitcnt lgkmcnt(0)\n"
        "s_nop 7\n"
        "s_nop 7\n"
        "s_branch .Lc15\n"
        ".Lp15:\n"
        "buffer_load_dwordx4 a[164:167], v2, s[24:27], s13 offen sc1\n"
        "buffer_load_dwordx4 a[168:171], v2, s[24:27], s13 offen offset:1024 sc1\n"
        "buffer_load_dwordx4 a[172:175], v2, s[24:27], s13 offen offset:2048 sc1\n"
        "buffer_load_dwordx4 a[176:179], v2, s[24:27], s13 offen offset:3072 sc1\n"
        "buffer_load_dwordx4 a[180:183], v3, s[24:27], s13 offen sc1\n"
        "buffer_load_dwordx4 a[184:187], v3, s[24:27], s13 offen offset:1024 sc1\n"
        "buffer_load_dwordx4 a[188:191], v2, s[24:27], s15 offen sc1\n"
        "buffer_load_dwordx4 a[192:195], v2, s[24:27], s15 offen offset:1024 sc1\n"
        "buffer_load_dwordx4 a[196:199], v2, s[24:27], s15 offen offset:2048 sc1\n"
        "buffer_load_dwordx4 a[200:203], v2, s[24:27], s15 offen offset:3072 sc1\n"
        "buffer_load_dwordx4 a[204:207], v3, s[24:27], s15 offen sc1\n"
        "buffer_load_dwordx4 a[208:211], v3, s[24:27], s15 offen offset:1024 sc1\n"
        "buffer_load_dwordx4 a[212:215], v3, s[24:27], s13 offen offset:2048 sc1\n"
        "buffer_load_dwordx4 a[12:15], v3, s[24:27], s13 offen offset:3072 sc1\n"
        "buffer_load_dwordx4 a[16:19], v4, s[24:27], s13 offen sc1\n"
        "buffer_load_dwordx4 a[20:23], v4, s[24:27], s13 offen offset:1024 sc1\n"
        "buffer_load_dwordx4 a[36:39], v4, s[24:27], s13 offen offset:2048 sc1\n"
        "buffer_load_dwordx4 a[52:55], v4, s[24:27], s13 offen offset:3072 sc1\n"
        "buffer_load_dwordx4 a[80:83], v3, s[24:27], s15 offen offset:2048 sc1\n"
        "buffer_load_dwordx4 a[108:111], v3, s[24:27], s15 offen offset:3072 sc1\n"
        "buffer_load_dwordx4 a[224:227], v4, s[24:27], s15 offen sc1\n"
        "buffer_load_dwordx4 a[252:255], v4, s[24:27], s15 offen offset:1024 sc1\n"
        "buffer_load_dwordx4 a[24:27], v4, s[24:27], s15 offen offset:2048 sc1\n"
        "buffer_load_dwordx4 a[28:31], v4, s[24:27], s15 offen offset:3072 sc1\n"
        "s_waitcnt vmcnt(0)\n"
        ".Lc15:\n"
        "v_accvgpr_read_b32 v17, a164\n"
        "v_cmpx_ne_u32 vcc, s47, v17\n"
        "v_accvgpr_read_b32 v17, a165\n"
        "v_cmpx_ne_u32 vcc, s47, v17\n"
        "v_accvgpr_read_b32 v17, a166\n"
        "v_cmpx_ne_u32 vcc, s47, v17\n"
        "v_accvgpr_read_b32 v17, a167\n"
        "v_cmpx_ne_u32 vcc, s47, v17\n"
        "v_accvgpr_read_b32 v17, a168\n"
        "v_cmpx_ne_u32 vcc, s47, v17\n"
        "v_accvgpr_read_b32 v17, a169\n"
        "v_cmpx_ne_u32 vcc, s47, v17\n"
        "v_accvgpr_read_b32 v17, a170\n"
        "v_cmpx_ne_u32 vcc, s47, v17\n"
        "v_accvgpr_read_b32 v17, a171\n"
        "v_cmpx_ne_u32 vcc, s47, v17\n"
        "v_accvgpr_read_b32 v17, a172\n"
        "v_cmpx_ne_u32 vcc, s47, v17\n"
        "v_accvgpr_read_b32 v17, a173\n"
        "v_cmpx_ne_u32 vcc, s47, v17\n"
        "v_accvgpr_read_b32 v17, a174\n"
        "v_cmpx_ne_u32 vcc, s47, v17\n"
        "v_accvgpr_read_b32 v17, a175\n"
        "v_cmpx_ne_u32 vcc, s47, v17\n"
        "v_accvgpr_read_b32 v17, a176\n"
        "v_cmpx_ne_u32 vcc, s47, v17\n"
        "v_accvgpr_read_b32 v17, a177\n"
        "v_cmpx_ne_u32 vcc, s47, v17\n"
        "v_accvgpr_read_b32 v17, a178\n"
        "v_cmpx_ne_u32 vcc, s47, v17\n"
        "v_accvgpr_read_b32 v17, a179\n"
        "v_cmpx_ne_u32 vcc, s47, v17\n"
        "v_accvgpr_read_b32 v17, a180\n"
        "v_cmpx_ne_u32 vcc, s47, v17\n"
        "v_accvgpr_read_b32 v17, a181\n"
        "v_cmpx_ne_u32 vcc, s47, v17\n"
        "v_accvgpr_read_b32 v17, a182\n"
        "v_cmpx_ne_u32 vcc, s47, v17\n"
        "v_accvgpr_read_b32 v17, a183\n"
        "v_cmpx_ne_u32 vcc, s47, v17\n"
        "v_accvgpr_read_b32 v17, a184\n"
        "v_cmpx_ne_u32 vcc, s47, v17\n"
        "v_accvgpr_read_b32 v17, a185\n"
        "v_cmpx_ne_u32 vcc, s47, v17\n"
        "v_accvgpr_read_b32 v17, a186\n"
        "v_cmpx_ne_u32 vcc, s47, v17\n"
        "v_accvgpr_read_b32 v17, a187\n"
        "v_cmpx_ne_u32 vcc, s47, v17\n"
        "v_accvgpr_read_b32 v17, a188\n"
        "v_cmpx_ne_u32 vcc, s47, v17\n"
        "v_accvgpr_read_b32 v17, a189\n"
        "v_cmpx_ne_u32 vcc, s47, v17\n"
        "v_accvgpr_read_b32 v17, a190\n"
        "v_cmpx_ne_u32 vcc, s47, v17\n"
        "v_accvgpr_read_b32 v17, a191\n"
        "v_cmpx_ne_u32 vcc, s47, v17\n"
        "v_accvgpr_read_b32 v17, a192\n"
        "v_cmpx_ne_u32 vcc, s47, v17\n"
        "v_accvgpr_read_b32 v17, a193\n"
        "v_cmpx_ne_u32 vcc, s47, v17\n"
        "v_accvgpr_read_b32 v17, a194\n"
        "v_cmpx_ne_u32 vcc, s47, v17\n"
        "v_accvgpr_read_b32 v17, a195\n"
        "v_cmpx_ne_u32 vcc, s47, v17\n"
        "v_accvgpr_read_b32 v17, a196\n"
        "v_cmpx_ne_u32 vcc, s47, v17\n"
        "v_accvgpr_read_b32 v17, a197\n"
        "v_cmpx_ne_u32 vcc, s47, v17\n"
        "v_accvgpr_read_b32 v17, a198\n"
        "v_cmpx_ne_u32 vcc, s47, v17\n"
        "v_accvgpr_read_b32 v17, a199\n"
        "v_cmpx_ne_u32 vcc, s47, v17\n"
        "v_accvgpr_read_b32 v17, a200\n"
        "v_cmpx_ne_u32 vcc, s47, v17\n"
        "v_accvgpr_read_b32 v17, a201\n"
        "v_cmpx_ne_u32 vcc, s47, v17\n"
        "v_accvgpr_read_b32 v17, a202\n"
        "v_cmpx_ne_u32 vcc, s47, v17\n"
        "v_accvgpr_read_b32 v17, a203\n"
        "v_cmpx_ne_u32 vcc, s47, v17\n"
        "v_accvgpr_read_b32 v17, a204\n"
        "v_cmpx_ne_u32 vcc, s47, v17\n"
        "v_accvgpr_read_b32 v17, a205\n"
        "v_cmpx_ne_u32 vcc, s47, v17\n"
        "v_accvgpr_read_b32 v17, a206\n"
        "v_cmpx_ne_u32 vcc, s47, v17\n"
        "v_accvgpr_read_b32 v17, a207\n"
        "v_cmpx_ne_u32 vcc, s47, v17\n"
        "v_accvgpr_read_b32 v17, a208\n"
        "v_cmpx_ne_u32 vcc, s47, v17\n"
        "v_accvgpr_read_b32 v17, a209\n"
        "v_cmpx_ne_u32 vcc, s47, v17\n"
        "v_accvgpr_read_b32 v17, a210\n"
        "v_cmpx_ne_u32 vcc, s47, v17\n"
        "v_accvgpr_read_b32 v17, a211\n"
        "v_cmpx_ne_u32 vcc, s47, v17\n"
        "s_nop 4\n"
        "s_cmp_eq_u64 exec, -1\n"
        "s_mov_b64 exec, -1\n"
        "s_cbranch_scc1 .Lq15\n"
        "s_sub_u32 s55, s55, 1\n"
        "s_cbranch_scc0 .Lp15\n"
        "s_mov_b32 s55, 0\n"
        ".Lq15:\n"
        "v_mfma_f32_16x16x32_bf16 v[84:87], v[124:127], a[164:167], 0\n"
        "v_mfma_f32_16x16x32_bf16 v[88:91], v[128:131], a[164:167], 0\n"
        "v_mfma_f32_16x16x32_bf16 v[92:95], v[132:135], a[164:167], 0\n"
        "v_mfma_f32_16x16x32_bf16 v[96:99], v[136:139], a[164:167], 0\n"
        "v_mfma_f32_16x16x32_bf16 v[100:103], v[140:143], a[164:167], 0\n"
        "v_mfma_f32_16x16x32_bf16 v[84:87], v[144:147], a[168:171], v[84:87]\n"
        "v_mfma_f32_16x16x32_bf16 v[88:91], v[148:151], a[168:171], v[88:91]\n"
        "v_mfma_f32_16x16x32_bf16 v[92:95], v[152:155], a[168:171], v[92:95]\n"
        "v_mfma_f32_16x16x32_bf16 v[96:99], v[156:159], a[168:171], v[96:99]\n"
        "v_mfma_f32_16x16x32_bf16 v[100:103], v[160:163], a[168:171], v[100:103]\n"
        "v_mfma_f32_16x16x32_bf16 v[84:87], v[164:167], a[172:175], v[84:87]\n"
        "v_mfma_f32_16x16x32_bf16 v[88:91], v[168:171], a[172:175], v[88:91]\n"
        "v_mfma_f32_16x16x32_bf16 v[92:95], v[172:175], a[172:175], v[92:95]\n"
        "v_mfma_f32_16x16x32_bf16 v[96:99], v[176:179], a[172:175], v[96:99]\n"
        "v_mfma_f32_16x16x32_bf16 v[100:103], v[180:183], a[172:175], v[100:103]\n"
        "v_mfma_f32_16x16x32_bf16 v[84:87], v[184:187], a[176:179], v[84:87]\n"
        "v_mfma_f32_16x16x32_bf16 v[88:91], v[188:191], a[176:179], v[88:91]\n"
        "v_mfma_f32_16x16x32_bf16 v[92:95], v[192:195], a[176:179], v[92:95]\n"
        "v_mfma_f32_16x16x32_bf16 v[96:99], v[196:199], a[176:179], v[96:99]\n"
        "v_mfma_f32_16x16x32_bf16 v[100:103], v[200:203], a[176:179], v[100:103]\n"
        "v_mfma_f32_16x16x32_bf16 v[84:87], v[204:207], a[180:183], v[84:87]\n"
        "v_mfma_f32_16x16x32_bf16 v[88:91], v[208:211], a[180:183], v[88:91]\n"
        "v_mfma_f32_16x16x32_bf16 v[92:95], v[212:215], a[180:183], v[92:95]\n"
        "v_mfma_f32_16x16x32_bf16 v[96:99], v[216:219], a[180:183], v[96:99]\n"
        "v_mfma_f32_16x16x32_bf16 v[100:103], v[220:223], a[180:183], v[100:103]\n"
        "v_mfma_f32_16x16x32_bf16 v[84:87], v[224:227], a[184:187], v[84:87]\n"
        "v_mfma_f32_16x16x32_bf16 v[88:91], v[228:231], a[184:187], v[88:91]\n"
        "v_mfma_f32_16x16x32_bf16 v[92:95], v[232:235], a[184:187], v[92:95]\n"
        "v_mfma_f32_16x16x32_bf16 v[96:99], v[236:239], a[184:187], v[96:99]\n"
        "v_mfma_f32_16x16x32_bf16 v[100:103], v[240:243], a[184:187], v[100:103]\n"
        "v_mfma_f32_16x16x32_bf16 v[84:87], a[0:3], a[188:191], v[84:87]\n"
        "v_mfma_f32_16x16x32_bf16 v[88:91], a[4:7], a[188:191], v[88:91]\n"
        "v_mfma_f32_16x16x32_bf16 v[92:95], a[8:11], a[188:191], v[92:95]\n"
        "v_mfma_f32_16x16x32_bf16 v[96:99], a[40:43], a[188:191], v[96:99]\n"
        "v_mfma_f32_16x16x32_bf16 v[100:103], a[44:47], a[188:191], v[100:103]\n"
        "v_mfma_f32_16x16x32_bf16 v[84:87], a[56:59], a[192:195], v[84:87]\n"
        "v_mfma_f32_16x16x32_bf16 v[88:91], a[60:63], a[192:195], v[88:91]\n"
        "v_mfma_f32_16x16x32_bf16 v[92:95], a[64:67], a[192:195], v[92:95]\n"
        "v_mfma_f32_16x16x32_bf16 v[96:99], a[68:71], a[192:195], v[96:99]\n"
        "v_mfma_f32_16x16x32_bf16 v[100:103], a[72:75], a[192:195], v[100:103]\n"
        "v_mfma_f32_16x16x32_bf16 v[84:87], a[84:87], a[196:199], v[84:87]\n"
        "v_mfma_f32_16x16x32_bf16 v[88:91], a[88:91], a[196:199], v[88:91]\n"
        "v_mfma_f32_16x16x32_bf16 v[92:95], a[92:95], a[196:199], v[92:95]\n"
        "v_mfma_f32_16x16x32_bf16 v[96:99], a[96:99], a[196:199], v[96:99]\n"
        "v_mfma_f32_16x16x32_bf16 v[100:103], a[100:103], a[196:199], v[100:103]\n"
        "v_mfma_f32_16x16x32_bf16 v[84:87], a[112:115], a[200:203], v[84:87]\n"
        "v_mfma_f32_16x16x32_bf16 v[88:91], a[116:119], a[200:203], v[88:91]\n"
        "v_mfma_f32_16x16x32_bf16 v[92:95], a[120:123], a[200:203], v[92:95]\n"
        "v_mfma_f32_16x16x32_bf16 v[96:99], a[124:127], a[200:203], v[96:99]\n"
        "v_mfma_f32_16x16x32_bf16 v[100:103], a[216:219], a[200:203], v[100:103]\n"
        "v_mfma_f32_16x16x32_bf16 v[84:87], a[228:231], a[204:207], v[84:87]\n"
        "v_mfma_f32_16x16x32_bf16 v[88:91], a[232:235], a[204:207], v[88:91]\n"
        "v_mfma_f32_16x16x32_bf16 v[92:95], a[236:239], a[204:207], v[92:95]\n"
        "v_mfma_f32_16x16x32_bf16 v[96:99], a[240:243], a[204:207], v[96:99]\n"
        "v_mfma_f32_16x16x32_bf16 v[100:103], a[244:247], a[204:207], v[100:103]\n"
        "v_mfma_f32_16x16x32_bf16 v[84:87], a[128:131], a[208:211], v[84:87]\n"
        "v_mfma_f32_16x16x32_bf16 v[88:91], a[132:135], a[208:211], v[88:91]\n"
        "v_mfma_f32_16x16x32_bf16 v[92:95], a[136:139], a[208:211], v[92:95]\n"
        "v_mfma_f32_16x16x32_bf16 v[96:99], a[140:143], a[208:211], v[96:99]\n"
        "v_mfma_f32_16x16x32_bf16 v[100:103], a[144:147], a[208:211], v[100:103]\n"
        "s_waitcnt lgkmcnt(0)\n"
        "s_nop 7\n"
        "s_nop 7\n"
        "s_branch .Lb27\n"
        ".Ls28:\n"
        "s_waitcnt lgkmcnt(0)\n"
        "s_nop 7\n"
        "s_nop 7\n"
        "s_branch .Lc16\n"
        ".Lp16:\n"
        "buffer_load_dwordx4 a[212:215], v3, s[24:27], s13 offen offset:2048 sc1\n"
        "buffer_load_dwordx4 a[12:15], v3, s[24:27], s13 offen offset:3072 sc1\n"
        "buffer_load_dwordx4 a[16:19], v4, s[24:27], s13 offen sc1\n"
        "buffer_load_dwordx4 a[20:23], v4, s[24:27], s13 offen offset:1024 sc1\n"
        "buffer_load_dwordx4 a[36:39], v4, s[24:27], s13 offen offset:2048 sc1\n"
        "buffer_load_dwordx4 a[52:55], v4, s[24:27], s13 offen offset:3072 sc1\n"
        "buffer_load_dwordx4 a[80:83], v3, s[24:27], s15 offen offset:2048 sc1\n"
        "buffer_load_dwordx4 a[108:111], v3, s[24:27], s15 offen offset:3072 sc1\n"
        "buffer_load_dwordx4 a[224:227], v4, s[24:27], s15 offen sc1\n"
        "buffer_load_dwordx4 a[252:255], v4, s[24:27], s15 offen offset:1024 sc1\n"
        "buffer_load_dwordx4 a[24:27], v4, s[24:27], s15 offen offset:2048 sc1\n"
        "buffer_load_dwordx4 a[28:31], v4, s[24:27], s15 offen offset:3072 sc1\n"
        "s_waitcnt vmcnt(0)\n"
        ".Lc16:\n"
        "v_accvgpr_read_b32 v17, a212\n"
        "v_cmpx_ne_u32 vcc, s47, v17\n"
        "v_accvgpr_read_b32 v17, a213\n"
        "v_cmpx_ne_u32 vcc, s47, v17\n"
        "v_accvgpr_read_b32 v17, a214\n"
        "v_cmpx_ne_u32 vcc, s47, v17\n"
        "v_accvgpr_read_b32 v17, a215\n"
        "v_cmpx_ne_u32 vcc, s47, v17\n"
        "v_accvgpr_read_b32 v17, a12\n"
        "v_cmpx_ne_u32 vcc, s47, v17\n"
        "v_accvgpr_read_b32 v17, a13\n"
        "v_cmpx_ne_u32 vcc, s47, v17\n"
        "v_accvgpr_read_b32 v17, a14\n"
        "v_cmpx_ne_u32 vcc, s47, v17\n"
        "v_accvgpr_read_b32 v17, a15\n"
        "v_cmpx_ne_u32 vcc, s47, v17\n"
        "v_accvgpr_read_b32 v17, a16\n"
        "v_cmpx_ne_u32 vcc, s47, v17\n"
        "v_accvgpr_read_b32 v17, a17\n"
        "v_cmpx_ne_u32 vcc, s47, v17\n"
        "v_accvgpr_read_b32 v17, a18\n"
        "v_cmpx_ne_u32 vcc, s47, v17\n"
        "v_accvgpr_read_b32 v17, a19\n"
        "v_cmpx_ne_u32 vcc, s47, v17\n"
        "v_accvgpr_read_b32 v17, a20\n"
        "v_cmpx_ne_u32 vcc, s47, v17\n"
        "v_accvgpr_read_b32 v17, a21\n"
        "v_cmpx_ne_u32 vcc, s47, v17\n"
        "v_accvgpr_read_b32 v17, a22\n"
        "v_cmpx_ne_u32 vcc, s47, v17\n"
        "v_accvgpr_read_b32 v17, a23\n"
        "v_cmpx_ne_u32 vcc, s47, v17\n"
        "v_accvgpr_read_b32 v17, a36\n"
        "v_cmpx_ne_u32 vcc, s47, v17\n"
        "v_accvgpr_read_b32 v17, a37\n"
        "v_cmpx_ne_u32 vcc, s47, v17\n"
        "v_accvgpr_read_b32 v17, a38\n"
        "v_cmpx_ne_u32 vcc, s47, v17\n"
        "v_accvgpr_read_b32 v17, a39\n"
        "v_cmpx_ne_u32 vcc, s47, v17\n"
        "v_accvgpr_read_b32 v17, a52\n"
        "v_cmpx_ne_u32 vcc, s47, v17\n"
        "v_accvgpr_read_b32 v17, a53\n"
        "v_cmpx_ne_u32 vcc, s47, v17\n"
        "v_accvgpr_read_b32 v17, a54\n"
        "v_cmpx_ne_u32 vcc, s47, v17\n"
        "v_accvgpr_read_b32 v17, a55\n"
        "v_cmpx_ne_u32 vcc, s47, v17\n"
        "v_accvgpr_read_b32 v17, a80\n"
        "v_cmpx_ne_u32 vcc, s47, v17\n"
        "v_accvgpr_read_b32 v17, a81\n"
        "v_cmpx_ne_u32 vcc, s47, v17\n"
        "v_accvgpr_read_b32 v17, a82\n"
        "v_cmpx_ne_u32 vcc, s47, v17\n"
        "v_accvgpr_read_b32 v17, a83\n"
        "v_cmpx_ne_u32 vcc, s47, v17\n"
        "v_accvgpr_read_b32 v17, a108\n"
        "v_cmpx_ne_u32 vcc, s47, v17\n"
        "v_accvgpr_read_b32 v17, a109\n"
        "v_cmpx_ne_u32 vcc, s47, v17\n"
        "v_accvgpr_read_b32 v17, a110\n"
        "v_cmpx_ne_u32 vcc, s47, v17\n"
        "v_accvgpr_read_b32 v17, a111\n"
        "v_cmpx_ne_u32 vcc, s47, v17\n"
        "v_accvgpr_read_b32 v17, a224\n"
        "v_cmpx_ne_u32 vcc, s47, v17\n"
        "v_accvgpr_read_b32 v17, a225\n"
        "v_cmpx_ne_u32 vcc, s47, v17\n"
        "v_accvgpr_read_b32 v17, a226\n"
        "v_cmpx_ne_u32 vcc, s47, v17\n"
        "v_accvgpr_read_b32 v17, a227\n"
        "v_cmpx_ne_u32 vcc, s47, v17\n"
        "v_accvgpr_read_b32 v17, a252\n"
        "v_cmpx_ne_u32 vcc, s47, v17\n"
        "v_accvgpr_read_b32 v17, a253\n"
        "v_cmpx_ne_u32 vcc, s47, v17\n"
        "v_accvgpr_read_b32 v17, a254\n"
        "v_cmpx_ne_u32 vcc, s47, v17\n"
        "v_accvgpr_read_b32 v17, a255\n"
        "v_cmpx_ne_u32 vcc, s47, v17\n"
        "v_accvgpr_read_b32 v17, a24\n"
        "v_cmpx_ne_u32 vcc, s47, v17\n"
        "v_accvgpr_read_b32 v17, a25\n"
        "v_cmpx_ne_u32 vcc, s47, v17\n"
        "v_accvgpr_read_b32 v17, a26\n"
        "v_cmpx_ne_u32 vcc, s47, v17\n"
        "v_accvgpr_read_b32 v17, a27\n"
        "v_cmpx_ne_u32 vcc, s47, v17\n"
        "v_accvgpr_read_b32 v17, a28\n"
        "v_cmpx_ne_u32 vcc, s47, v17\n"
        "v_accvgpr_read_b32 v17, a29\n"
        "v_cmpx_ne_u32 vcc, s47, v17\n"
        "v_accvgpr_read_b32 v17, a30\n"
        "v_cmpx_ne_u32 vcc, s47, v17\n"
        "v_accvgpr_read_b32 v17, a31\n"
        "v_cmpx_ne_u32 vcc, s47, v17\n"
        "s_nop 4\n"
        "s_cmp_eq_u64 exec, -1\n"
        "s_mov_b64 exec, -1\n"
        "s_cbranch_scc1 .Lq16\n"
        "s_sub_u32 s55, s55, 1\n"
        "s_cbranch_scc0 .Lp16\n"
        "s_mov_b32 s55, 0\n"
        ".Lq16:\n"
        "v_mfma_f32_16x16x32_bf16 v[104:107], v[124:127], a[212:215], 0\n"
        "v_mfma_f32_16x16x32_bf16 v[108:111], v[128:131], a[212:215], 0\n"
        "v_mfma_f32_16x16x32_bf16 v[112:115], v[132:135], a[212:215], 0\n"
        "v_mfma_f32_16x16x32_bf16 v[116:119], v[136:139], a[212:215], 0\n"
        "v_mfma_f32_16x16x32_bf16 v[120:123], v[140:143], a[212:215], 0\n"
        "v_mfma_f32_16x16x32_bf16 v[104:107], v[144:147], a[12:15], v[104:107]\n"
        "v_mfma_f32_16x16x32_bf16 v[108:111], v[148:151], a[12:15], v[108:111]\n"
        "v_mfma_f32_16x16x32_bf16 v[112:115], v[152:155], a[12:15], v[112:115]\n"
        "v_mfma_f32_16x16x32_bf16 v[116:119], v[156:159], a[12:15], v[116:119]\n"
        "v_mfma_f32_16x16x32_bf16 v[120:123], v[160:163], a[12:15], v[120:123]\n"
        "v_mfma_f32_16x16x32_bf16 v[104:107], v[164:167], a[16:19], v[104:107]\n"
        "v_mfma_f32_16x16x32_bf16 v[108:111], v[168:171], a[16:19], v[108:111]\n"
        "v_mfma_f32_16x16x32_bf16 v[112:115], v[172:175], a[16:19], v[112:115]\n"
        "v_mfma_f32_16x16x32_bf16 v[116:119], v[176:179], a[16:19], v[116:119]\n"
        "v_mfma_f32_16x16x32_bf16 v[120:123], v[180:183], a[16:19], v[120:123]\n"
        "v_mfma_f32_16x16x32_bf16 v[104:107], v[184:187], a[20:23], v[104:107]\n"
        "v_mfma_f32_16x16x32_bf16 v[108:111], v[188:191], a[20:23], v[108:111]\n"
        "v_mfma_f32_16x16x32_bf16 v[112:115], v[192:195], a[20:23], v[112:115]\n"
        "v_mfma_f32_16x16x32_bf16 v[116:119], v[196:199], a[20:23], v[116:119]\n"
        "v_mfma_f32_16x16x32_bf16 v[120:123], v[200:203], a[20:23], v[120:123]\n"
        "v_mfma_f32_16x16x32_bf16 v[104:107], v[204:207], a[36:39], v[104:107]\n"
        "v_mfma_f32_16x16x32_bf16 v[108:111], v[208:211], a[36:39], v[108:111]\n"
        "v_mfma_f32_16x16x32_bf16 v[112:115], v[212:215], a[36:39], v[112:115]\n"
        "v_mfma_f32_16x16x32_bf16 v[116:119], v[216:219], a[36:39], v[116:119]\n"
        "v_mfma_f32_16x16x32_bf16 v[120:123], v[220:223], a[36:39], v[120:123]\n"
        "v_mfma_f32_16x16x32_bf16 v[104:107], v[224:227], a[52:55], v[104:107]\n"
        "v_mfma_f32_16x16x32_bf16 v[108:111], v[228:231], a[52:55], v[108:111]\n"
        "v_mfma_f32_16x16x32_bf16 v[112:115], v[232:235], a[52:55], v[112:115]\n"
        "v_mfma_f32_16x16x32_bf16 v[116:119], v[236:239], a[52:55], v[116:119]\n"
        "v_mfma_f32_16x16x32_bf16 v[120:123], v[240:243], a[52:55], v[120:123]\n"
        "v_mfma_f32_16x16x32_bf16 v[104:107], a[0:3], a[80:83], v[104:107]\n"
        "v_mfma_f32_16x16x32_bf16 v[108:111], a[4:7], a[80:83], v[108:111]\n"
        "v_mfma_f32_16x16x32_bf16 v[112:115], a[8:11], a[80:83], v[112:115]\n"
        "v_mfma_f32_16x16x32_bf16 v[116:119], a[40:43], a[80:83], v[116:119]\n"
        "v_mfma_f32_16x16x32_bf16 v[120:123], a[44:47], a[80:83], v[120:123]\n"
        "v_mfma_f32_16x16x32_bf16 v[104:107], a[56:59], a[108:111], v[104:107]\n"
        "v_mfma_f32_16x16x32_bf16 v[108:111], a[60:63], a[108:111], v[108:111]\n"
        "v_mfma_f32_16x16x32_bf16 v[112:115], a[64:67], a[108:111], v[112:115]\n"
        "v_mfma_f32_16x16x32_bf16 v[116:119], a[68:71], a[108:111], v[116:119]\n"
        "v_mfma_f32_16x16x32_bf16 v[120:123], a[72:75], a[108:111], v[120:123]\n"
        "v_mfma_f32_16x16x32_bf16 v[104:107], a[84:87], a[224:227], v[104:107]\n"
        "v_mfma_f32_16x16x32_bf16 v[108:111], a[88:91], a[224:227], v[108:111]\n"
        "v_mfma_f32_16x16x32_bf16 v[112:115], a[92:95], a[224:227], v[112:115]\n"
        "v_mfma_f32_16x16x32_bf16 v[116:119], a[96:99], a[224:227], v[116:119]\n"
        "v_mfma_f32_16x16x32_bf16 v[120:123], a[100:103], a[224:227], v[120:123]\n"
        "v_mfma_f32_16x16x32_bf16 v[104:107], a[112:115], a[252:255], v[104:107]\n"
        "v_mfma_f32_16x16x32_bf16 v[108:111], a[116:119], a[252:255], v[108:111]\n"
        "v_mfma_f32_16x16x32_bf16 v[112:115], a[120:123], a[252:255], v[112:115]\n"
        "v_mfma_f32_16x16x32_bf16 v[116:119], a[124:127], a[252:255], v[116:119]\n"
        "v_mfma_f32_16x16x32_bf16 v[120:123], a[216:219], a[252:255], v[120:123]\n"
        "v_mfma_f32_16x16x32_bf16 v[104:107], a[228:231], a[24:27], v[104:107]\n"
        "v_mfma_f32_16x16x32_bf16 v[108:111], a[232:235], a[24:27], v[108:111]\n"
        "v_mfma_f32_16x16x32_bf16 v[112:115], a[236:239], a[24:27], v[112:115]\n"
        "v_mfma_f32_16x16x32_bf16 v[116:119], a[240:243], a[24:27], v[116:119]\n"
        "v_mfma_f32_16x16x32_bf16 v[120:123], a[244:247], a[24:27], v[120:123]\n"
        "v_mfma_f32_16x16x32_bf16 v[104:107], a[128:131], a[28:31], v[104:107]\n"
        "v_mfma_f32_16x16x32_bf16 v[108:111], a[132:135], a[28:31], v[108:111]\n"
        "v_mfma_f32_16x16x32_bf16 v[112:115], a[136:139], a[28:31], v[112:115]\n"
        "v_mfma_f32_16x16x32_bf16 v[116:119], a[140:143], a[28:31], v[116:119]\n"
        "v_mfma_f32_16x16x32_bf16 v[120:123], a[144:147], a[28:31], v[120:123]\n"
        "s_waitcnt lgkmcnt(0)\n"
        "s_nop 7\n"
        "s_nop 7\n"
        "s_branch .Lb28\n"
        ".Lend:\n"
        :
        : "{s[0:1]}"(__builtin_amdgcn_kernarg_segment_ptr()), "{s2}"(blockIdx.x), "{v0}"(threadIdx.x),
          "{s3}"((unsigned)(uintptr_t)(__attribute__((address_space(3))) char*)lds)
        : "v1", "v2", "v3", "v4", "v5", "v6", "v7", "v8", "v9", "v10", "v11", "v12", "v13", "v14", "v15", "v16", "v17", "v18", "v19", "v20", "v21", "v22", "v23", "v24", "v25", "v26", "v27", "v28", "v29", "v30", "v31", "v32", "v33", "v34", "v35", "v36", "v37", "v38", "v39", "v40", "v41", "v42", "v43", "v44", "v45", "v46", "v47", "v48", "v49", "v50", "v51", "v52", "v53", "v54", "v55", "v56", "v57", "v58", "v59", "v60", "v61", "v62", "v63", "v64", "v65", "v66", "v67", "v68", "v69", "v70", "v71", "v72", "v73", "v74", "v75", "v76", "v77", "v78", "v79", "v80", "v81", "v82", "v83", "v84", "v85", "v86", "v87", "v88", "v89", "v90", "v91", "v92", "v93", "v94", "v95", "v96", "v97", "v98", "v99", "v100", "v101", "v102", "v103", "v104", "v105", "v106", "v107", "v108", "v109", "v110", "v111", "v112", "v113", "v114", "v115", "v116", "v117", "v118", "v119", "v120", "v121", "v122", "v123", "v124", "v125", "v126", "v127", "v128", "v129", "v130", "v131", "v132", "v133", "v134", "v135", "v136", "v137", "v138", "v139", "v140", "v141", "v142", "v143", "v144", "v145", "v146", "v147", "v148", "v149", "v150", "v151", "v152", "v153", "v154", "v155", "v156", "v157", "v158", "v159", "v160", "v161", "v162", "v163", "v164", "v165", "v166", "v167", "v168", "v169", "v170", "v171", "v172", "v173", "v174", "v175", "v176", "v177", "v178", "v179", "v180", "v181", "v182", "v183", "v184", "v185", "v186", "v187", "v188", "v189", "v190", "v191", "v192", "v193", "v194", "v195", "v196", "v197", "v198", "v199", "v200", "v201", "v202", "v203", "v204", "v205", "v206", "v207", "v208", "v209", "v210", "v211", "v212", "v213", "v214", "v215", "v216", "v217", "v218", "v219", "v220", "v221", "v222", "v223", "v224", "v225", "v226", "v227", "v228", "v229", "v230", "v231", "v232", "v233", "v234", "v235", "v236", "v237", "v238", "v239", "v240", "v241", "v242", "v243", "v244", "v245", "v246", "v247", "v248", "v249", "v250", "v251", "v252", "v253", "v254", "v255", "a0", "a1", "a2", "a3", "a4", "a5", "a6", "a7", "a8", "a9", "a10", "a11", "a12", "a13", "a14", "a15", "a16", "a17", "a18", "a19", "a20", "a21", "a22", "a23", "a24", "a25", "a26", "a27", "a28", "a29", "a30", "a31", "a32", "a33", "a34", "a35", "a36", "a37", "a38", "a39", "a40", "a41", "a42", "a43", "a44", "a45", "a46", "a47", "a48", "a49", "a50", "a51", "a52", "a53", "a54", "a55", "a56", "a57", "a58", "a59", "a60", "a61", "a62", "a63", "a64", "a65", "a66", "a67", "a68", "a69", "a70", "a71", "a72", "a73", "a74", "a75", "a76", "a77", "a78", "a79", "a80", "a81", "a82", "a83", "a84", "a85", "a86", "a87", "a88", "a89", "a90", "a91", "a92", "a93", "a94", "a95", "a96", "a97", "a98", "a99", "a100", "a101", "a102", "a103", "a104", "a105", "a106", "a107", "a108", "a109", "a110", "a111", "a112", "a113", "a114", "a115", "a116", "a117", "a118", "a119", "a120", "a121", "a122", "a123", "a124", "a125", "a126", "a127", "a128", "a129", "a130", "a131", "a132", "a133", "a134", "a135", "a136", "a137", "a138", "a139", "a140", "a141", "a142", "a143", "a144", "a145", "a146", "a147", "a148", "a149", "a150", "a151", "a152", "a153", "a154", "a155", "a156", "a157", "a158", "a159", "a160", "a161", "a162", "a163", "a164", "a165", "a166", "a167", "a168", "a169", "a170", "a171", "a172", "a173", "a174", "a175", "a176", "a177", "a178", "a179", "a180", "a181", "a182", "a183", "a184", "a185", "a186", "a187", "a188", "a189", "a190", "a191", "a192", "a193", "a194", "a195", "a196", "a197", "a198", "a199", "a200", "a201", "a202", "a203", "a204", "a205", "a206", "a207", "a208", "a209", "a210", "a211", "a212", "a213", "a214", "a215", "a216", "a217", "a218", "a219", "a220", "a221", "a222", "a223", "a224", "a225", "a226", "a227", "a228", "a229", "a230", "a231", "a232", "a233", "a234", "a235", "a236", "a237", "a238", "a239", "a240", "a241", "a242", "a243", "a244", "a245", "a246", "a247", "a248", "a249", "a250", "a251", "a252", "a253", "a254", "a255", "s4", "s5", "s6", "s7", "s8", "s9", "s10", "s11", "s12", "s13", "s14", "s15", "s16", "s17", "s18", "s19", "s20", "s21", "s22", "s23", "s24", "s25", "s26", "s27", "s28", "s29", "s30", "s31", "s36", "s37", "s38", "s39", "s40", "s41", "s42", "s43", "s44", "s45", "s46", "s47", "s48", "s49", "s50", "s51", "s52", "s53", "s54", "s55", "s56", "s57", "s58", "s59", "s60", "s61", "s62", "s63", "s64", "s65", "s66", "s67", "s68", "s69", "s70", "s71", "s72", "s73", "s74", "s75", "s76", "s77", "s78", "s79", "s80", "s81", "s82", "s83", "s84", "s85", "s86", "s87", "s88", "s89", "s90", "s91", "s92", "s93", "s94", "s95", "s96", "s97", "s98", "s99", "vcc", "scc", "memory");
}

extern "C" void kernel_launch(void* const* d_in, const int* in_sizes, int n_in, void* d_out, int out_size, void* d_ws,
                              size_t ws_size, hipStream_t stream) {
    k_block<<<256, 256, 0, stream>>>(d_in[0], d_in[1], d_in[2], d_in[4], d_in[5], d_in[6], d_in[7], d_out, d_ws);
}
```
